```python
import torch

def _softmax(x, axis=-1):
    return torch.softmax(x, dim=axis)
def _gelu(x):
    return torch.nn.functional.gelu(x, approximate="tanh")
def _silu(x):
    return torch.nn.functional.silu(x)
def _sigmoid(x):
    return torch.sigmoid(x)
def _layernorm(x, axis=-1, eps=1e-5):
    m = x.mean(dim=axis, keepdim=True)
    v = x.var(dim=axis, keepdim=True, unbiased=False)
    return (x - m) / torch.sqrt(v + eps)
def _scatter_add(tgt, idx, src, axis=0):
    return tgt.clone().index_add_(axis, idx, src)
def _segment_sum(src, seg, n):
    out = torch.zeros((n,) + src.shape[1:], dtype=src.dtype)
    return out.index_add_(0, seg, src)
def _pad(x, pw):
    flat = tuple(v for pair in reversed(pw) for v in pair)
    return torch.nn.functional.pad(x, flat)

kernel_name = 'standalone__kv_cache_quant_int4_int2_kv_with_per_channel_or_per_token_scales__prompt'

def setup_inputs():

    _w = (lambda a: (lambda t: t.to(torch.bfloat16) if t.is_floating_point() else t)(torch.as_tensor(a)))
    B = 1
    Sq = 2048
    Skv = 2048
    Hq = 32
    Hkv = 8
    Dh = 128
    bits = 8
    G = 128
    if G <= 0 or G >= Skv:
        G = max(1, Skv // 2)
    Skv = (Skv // G) * G
    ngk = Skv // G
    qmax = (1 << bits) - 1
    return {
        "q": _w((lambda *s: torch.randn(*s, dtype=torch.bfloat16) * 0.2)(B, Hq, Sq, Dh)),
        "qk": _w(torch.randint(0, qmax + 1, (B, Hkv, Skv, Dh)).to(torch.int32)),
        "qv": _w(torch.randint(0, qmax + 1, (B, Hkv, Skv, Dh)).to(torch.int32)),
        "k_scales": _w(torch.abs((lambda *s: torch.randn(*s, dtype=torch.bfloat16) * 0.2)(B, Hkv, ngk, Dh)) * 0.05 + 0.02),
        "k_zeros": _w((lambda *s: torch.randn(*s, dtype=torch.bfloat16) * 0.2)(B, Hkv, ngk, Dh) + float(qmax) / 2.0),
        "v_scales": _w(torch.abs((lambda *s: torch.randn(*s, dtype=torch.bfloat16) * 0.05)(B, Hkv, Skv, 1)) * 0.05 + 0.02),
        "v_zeros": _w((lambda *s: torch.randn(*s, dtype=torch.bfloat16) * 0.05)(B, Hkv, Skv, 1) + float(qmax) / 2.0),
    }

def reference(q, qk, qv, k_scales, k_zeros, v_scales, v_zeros):
    B, Hq, Sq, Dh = q.shape
    Hkv, Skv = qk.shape[1], qk.shape[2]
    ngk = k_scales.shape[2]
    G = Skv // ngk
    ks = (lambda x, n, axis: torch.repeat_interleave(x, n, dim=axis))(k_scales, G, axis=2)
    kz = (lambda x, n, axis: torch.repeat_interleave(x, n, dim=axis))(k_zeros, G, axis=2)
    k = ((lambda x, dt: x.to(dt))(qk, q.dtype) - kz) * ks
    v = ((lambda x, dt: x.to(dt))(qv, q.dtype) - v_zeros) * v_scales
    rep = Hq // Hkv
    if rep > 1:
        k = (lambda x, n, axis: torch.repeat_interleave(x, n, dim=axis))(k, rep, axis=1)
        v = (lambda x, n, axis: torch.repeat_interleave(x, n, dim=axis))(v, rep, axis=1)
    scale = float(Dh) ** -0.5
    logits = torch.einsum("bhqd,bhkd->bhqk", q, k) * scale
    p = _softmax(logits, axis=-1)
    o = torch.einsum("bhqk,bhkd->bhqd", p, v)
    return o
```

```cpp
#include <hip/hip_runtime.h>
#include <hip/hip_bf16.h>
typedef unsigned u32x4 __attribute__((ext_vector_type(4)));
typedef int i32x4 __attribute__((ext_vector_type(4)));
typedef float f32x2 __attribute__((ext_vector_type(2)));
typedef float f32x4 __attribute__((ext_vector_type(4)));
typedef __bf16 bf16x2 __attribute__((ext_vector_type(2)));
typedef __bf16 bf16x8 __attribute__((ext_vector_type(8)));
typedef unsigned short u16;
__device__ __forceinline__ unsigned cvtpk(float lo, float hi) { f32x2 v = {lo, hi}; bf16x2 b = __builtin_convertvector(v, bf16x2); return __builtin_bit_cast(unsigned, b); }
__device__ __forceinline__ float bflo(unsigned x) { return __uint_as_float(x << 16); }
__device__ __forceinline__ float bfhi(unsigned x) { return __uint_as_float(x & 0xffff0000u); }
__device__ __forceinline__ float bfel(const u32x4& v, int e) { return (e & 1) ? bfhi(v[e >> 1]) : bflo(v[e >> 1]); }
__device__ __forceinline__ float rbf(float x) { return bflo(cvtpk(x, 0.f)); }

__device__ __forceinline__ void exact_rows(const u16* __restrict__ q, const int* __restrict__ qk, const int* __restrict__ qv, const u16* __restrict__ ks, const u16* __restrict__ kz,
                                           const u16* __restrict__ vs, const u16* __restrict__ vz, u16* __restrict__ out, unsigned g, unsigned row0) {
  const unsigned l = __builtin_amdgcn_mbcnt_hi(~0u, __builtin_amdgcn_mbcnt_lo(~0u, 0u)), c = l & 15, r4 = l >> 4;
  const float CS = 0.12751743082459868f;
#pragma unroll 1
  for (unsigned qb = 0; qb < 4; ++qb) {
    const size_t row = row0 + 16 * qb + c;
    bf16x8 qf[4];
#pragma unroll
    for (int k = 0; k < 4; ++k) qf[k] = *(const bf16x8*)(q + row * 128 + 32 * k + 8 * r4);
    f32x4 o[8];
#pragma unroll
    for (int db = 0; db < 8; ++db) o[db] = f32x4{0.f, 0.f, 0.f, 0.f};
    float m = -INFINITY, lsum = 0.f;
#pragma unroll 1
    for (unsigned st = 0; st < 64; ++st) {
      const unsigned key0 = g * 2048 + 32 * st;
      f32x4 s[2];
#pragma unroll
      for (int hi = 0; hi < 2; ++hi) {
        s[hi] = f32x4{0.f, 0.f, 0.f, 0.f};
        const unsigned key = key0 + 8 * (c >> 2) + 4 * hi + (c & 3);
#pragma unroll
        for (int k = 0; k < 4; ++k) {
          const unsigned d0 = 32 * k + 8 * r4, gi = (g * 16 + (st >> 2)) * 128 + d0;
          const i32x4 a = *(const i32x4*)(qk + (size_t)key * 128 + d0), b = *(const i32x4*)(qk + (size_t)key * 128 + d0 + 4);
          const u32x4 sc = *(const u32x4*)(ks + gi), zz = *(const u32x4*)(kz + gi);
          u32x4 w;
#pragma unroll
          for (int e = 0; e < 4; ++e) {
            const int x0 = e < 2 ? a[2 * e] : b[2 * e - 4], x1 = e < 2 ? a[2 * e + 1] : b[2 * e - 3];
            w[e] = cvtpk(rbf(rbf((float)x0) - bflo(zz[e])) * bflo(sc[e]), rbf(rbf((float)x1) - bfhi(zz[e])) * bfhi(sc[e]));
          }
          s[hi] = __builtin_amdgcn_mfma_f32_16x16x32_bf16(__builtin_bit_cast(bf16x8, w), qf[k], s[hi], 0, 0, 0);
        }
      }
      float mx = m;
#pragma unroll
      for (int e = 0; e < 8; ++e) { const float t = s[e >> 2][e & 3] * CS; s[e >> 2][e & 3] = t; mx = (t > mx || t != t) ? t : mx; }
#pragma unroll
      for (int sh = 16; sh <= 32; sh <<= 1) { const float t = __shfl_xor(mx, sh); mx = (t > mx || t != t) ? t : mx; }
      const float ms = (mx == -INFINITY) ? 0.f : mx;
      const float alpha = __builtin_amdgcn_exp2f(m - ms);
      m = mx;
      u32x4 pw;
#pragma unroll
      for (int e = 0; e < 4; ++e) pw[e] = cvtpk(__builtin_amdgcn_exp2f(s[e >> 1][2 * (e & 1)] - ms), __builtin_amdgcn_exp2f(s[e >> 1][2 * (e & 1) + 1] - ms));
      float ps = 0.f;
#pragma unroll
      for (int e = 0; e < 8; ++e) ps += bfel(pw, e);
      lsum = lsum * alpha + ps;
      const u32x4 sc = *(const u32x4*)(vs + key0 + 8 * r4), zz = *(const u32x4*)(vz + key0 + 8 * r4);
      float y[8][8];
#pragma unroll
      for (int j = 0; j < 8; ++j) {
        const int* src = qv + (size_t)(key0 + 8 * r4 + j) * 128 + 8 * c;
        const i32x4 a = *(const i32x4*)src, b = *(const i32x4*)(src + 4);
#pragma unroll
        for (int e = 0; e < 8; ++e) y[j][e] = rbf(rbf((float)(e < 4 ? a[e] : b[e - 4])) - bfel(zz, j)) * bfel(sc, j);
      }
#pragma unroll
      for (int db = 0; db < 8; ++db) {
        u32x4 w;
#pragma unroll
        for (int e = 0; e < 4; ++e) w[e] = cvtpk(y[2 * e][db], y[2 * e + 1][db]);
        o[db] *= alpha;
        o[db] = __builtin_amdgcn_mfma_f32_16x16x32_bf16(__builtin_bit_cast(bf16x8, w), __builtin_bit_cast(bf16x8, pw), o[db], 0, 0, 0);
      }
    }
    lsum += __shfl_xor(lsum, 16); lsum += __shfl_xor(lsum, 32);
    const float il = 1.f / lsum;
#pragma unroll
    for (int r = 0; r < 4; ++r) {
      u32x4 w;
#pragma unroll
      for (int e = 0; e < 4; ++e) w[e] = cvtpk(o[2 * e][r] * il, o[2 * e + 1][r] * il);
      *(u32x4*)(out + row * 128 + 8 * (4 * r4 + r)) = w;
    }
  }
}

struct KArgs { const u16* q; const int* qk; const int* qv; const u16* ks; const u16* kz; const u16* vs; const u16* vz; u16* out; void* ws; };
typedef const __attribute__((address_space(4))) KArgs* KArgsPtr;

__global__ __launch_bounds__(256, 1) __attribute__((aligned(4096))) void kv_quant_attn_kernel(const u16* q, const int* qk, const int* qv, const u16* ks, const u16* kz, const u16* vs, const u16* vz, u16* out, void* ws) {
  const unsigned wid = __builtin_amdgcn_readfirstlane(threadIdx.x >> 6);
  const unsigned bid = blockIdx.x;
  KArgsPtr ka = (KArgsPtr)__builtin_amdgcn_kernarg_segment_ptr();
  unsigned status;
  asm volatile(
    "s_load_dwordx16 s[40:55], %1, 0x0\n"
    "s_load_dwordx2 s[56:57], %1, 0x40\n"
    "v_mbcnt_lo_u32_b32 v104, -1, 0\n"
    "v_mbcnt_hi_u32_b32 v104, -1, v104\n"
    "s_lshr_b32 s59, %2, 3\n"
    "s_getpc_b64 s[78:79]\n"
    "L%=_pc:\n"
    "s_lshr_b32 s61, %2, 3\n"
    "s_lshl_b32 s61, s61, 2\n"
    "s_add_u32 s61, s61, %3\n"
    "s_lshl_b32 s61, s61, 9\n"
    "v_and_b32 v13, 7, v104\n"
    "v_lshl_add_u32 v13, v13, 6, s61\n"
    "v_min_u32 v13, L%=_fin-L%=_pc, v13\n"
    "global_load_dword a159, v13, s[78:79]\n"
    "v_and_b32 v105, 15, v104\n"
    "v_lshrrev_b32 v106, 4, v104\n"
    "v_lshlrev_b32 v6, 4, v104\n"
    "s_and_b32 s58, %2, 7\n"
    "s_lshl_b32 s69, %3, 12\n"
    "s_and_b32 s61, %3, 1\n"
    "s_lshl_b32 s75, s59, 1\n"
    "s_add_u32 s68, s61, s75\n"
    "s_lshr_b32 s61, %3, 1\n"
    "s_lshl_b32 s61, s61, 6\n"
    "s_lshl_b32 s60, s68, 3\n"
    "s_add_u32 s60, s60, s61\n"
    "s_lshl_b32 s78, s60, 9\n"
    "s_lshr_b32 s79, s60, 7\n"
    "s_lshl_b32 s79, s79, 8\n"
    "s_lshl_b32 s60, s60, 1\n"
    "v_lshlrev_b32 v107, 5, v105\n"
    "v_lshl_add_u32 v108, v106, 9, v107\n"
    "v_lshlrev_b32 v110, 4, v105\n"
    "v_and_b32 v4, 1, v106\n"
    "v_lshrrev_b32 v5, 1, v106\n"
    "v_lshl_add_u32 v109, v4, 4, v107\n"
    "v_lshl_add_u32 v109, v5, 10, v109\n"
    "v_lshl_add_u32 v8, v4, 13, v110\n"
    "v_lshl_add_u32 v8, v5, 12, v8\n"
    "s_cmp_ge_u32 %3, 2\n"
    "s_cselect_b64 vcc, -1, 0\n"
    "s_cselect_b32 s79, s79, s60\n"
    "s_cselect_b32 s60, 16, 0x200\n"
    "s_mov_b32 s34, 0\n"
    "s_mov_b32 s35, -1\n"
    "s_mov_b32 s36, 0x3e0293ee\n"
    "s_mov_b32 s37, 0x3e0293ee\n"
    "v_cndmask_b32 v12, v109, v108, vcc\n"
    "v_cndmask_b32 v15, 0, v110, vcc\n"
    "v_add_u32 v12, s78, v12\n"
    "v_add_u32 v15, s79, v15\n"
    "s_lshl_b32 s78, %3, 13\n"
    "s_add_u32 s78, s78, 0x1000\n"
    "v_add_u32 v108, s78, v108\n"
    "s_lshl_b32 s61, s58, 20\n"
    "s_lshl_b32 s75, s58, 12\n"
    "s_cmp_ge_u32 %3, 2\n"
    "s_waitcnt lgkmcnt(0)\n"
    "s_cselect_b64 s[80:81], s[42:43], s[44:45]\n"
    "s_cselect_b64 s[82:83], s[48:49], s[52:53]\n"
    "s_cselect_b64 s[84:85], s[46:47], s[50:51]\n"
    "s_add_u32 s82, s82, s75\n"
    "s_addc_u32 s83, s83, 0\n"
    "s_add_u32 s84, s84, s75\n"
    "s_addc_u32 s85, s85, 0\n"
    "s_add_u32 s80, s80, s61\n"
    "s_addc_u32 s81, s81, 0\n"
    "global_load_dwordx4 v[36:39], v15, s[82:83]\n"
    "v_add_u32 v2, s60, v12\n"
    "global_load_dwordx4 v[44:47], v15, s[84:85]\n"
    "global_load_dwordx4 v[112:115], v12, s[80:81] offset:0 nt\n"
    "global_load_dwordx4 v[116:119], v2, s[80:81] offset:0 nt\n"
    "global_load_dwordx4 v[120:123], v12, s[80:81] offset:2048 nt\n"
    "global_load_dwordx4 v[124:127], v2, s[80:81] offset:2048 nt\n"
    "v_add_u32 v12, 0x40000, v12\n"
    "v_add_u32 v15, 0x400, v15\n"
    "v_and_b32 v12, 0xfffff, v12\n"
    "v_and_b32 v15, 0xfff, v15\n"
    "s_add_u32 s42, s42, s61\n"
    "s_addc_u32 s43, s43, 0\n"
    "s_add_u32 s46, s46, s75\n"
    "s_addc_u32 s47, s47, 0\n"
    "s_add_u32 s48, s48, s75\n"
    "s_addc_u32 s49, s49, 0\n"
    "s_add_u32 s50, s50, s75\n"
    "s_addc_u32 s51, s51, 0\n"
    "s_add_u32 s52, s52, s75\n"
    "s_addc_u32 s53, s53, 0\n"
    "s_add_u32 s56, s56, 0x1fffff\n"
    "s_addc_u32 s57, s57, 0\n"
    "s_and_b32 s56, s56, 0xffe00000\n"
    "s_mul_i32 s61, s58, 0x200000\n"
    "s_lshl_b32 s75, s58, 21\n"
    "s_add_u32 s62, s56, s61\n"
    "s_addc_u32 s63, s57, 0\n"
    "s_add_u32 s76, s62, 0x100000\n"
    "s_mov_b32 s77, s63\n"
    "s_mov_b64 s[38:39], s[76:77]\n"
    "s_mov_b32 s65, s63\n"
    "s_load_dword s72, s[76:77], 0x8000\n"
    "s_lshr_b32 s61, s68, 3\n"
    "s_lshl_b32 s61, s61, 15\n"
    "s_add_u32 s64, s62, s61\n"
    "s_lshl_b32 s79, s59, 16\n"
    "s_add_u32 s75, s75, s79\n"
    "s_lshl_b32 s79, %3, 14\n"
    "s_add_u32 s79, s79, s75\n"
    "s_add_u32 s40, s40, s79\n"
    "s_addc_u32 s41, s41, 0\n"
    "s_add_u32 s54, s54, s79\n"
    "s_addc_u32 s55, s55, 0\n"
    "s_mov_b32 s70, 2\n"
    "s_mov_b32 s67, 8192\n"
    "s_mov_b32 s71, 0\n"
    "v_lshrrev_b32 v4, 2, v105\n"
    "v_lshlrev_b32 v4, 10, v4\n"
    "v_lshl_add_u32 v4, v106, 6, v4\n"
    "v_and_b32 v5, 3, v105\n"
    "s_and_b32 s61, s68, 2\n"
    "v_xor_b32 v7, s61, v5\n"
    "v_lshl_add_u32 v7, v7, 4, v4\n"
    "s_and_b32 s61, s68, 3\n"
    "s_lshl_b32 s61, s61, 8\n"
    "s_and_b32 s75, s68, 4\n"
    "s_lshl_b32 s75, s75, 11\n"
    "s_add_u32 s61, s61, s75\n"
    "s_add_u32 s61, s61, 0x1000\n"
    "v_add_u32 v7, s61, v7\n"
    "s_and_b32 s61, s68, 7\n"
    "s_lshl_b32 s61, s61, 8\n"
    "s_add_u32 s61, s61, 0x4000\n"
    "v_add_u32 v8, s61, v8\n"
    "v_cndmask_b32 v14, v8, v7, vcc\n"
    "s_and_b32 s61, %3, 1\n"
    "s_lshl_b32 s75, s61, 1\n"
    "v_xor_b32 v5, s75, v5\n"
    "v_lshl_add_u32 v4, v5, 4, v4\n"
    "s_lshl_b32 s61, s61, 9\n"
    "s_lshr_b32 s75, %3, 1\n"
    "s_lshl_b32 s75, s75, 13\n"
    "s_add_u32 s61, s61, s75\n"
    "s_add_u32 s61, s61, 0x18000\n"
    "v_add_u32 v107, s61, v4\n"
    "v_xor_b32 v4, v105, v106\n"
    "v_lshlrev_b32 v4, 4, v4\n"
    "v_lshl_add_u32 v21, v106, 8, v4\n"
    "s_lshl_b32 s66, %3, 14\n"
    "s_add_u32 s66, s66, 0x10000\n"
    "s_cmp_ge_u32 %3, 2\n"
    "s_cselect_b32 s61, 0x8000, 0\n"
    "s_add_u32 s66, s66, s61\n"
    "s_add_u32 s74, s59, %3\n"
    "v_and_b32 v5, 3, v105\n"
    "v_lshrrev_b32 v4, 2, v105\n"
    "v_lshl_add_u32 v5, v5, 2, v4\n"
    "v_xor_b32 v4, v5, v106\n"
    "v_lshlrev_b32 v4, 4, v4\n"
    "v_lshl_add_u32 v4, v5, 8, v4\n"
    "v_add_u32 v16, s66, v4\n"
    "global_load_dwordx4 v[96:99], v110, s[48:49]\n"
    "global_load_dwordx4 v[100:103], v110, s[46:47]\n"
    "global_load_dwordx4 v[48:51], v108, s[42:43] offset:-4096\n"
    "global_load_dwordx4 v[52:55], v108, s[42:43] offset:-4080\n"
    "global_load_dwordx4 v[56:59], v108, s[42:43] offset:-2048\n"
    "global_load_dwordx4 v[60:63], v108, s[42:43] offset:-2032\n"
    "global_load_dwordx4 v[64:67], v108, s[42:43] offset:0\n"
    "global_load_dwordx4 v[68:71], v108, s[42:43] offset:16\n"
    "global_load_dwordx4 v[72:75], v108, s[42:43] offset:2048\n"
    "global_load_dwordx4 v[76:79], v108, s[42:43] offset:2064\n"
    "s_cmp_ge_u32 %3, 2\n"
    "s_cbranch_scc1 L%=_u0k\n"
    "s_waitcnt vmcnt(10)\n"
    "v_cndmask_b32 v33, v36, v37, s[34:35]\n"
    "v_cndmask_b32 v35, v38, v39, s[34:35]\n"
    "v_cndmask_b32 v41, v44, v45, s[34:35]\n"
    "v_cndmask_b32 v43, v46, v47, s[34:35]\n"
    "v_lshlrev_b32 v32, 16, v33\n"
    "v_lshlrev_b32 v34, 16, v35\n"
    "v_lshlrev_b32 v40, 16, v41\n"
    "v_lshlrev_b32 v42, 16, v43\n"
    "v_and_b32 v33, 0xffff0000, v33\n"
    "v_and_b32 v35, 0xffff0000, v35\n"
    "v_and_b32 v41, 0xffff0000, v41\n"
    "v_and_b32 v43, 0xffff0000, v43\n"
    "v_mul_f32 v40, 0x3d000000, v40\n"
    "v_mul_f32 v41, 0x3d000000, v41\n"
    "v_mul_f32 v42, 0x3d000000, v42\n"
    "v_mul_f32 v43, 0x3d000000, v43\n"
    "v_pk_mul_f32 v[32:33], v[32:33], v[40:41]\n"
    "v_pk_mul_f32 v[34:35], v[34:35], v[42:43]\n"
    "v_cvt_f32_i32 v112, v112\n"
    "v_cvt_f32_i32 v113, v113\n"
    "v_cvt_f32_i32 v114, v114\n"
    "v_cvt_f32_i32 v115, v115\n"
    "v_cvt_f32_i32 v116, v116\n"
    "v_cvt_f32_i32 v117, v117\n"
    "v_cvt_f32_i32 v118, v118\n"
    "v_cvt_f32_i32 v119, v119\n"
    "v_pk_fma_f32 v[112:113], v[112:113], v[40:41], v[32:33] op_sel:[0,0,0] op_sel_hi:[1,0,0] neg_lo:[0,0,1] neg_hi:[0,0,1]\n"
    "v_pk_fma_f32 v[114:115], v[114:115], v[40:41], v[32:33] op_sel:[0,0,0] op_sel_hi:[1,0,0] neg_lo:[0,0,1] neg_hi:[0,0,1]\n"
    "v_pk_fma_f32 v[116:117], v[116:117], v[40:41], v[32:33] op_sel:[0,1,1] op_sel_hi:[1,1,1] neg_lo:[0,0,1] neg_hi:[0,0,1]\n"
    "v_pk_fma_f32 v[118:119], v[118:119], v[40:41], v[32:33] op_sel:[0,1,1] op_sel_hi:[1,1,1] neg_lo:[0,0,1] neg_hi:[0,0,1]\n"
    "v_cvt_f32_i32 v120, v120\n"
    "v_cvt_f32_i32 v121, v121\n"
    "v_cvt_f32_i32 v122, v122\n"
    "v_cvt_f32_i32 v123, v123\n"
    "v_cvt_f32_i32 v124, v124\n"
    "v_cvt_f32_i32 v125, v125\n"
    "v_cvt_f32_i32 v126, v126\n"
    "v_cvt_f32_i32 v127, v127\n"
    "v_pk_fma_f32 v[120:121], v[120:121], v[42:43], v[34:35] op_sel:[0,0,0] op_sel_hi:[1,0,0] neg_lo:[0,0,1] neg_hi:[0,0,1]\n"
    "v_pk_fma_f32 v[122:123], v[122:123], v[42:43], v[34:35] op_sel:[0,0,0] op_sel_hi:[1,0,0] neg_lo:[0,0,1] neg_hi:[0,0,1]\n"
    "v_pk_fma_f32 v[124:125], v[124:125], v[42:43], v[34:35] op_sel:[0,1,1] op_sel_hi:[1,1,1] neg_lo:[0,0,1] neg_hi:[0,0,1]\n"
    "v_pk_fma_f32 v[126:127], v[126:127], v[42:43], v[34:35] op_sel:[0,1,1] op_sel_hi:[1,1,1] neg_lo:[0,0,1] neg_hi:[0,0,1]\n"
    "v_cvt_pk_bf16_f32 v36, v112, v116\n"
    "v_cvt_pk_bf16_f32 v37, v114, v118\n"
    "v_cvt_pk_bf16_f32 v38, v120, v124\n"
    "v_cvt_pk_bf16_f32 v39, v122, v126\n"
    "v_cvt_pk_bf16_f32 v44, v113, v117\n"
    "v_cvt_pk_bf16_f32 v45, v115, v119\n"
    "v_cvt_pk_bf16_f32 v46, v121, v125\n"
    "v_cvt_pk_bf16_f32 v47, v123, v127\n"
    "v_permlane32_swap_b32 v36, v37\n"
    "v_permlane32_swap_b32 v38, v39\n"
    "v_permlane32_swap_b32 v44, v45\n"
    "v_permlane32_swap_b32 v46, v47\n"
    "global_store_dwordx4 v14, v[36:39], s[64:65] offset:0 sc1\n"
    "global_store_dwordx4 v14, v[44:47], s[64:65] offset:2048 sc1\n"
    "s_branch L%=_u0e\n"
    "L%=_u0k:\n"
    "s_waitcnt vmcnt(10)\n"
    "v_lshlrev_b32 v32, 16, v36\n"
    "v_and_b32 v33, 0xffff0000, v36\n"
    "v_lshlrev_b32 v34, 16, v37\n"
    "v_and_b32 v35, 0xffff0000, v37\n"
    "v_lshlrev_b32 v36, 16, v38\n"
    "v_and_b32 v37, 0xffff0000, v38\n"
    "v_lshlrev_b32 v38, 16, v39\n"
    "v_and_b32 v39, 0xffff0000, v39\n"
    "v_lshlrev_b32 v40, 16, v44\n"
    "v_and_b32 v41, 0xffff0000, v44\n"
    "v_lshlrev_b32 v42, 16, v45\n"
    "v_and_b32 v43, 0xffff0000, v45\n"
    "v_lshlrev_b32 v44, 16, v46\n"
    "v_and_b32 v45, 0xffff0000, v46\n"
    "v_lshlrev_b32 v46, 16, v47\n"
    "v_and_b32 v47, 0xffff0000, v47\n"
    "v_pk_mul_f32 v[40:41], v[40:41], s[36:37]\n"
    "v_pk_mul_f32 v[42:43], v[42:43], s[36:37]\n"
    "v_pk_mul_f32 v[44:45], v[44:45], s[36:37]\n"
    "v_pk_mul_f32 v[46:47], v[46:47], s[36:37]\n"
    "v_pk_mul_f32 v[32:33], v[32:33], v[40:41]\n"
    "v_pk_mul_f32 v[34:35], v[34:35], v[42:43]\n"
    "v_pk_mul_f32 v[36:37], v[36:37], v[44:45]\n"
    "v_pk_mul_f32 v[38:39], v[38:39], v[46:47]\n"
    "v_cvt_f32_i32 v112, v112\n"
    "v_cvt_f32_i32 v113, v113\n"
    "v_cvt_f32_i32 v114, v114\n"
    "v_cvt_f32_i32 v115, v115\n"
    "v_cvt_f32_i32 v116, v116\n"
    "v_cvt_f32_i32 v117, v117\n"
    "v_cvt_f32_i32 v118, v118\n"
    "v_cvt_f32_i32 v119, v119\n"
    "v_pk_fma_f32 v[112:113], v[112:113], v[40:41], v[32:33] op_sel:[0,0,0] op_sel_hi:[1,1,1] neg_lo:[0,0,1] neg_hi:[0,0,1]\n"
    "v_pk_fma_f32 v[114:115], v[114:115], v[42:43], v[34:35] op_sel:[0,0,0] op_sel_hi:[1,1,1] neg_lo:[0,0,1] neg_hi:[0,0,1]\n"
    "v_pk_fma_f32 v[116:117], v[116:117], v[44:45], v[36:37] op_sel:[0,0,0] op_sel_hi:[1,1,1] neg_lo:[0,0,1] neg_hi:[0,0,1]\n"
    "v_pk_fma_f32 v[118:119], v[118:119], v[46:47], v[38:39] op_sel:[0,0,0] op_sel_hi:[1,1,1] neg_lo:[0,0,1] neg_hi:[0,0,1]\n"
    "v_cvt_pk_bf16_f32 v112, v112, v113\n"
    "v_cvt_pk_bf16_f32 v113, v114, v115\n"
    "v_cvt_pk_bf16_f32 v114, v116, v117\n"
    "v_cvt_pk_bf16_f32 v115, v118, v119\n"
    "global_store_dwordx4 v14, v[112:115], s[64:65] offset:-4096 sc1\n"
    "v_cvt_f32_i32 v120, v120\n"
    "v_cvt_f32_i32 v121, v121\n"
    "v_cvt_f32_i32 v122, v122\n"
    "v_cvt_f32_i32 v123, v123\n"
    "v_cvt_f32_i32 v124, v124\n"
    "v_cvt_f32_i32 v125, v125\n"
    "v_cvt_f32_i32 v126, v126\n"
    "v_cvt_f32_i32 v127, v127\n"
    "v_pk_fma_f32 v[120:121], v[120:121], v[40:41], v[32:33] op_sel:[0,0,0] op_sel_hi:[1,1,1] neg_lo:[0,0,1] neg_hi:[0,0,1]\n"
    "v_pk_fma_f32 v[122:123], v[122:123], v[42:43], v[34:35] op_sel:[0,0,0] op_sel_hi:[1,1,1] neg_lo:[0,0,1] neg_hi:[0,0,1]\n"
    "v_pk_fma_f32 v[124:125], v[124:125], v[44:45], v[36:37] op_sel:[0,0,0] op_sel_hi:[1,1,1] neg_lo:[0,0,1] neg_hi:[0,0,1]\n"
    "v_pk_fma_f32 v[126:127], v[126:127], v[46:47], v[38:39] op_sel:[0,0,0] op_sel_hi:[1,1,1] neg_lo:[0,0,1] neg_hi:[0,0,1]\n"
    "v_cvt_pk_bf16_f32 v120, v120, v121\n"
    "v_cvt_pk_bf16_f32 v121, v122, v123\n"
    "v_cvt_pk_bf16_f32 v122, v124, v125\n"
    "v_cvt_pk_bf16_f32 v123, v126, v127\n"
    "global_store_dwordx4 v14, v[120:123], s[64:65] offset:0 sc1\n"
    "L%=_u0e:\n"
    "s_add_u32 s64, s64, 0x40000\n"
    "s_barrier\n"
    "s_add_u32 m0, s66, 0\n"
    "s_add_u32 s75, s74, 0\n"
    "s_and_b32 s75, s75, 3\n"
    "s_lshl_b32 s75, s75, 12\n"
    "s_add_u32 s44, s40, s75\n"
    "s_addc_u32 s45, s41, 0\n"
    "v_xor_b32 v20, 0, v21\n"
    "global_load_lds_dwordx4 v20, s[44:45] offset:0 nt\n"
    "v_xor_b32 v20, 64, v21\n"
    "global_load_lds_dwordx4 v20, s[44:45] offset:1024 nt\n"
    "v_xor_b32 v20, 128, v21\n"
    "global_load_lds_dwordx4 v20, s[44:45] offset:2048 nt\n"
    "v_xor_b32 v20, 192, v21\n"
    "global_load_lds_dwordx4 v20, s[44:45] offset:3072 nt\n"
    "s_add_u32 m0, s66, 4096\n"
    "s_add_u32 s75, s74, 1\n"
    "s_and_b32 s75, s75, 3\n"
    "s_lshl_b32 s75, s75, 12\n"
    "s_add_u32 s44, s40, s75\n"
    "s_addc_u32 s45, s41, 0\n"
    "v_xor_b32 v20, 0, v21\n"
    "global_load_lds_dwordx4 v20, s[44:45] offset:0 nt\n"
    "v_xor_b32 v20, 64, v21\n"
    "global_load_lds_dwordx4 v20, s[44:45] offset:1024 nt\n"
    "v_add_u32 v8, s69, v6\n"
    "v_add_u32 v9, 0x4000, v8\n"
    "v_lshrrev_b32 v4, 3, v105\n"
    "v_lshlrev_b32 v4, 1, v4\n"
    "v_xor_b32 v4, v106, v4\n"
    "v_lshl_add_u32 v4, v105, 2, v4\n"
    "v_lshlrev_b32 v4, 4, v4\n"
    "v_add_u32 v5, 0x10000, v4\n"
    "v_add_u32 v7, 0x10000, v6\n"
    "s_lshl_b32 s61, %3, 13\n"
    "s_lshl_b32 s75, s59, 3\n"
    "s_add_u32 s61, s61, s75\n"
    "v_lshl_add_u32 v22, v104, 10, s61\n"
    "v_accvgpr_write_b32 a158, v22\n"
    "s_waitcnt vmcnt(16)\n"
    "v_lshlrev_b32 v80, 16, v96\n"
    "v_and_b32 v81, 0xffff0000, v96\n"
    "v_lshlrev_b32 v82, 16, v97\n"
    "v_and_b32 v83, 0xffff0000, v97\n"
    "v_lshlrev_b32 v84, 16, v98\n"
    "v_and_b32 v85, 0xffff0000, v98\n"
    "v_lshlrev_b32 v86, 16, v99\n"
    "v_and_b32 v87, 0xffff0000, v99\n"
    "v_lshlrev_b32 v88, 16, v100\n"
    "v_and_b32 v89, 0xffff0000, v100\n"
    "v_lshlrev_b32 v90, 16, v101\n"
    "v_and_b32 v91, 0xffff0000, v101\n"
    "v_lshlrev_b32 v92, 16, v102\n"
    "v_and_b32 v93, 0xffff0000, v102\n"
    "v_lshlrev_b32 v94, 16, v103\n"
    "v_and_b32 v95, 0xffff0000, v103\n"
    "v_pk_mul_f32 v[88:89], v[88:89], s[36:37]\n"
    "v_pk_mul_f32 v[90:91], v[90:91], s[36:37]\n"
    "v_pk_mul_f32 v[92:93], v[92:93], s[36:37]\n"
    "v_pk_mul_f32 v[94:95], v[94:95], s[36:37]\n"
    "v_pk_mul_f32 v[80:81], v[80:81], v[88:89]\n"
    "v_pk_mul_f32 v[82:83], v[82:83], v[90:91]\n"
    "v_pk_mul_f32 v[84:85], v[84:85], v[92:93]\n"
    "v_pk_mul_f32 v[86:87], v[86:87], v[94:95]\n"
    "s_waitcnt vmcnt(14)\n"
    "v_cvt_f32_i32 v48, v48\n"
    "v_cvt_f32_i32 v49, v49\n"
    "v_cvt_f32_i32 v50, v50\n"
    "v_cvt_f32_i32 v51, v51\n"
    "v_cvt_f32_i32 v52, v52\n"
    "v_cvt_f32_i32 v53, v53\n"
    "v_cvt_f32_i32 v54, v54\n"
    "v_cvt_f32_i32 v55, v55\n"
    "v_pk_fma_f32 v[48:49], v[48:49], v[88:89], v[80:81] op_sel:[0,0,0] op_sel_hi:[1,1,1] neg_lo:[0,0,1] neg_hi:[0,0,1]\n"
    "v_pk_fma_f32 v[50:51], v[50:51], v[90:91], v[82:83] op_sel:[0,0,0] op_sel_hi:[1,1,1] neg_lo:[0,0,1] neg_hi:[0,0,1]\n"
    "v_pk_fma_f32 v[52:53], v[52:53], v[92:93], v[84:85] op_sel:[0,0,0] op_sel_hi:[1,1,1] neg_lo:[0,0,1] neg_hi:[0,0,1]\n"
    "v_pk_fma_f32 v[54:55], v[54:55], v[94:95], v[86:87] op_sel:[0,0,0] op_sel_hi:[1,1,1] neg_lo:[0,0,1] neg_hi:[0,0,1]\n"
    "v_cvt_pk_bf16_f32 v96, v48, v49\n"
    "v_cvt_pk_bf16_f32 v97, v50, v51\n"
    "v_cvt_pk_bf16_f32 v98, v52, v53\n"
    "v_cvt_pk_bf16_f32 v99, v54, v55\n"
    "ds_write_b128 v107, v[96:99] offset:0\n"
    "s_waitcnt vmcnt(12)\n"
    "v_cvt_f32_i32 v56, v56\n"
    "v_cvt_f32_i32 v57, v57\n"
    "v_cvt_f32_i32 v58, v58\n"
    "v_cvt_f32_i32 v59, v59\n"
    "v_cvt_f32_i32 v60, v60\n"
    "v_cvt_f32_i32 v61, v61\n"
    "v_cvt_f32_i32 v62, v62\n"
    "v_cvt_f32_i32 v63, v63\n"
    "v_pk_fma_f32 v[56:57], v[56:57], v[88:89], v[80:81] op_sel:[0,0,0] op_sel_hi:[1,1,1] neg_lo:[0,0,1] neg_hi:[0,0,1]\n"
    "v_pk_fma_f32 v[58:59], v[58:59], v[90:91], v[82:83] op_sel:[0,0,0] op_sel_hi:[1,1,1] neg_lo:[0,0,1] neg_hi:[0,0,1]\n"
    "v_pk_fma_f32 v[60:61], v[60:61], v[92:93], v[84:85] op_sel:[0,0,0] op_sel_hi:[1,1,1] neg_lo:[0,0,1] neg_hi:[0,0,1]\n"
    "v_pk_fma_f32 v[62:63], v[62:63], v[94:95], v[86:87] op_sel:[0,0,0] op_sel_hi:[1,1,1] neg_lo:[0,0,1] neg_hi:[0,0,1]\n"
    "v_cvt_pk_bf16_f32 v100, v56, v57\n"
    "v_cvt_pk_bf16_f32 v101, v58, v59\n"
    "v_cvt_pk_bf16_f32 v102, v60, v61\n"
    "v_cvt_pk_bf16_f32 v103, v62, v63\n"
    "ds_write_b128 v107, v[100:103] offset:4096\n"
    "s_waitcnt vmcnt(10)\n"
    "v_cvt_f32_i32 v64, v64\n"
    "v_cvt_f32_i32 v65, v65\n"
    "v_cvt_f32_i32 v66, v66\n"
    "v_cvt_f32_i32 v67, v67\n"
    "v_cvt_f32_i32 v68, v68\n"
    "v_cvt_f32_i32 v69, v69\n"
    "v_cvt_f32_i32 v70, v70\n"
    "v_cvt_f32_i32 v71, v71\n"
    "v_pk_fma_f32 v[64:65], v[64:65], v[88:89], v[80:81] op_sel:[0,0,0] op_sel_hi:[1,1,1] neg_lo:[0,0,1] neg_hi:[0,0,1]\n"
    "v_pk_fma_f32 v[66:67], v[66:67], v[90:91], v[82:83] op_sel:[0,0,0] op_sel_hi:[1,1,1] neg_lo:[0,0,1] neg_hi:[0,0,1]\n"
    "v_pk_fma_f32 v[68:69], v[68:69], v[92:93], v[84:85] op_sel:[0,0,0] op_sel_hi:[1,1,1] neg_lo:[0,0,1] neg_hi:[0,0,1]\n"
    "v_pk_fma_f32 v[70:71], v[70:71], v[94:95], v[86:87] op_sel:[0,0,0] op_sel_hi:[1,1,1] neg_lo:[0,0,1] neg_hi:[0,0,1]\n"
    "v_cvt_pk_bf16_f32 v96, v64, v65\n"
    "v_cvt_pk_bf16_f32 v97, v66, v67\n"
    "v_cvt_pk_bf16_f32 v98, v68, v69\n"
    "v_cvt_pk_bf16_f32 v99, v70, v71\n"
    "ds_write_b128 v107, v[96:99] offset:256\n"
    "s_waitcnt vmcnt(8)\n"
    "v_cvt_f32_i32 v72, v72\n"
    "v_cvt_f32_i32 v73, v73\n"
    "v_cvt_f32_i32 v74, v74\n"
    "v_cvt_f32_i32 v75, v75\n"
    "v_cvt_f32_i32 v76, v76\n"
    "v_cvt_f32_i32 v77, v77\n"
    "v_cvt_f32_i32 v78, v78\n"
    "v_cvt_f32_i32 v79, v79\n"
    "v_pk_fma_f32 v[72:73], v[72:73], v[88:89], v[80:81] op_sel:[0,0,0] op_sel_hi:[1,1,1] neg_lo:[0,0,1] neg_hi:[0,0,1]\n"
    "v_pk_fma_f32 v[74:75], v[74:75], v[90:91], v[82:83] op_sel:[0,0,0] op_sel_hi:[1,1,1] neg_lo:[0,0,1] neg_hi:[0,0,1]\n"
    "v_pk_fma_f32 v[76:77], v[76:77], v[92:93], v[84:85] op_sel:[0,0,0] op_sel_hi:[1,1,1] neg_lo:[0,0,1] neg_hi:[0,0,1]\n"
    "v_pk_fma_f32 v[78:79], v[78:79], v[94:95], v[86:87] op_sel:[0,0,0] op_sel_hi:[1,1,1] neg_lo:[0,0,1] neg_hi:[0,0,1]\n"
    "v_cvt_pk_bf16_f32 v100, v72, v73\n"
    "v_cvt_pk_bf16_f32 v101, v74, v75\n"
    "v_cvt_pk_bf16_f32 v102, v76, v77\n"
    "v_cvt_pk_bf16_f32 v103, v78, v79\n"
    "ds_write_b128 v107, v[100:103] offset:4352\n"
    "s_waitcnt lgkmcnt(0)\n"
    "s_add_u32 s72, s72, 1\n"
    "s_not_b32 s73, s72\n"
    "v_accvgpr_write_b32 a156, s72\n"
    "v_accvgpr_write_b32 a157, s73\n"
    "v_accvgpr_read_b32 v2, a158\n"
    "s_waitcnt vmcnt(6)\n"
    "s_barrier\n"
    "s_mov_b64 exec, 0xff\n"
    "global_store_dwordx2 v2, a[156:157], s[38:39] sc1\n"
    "s_mov_b64 exec, -1\n"
    "ds_read_b128 a[160:163], v5 offset:32768\n"
    "ds_read_b128 a[164:167], v5 offset:33792\n"
    "ds_read_b128 a[168:171], v5 offset:34816\n"
    "ds_read_b128 a[172:175], v5 offset:35840\n"
    "ds_read_b128 a[176:179], v5 offset:36864\n"
    "ds_read_b128 a[180:183], v5 offset:37888\n"
    "ds_read_b128 a[184:187], v5 offset:38912\n"
    "ds_read_b128 a[188:191], v5 offset:39936\n"
    "ds_read_b128 a[192:195], v5 offset:40960\n"
    "ds_read_b128 a[196:199], v5 offset:41984\n"
    "ds_read_b128 a[200:203], v5 offset:43008\n"
    "ds_read_b128 a[204:207], v5 offset:44032\n"
    "v_xor_b32 v20, 128, v21\n"
    "global_load_lds_dwordx4 v20, s[44:45] offset:2048 nt\n"
    "v_xor_b32 v20, 192, v21\n"
    "global_load_lds_dwordx4 v20, s[44:45] offset:3072 nt\n"
    "s_add_u32 m0, s66, 8192\n"
    "s_add_u32 s75, s74, 2\n"
    "s_and_b32 s75, s75, 3\n"
    "s_lshl_b32 s75, s75, 12\n"
    "s_add_u32 s44, s40, s75\n"
    "s_addc_u32 s45, s41, 0\n"
    "v_xor_b32 v20, 0, v21\n"
    "global_load_lds_dwordx4 v20, s[44:45] offset:0 nt\n"
    "v_xor_b32 v20, 64, v21\n"
    "global_load_lds_dwordx4 v20, s[44:45] offset:1024 nt\n"
    "v_and_b32 v22, 11, v104\n"
    "v_cmp_eq_u32 vcc, 0, v22\n"
    "v_mov_b32 v22, 0x3f803f80\n"
    "v_xor_b32 v17, 64, v16\n"
    "v_cndmask_b32 v22, 0, v22, vcc\n"
    "v_accvgpr_write_b32 a144, v22\n"
    "v_accvgpr_write_b32 a145, v22\n"
    "v_accvgpr_write_b32 a146, v22\n"
    "v_accvgpr_write_b32 a147, v22\n"
    "v_cmp_eq_u32 vcc, 4, v105\n"
    "v_mov_b32 v10, 0x44444444\n"
    "v_mov_b32 v22, 0xeeeeeeee\n"
    "v_cndmask_b32 v10, v10, v22, vcc\n"
    "v_xor_b32 v18, 0x80, v16\n"
    "v_xor_b32 v19, 0xc0, v16\n"
    "v_and_b32 v22, 31, v104\n"
    "s_lshl_b32 s61, s59, 10\n"
    "v_lshl_add_u32 v22, v22, 3, s61\n"
    "v_accvgpr_write_b32 a155, v22\n"
    "s_waitcnt lgkmcnt(6)\n"
    "ds_read_b128 a[208:211], v5 offset:45056\n"
    "ds_read_b128 a[212:215], v5 offset:46080\n"
    "ds_read_b128 a[216:219], v5 offset:47104\n"
    "ds_read_b128 a[220:223], v5 offset:48128\n"
    "v_accvgpr_write_b32 a128, 0\n"
    "v_accvgpr_write_b32 a129, 0\n"
    "v_accvgpr_write_b32 a130, 0\n"
    "v_accvgpr_write_b32 a131, 0\n"
    "v_accvgpr_write_b32 a132, 0\n"
    "v_accvgpr_write_b32 a133, 0\n"
    "v_accvgpr_write_b32 a134, 0\n"
    "v_accvgpr_write_b32 a135, 0\n"
    "v_accvgpr_write_b32 a136, 0\n"
    "v_accvgpr_write_b32 a137, 0\n"
    "v_accvgpr_write_b32 a138, 0\n"
    "v_accvgpr_write_b32 a139, 0\n"
    "v_accvgpr_write_b32 a140, 0\n"
    "v_accvgpr_write_b32 a141, 0\n"
    "v_accvgpr_write_b32 a142, 0\n"
    "v_accvgpr_write_b32 a143, 0\n"
    "s_waitcnt vmcnt(7)\n"
    "ds_read_b128 a[0:3], v16 offset:0\n"
    "ds_read_b128 a[4:7], v17 offset:0\n"
    "ds_read_b128 a[8:11], v18 offset:0\n"
    "ds_read_b128 a[12:15], v19 offset:0\n"
    "s_waitcnt lgkmcnt(0)\n"
    "v_mfma_f32_16x16x32_bf16 v[80:83], a[160:163], a[0:3], 4.0\n"
    "v_mfma_f32_16x16x32_bf16 v[80:83], a[164:167], a[4:7], v[80:83]\n"
    "v_mfma_f32_16x16x32_bf16 v[80:83], a[168:171], a[8:11], v[80:83]\n"
    "v_mfma_f32_16x16x32_bf16 v[80:83], a[172:175], a[12:15], v[80:83]\n"
    "v_mfma_f32_16x16x32_bf16 v[84:87], a[188:191], a[12:15], 4.0\n"
    "v_mfma_f32_16x16x32_bf16 v[84:87], a[184:187], a[8:11], v[84:87]\n"
    "v_mfma_f32_16x16x32_bf16 v[84:87], a[180:183], a[4:7], v[84:87]\n"
    "v_mfma_f32_16x16x32_bf16 v[84:87], a[176:179], a[0:3], v[84:87]\n"
    "v_mfma_f32_16x16x32_bf16 v[88:91], a[192:195], a[0:3], 4.0\n"
    "v_mfma_f32_16x16x32_bf16 v[88:91], a[196:199], a[4:7], v[88:91]\n"
    "v_mfma_f32_16x16x32_bf16 v[88:91], a[200:203], a[8:11], v[88:91]\n"
    "v_mfma_f32_16x16x32_bf16 v[88:91], a[204:207], a[12:15], v[88:91]\n"
    "v_exp_f32 v80, v80\n"
    "v_mfma_f32_16x16x32_bf16 v[92:95], a[220:223], a[12:15], 4.0\n"
    "v_exp_f32 v81, v81\n"
    "v_mfma_f32_16x16x32_bf16 v[92:95], a[216:219], a[8:11], v[92:95]\n"
    "v_exp_f32 v82, v82\n"
    "v_mfma_f32_16x16x32_bf16 v[92:95], a[212:215], a[4:7], v[92:95]\n"
    "v_exp_f32 v83, v83\n"
    "v_mfma_f32_16x16x32_bf16 v[92:95], a[208:211], a[0:3], v[92:95]\n"
    "v_cvt_pk_bf16_f32 v48, v80, v81\n"
    "v_cvt_pk_bf16_f32 v49, v82, v83\n"
    "v_exp_f32 v84, v84\n"
    "v_exp_f32 v85, v85\n"
    "v_exp_f32 v86, v86\n"
    "v_exp_f32 v87, v87\n"
    "v_cvt_pk_bf16_f32 v50, v84, v85\n"
    "v_cvt_pk_bf16_f32 v51, v86, v87\n"
    "v_exp_f32 v88, v88\n"
    "v_exp_f32 v89, v89\n"
    "v_exp_f32 v90, v90\n"
    "v_exp_f32 v91, v91\n"
    "v_cvt_pk_bf16_f32 v52, v88, v89\n"
    "v_cvt_pk_bf16_f32 v53, v90, v91\n"
    "v_exp_f32 v92, v92\n"
    "v_exp_f32 v93, v93\n"
    "v_exp_f32 v94, v94\n"
    "v_exp_f32 v95, v95\n"
    "v_cvt_pk_bf16_f32 v54, v92, v93\n"
    "v_cvt_pk_bf16_f32 v55, v94, v95\n"
    "s_mov_b64 exec, 0\n"
    "s_cmp_eq_u32 %3, 0\n"
    "s_cbranch_scc1 L%=_w0\n"
    "s_cmp_eq_u32 %3, 1\n"
    "s_cbranch_scc1 L%=_w1\n"
    "s_cmp_eq_u32 %3, 2\n"
    "s_cbranch_scc1 L%=_w2\n"
    "s_branch L%=_w3\n"
    "L%=_wret:\n"
    "s_mov_b64 exec, -1\n"
    "v_accvgpr_read_b32 v2, a155\n"
    "global_load_dwordx2 v[2:3], v2, s[38:39] sc1\n"
    "v_xor_b32 v20, 128, v21\n"
    "global_load_lds_dwordx4 v20, s[44:45] offset:2048 nt\n"
    "v_xor_b32 v20, 192, v21\n"
    "global_load_lds_dwordx4 v20, s[44:45] offset:3072 nt\n"
    "s_add_u32 m0, s66, 12288\n"
    "s_add_u32 s75, s74, 3\n"
    "s_and_b32 s75, s75, 3\n"
    "s_lshl_b32 s75, s75, 12\n"
    "s_add_u32 s44, s40, s75\n"
    "s_addc_u32 s45, s41, 0\n"
    "v_xor_b32 v20, 0, v21\n"
    "global_load_lds_dwordx4 v20, s[44:45] offset:0 nt\n"
    "v_xor_b32 v20, 64, v21\n"
    "global_load_lds_dwordx4 v20, s[44:45] offset:1024 nt\n"
    "v_xor_b32 v20, 128, v21\n"
    "global_load_lds_dwordx4 v20, s[44:45] offset:2048 nt\n"
    "v_xor_b32 v20, 192, v21\n"
    "global_load_lds_dwordx4 v20, s[44:45] offset:3072 nt\n"
    "s_waitcnt vmcnt(9)\n"
    "ds_read_b128 a[16:19], v16 offset:4096\n"
    "ds_read_b128 a[20:23], v17 offset:4096\n"
    "ds_read_b128 a[24:27], v18 offset:4096\n"
    "ds_read_b128 a[28:31], v19 offset:4096\n"
    "s_waitcnt lgkmcnt(0)\n"
    "v_mfma_f32_16x16x32_bf16 v[96:99], a[160:163], a[16:19], 4.0\n"
    "v_mfma_f32_16x16x32_bf16 v[96:99], a[164:167], a[20:23], v[96:99]\n"
    "v_mfma_f32_16x16x32_bf16 v[96:99], a[168:171], a[24:27], v[96:99]\n"
    "v_mfma_f32_16x16x32_bf16 v[96:99], a[172:175], a[28:31], v[96:99]\n"
    "v_mfma_f32_16x16x32_bf16 v[100:103], a[188:191], a[28:31], 4.0\n"
    "v_mfma_f32_16x16x32_bf16 v[100:103], a[184:187], a[24:27], v[100:103]\n"
    "v_mfma_f32_16x16x32_bf16 v[100:103], a[180:183], a[20:23], v[100:103]\n"
    "v_mfma_f32_16x16x32_bf16 v[100:103], a[176:179], a[16:19], v[100:103]\n"
    "v_mfma_f32_16x16x32_bf16 v[104:107], a[192:195], a[16:19], 4.0\n"
    "v_mfma_f32_16x16x32_bf16 v[104:107], a[196:199], a[20:23], v[104:107]\n"
    "v_mfma_f32_16x16x32_bf16 v[104:107], a[200:203], a[24:27], v[104:107]\n"
    "v_mfma_f32_16x16x32_bf16 v[104:107], a[204:207], a[28:31], v[104:107]\n"
    "v_exp_f32 v96, v96\n"
    "v_mfma_f32_16x16x32_bf16 v[108:111], a[220:223], a[28:31], 4.0\n"
    "v_exp_f32 v97, v97\n"
    "v_mfma_f32_16x16x32_bf16 v[108:111], a[216:219], a[24:27], v[108:111]\n"
    "v_exp_f32 v98, v98\n"
    "v_mfma_f32_16x16x32_bf16 v[108:111], a[212:215], a[20:23], v[108:111]\n"
    "v_exp_f32 v99, v99\n"
    "v_mfma_f32_16x16x32_bf16 v[108:111], a[208:211], a[16:19], v[108:111]\n"
    "v_cvt_pk_bf16_f32 v56, v96, v97\n"
    "v_cvt_pk_bf16_f32 v57, v98, v99\n"
    "v_exp_f32 v100, v100\n"
    "v_exp_f32 v101, v101\n"
    "v_exp_f32 v102, v102\n"
    "v_exp_f32 v103, v103\n"
    "v_cvt_pk_bf16_f32 v58, v100, v101\n"
    "v_cvt_pk_bf16_f32 v59, v102, v103\n"
    "v_exp_f32 v104, v104\n"
    "v_exp_f32 v105, v105\n"
    "v_exp_f32 v106, v106\n"
    "v_exp_f32 v107, v107\n"
    "v_cvt_pk_bf16_f32 v60, v104, v105\n"
    "v_cvt_pk_bf16_f32 v61, v106, v107\n"
    "v_exp_f32 v108, v108\n"
    "v_exp_f32 v109, v109\n"
    "v_exp_f32 v110, v110\n"
    "v_exp_f32 v111, v111\n"
    "v_cvt_pk_bf16_f32 v62, v108, v109\n"
    "v_cvt_pk_bf16_f32 v63, v110, v111\n"
    "s_waitcnt vmcnt(6)\n"
    "v_cmp_ne_u64 vcc, s[72:73], v[2:3]\n"
    "s_and_b32 s78, vcc_lo, 0xf\n"
    "s_cbranch_scc1 L%=_p1_retry\n"
    "L%=_p1_ok:\n"
    "s_add_u32 m0, s69, 0\n"
    "s_nop 0\n"
    "global_load_lds_dwordx4 v8, s[62:63] offset:0 sc1\n"
    "global_load_lds_dwordx4 v8, s[62:63] offset:1024 sc1\n"
    "global_load_lds_dwordx4 v8, s[62:63] offset:2048 sc1\n"
    "global_load_lds_dwordx4 v8, s[62:63] offset:3072 sc1\n"
    "s_add_u32 m0, s69, 16384\n"
    "s_nop 0\n"
    "global_load_lds_dwordx4 v9, s[62:63] offset:0 sc1\n"
    "global_load_lds_dwordx4 v9, s[62:63] offset:1024 sc1\n"
    "global_load_lds_dwordx4 v9, s[62:63] offset:2048 sc1\n"
    "global_load_lds_dwordx4 v9, s[62:63] offset:3072 sc1\n"
    "s_add_u32 s62, s62, 0x8000\n"
    "s_waitcnt vmcnt(12)\n"
    "ds_read_b128 a[32:35], v16 offset:8192\n"
    "ds_read_b128 a[36:39], v17 offset:8192\n"
    "ds_read_b128 a[40:43], v18 offset:8192\n"
    "ds_read_b128 a[44:47], v19 offset:8192\n"
    "s_waitcnt lgkmcnt(0)\n"
    "v_mfma_f32_16x16x32_bf16 v[80:83], a[160:163], a[32:35], 4.0\n"
    "v_mfma_f32_16x16x32_bf16 v[80:83], a[164:167], a[36:39], v[80:83]\n"
    "v_mfma_f32_16x16x32_bf16 v[80:83], a[168:171], a[40:43], v[80:83]\n"
    "v_mfma_f32_16x16x32_bf16 v[80:83], a[172:175], a[44:47], v[80:83]\n"
    "v_mfma_f32_16x16x32_bf16 v[84:87], a[188:191], a[44:47], 4.0\n"
    "v_mfma_f32_16x16x32_bf16 v[84:87], a[184:187], a[40:43], v[84:87]\n"
    "v_mfma_f32_16x16x32_bf16 v[84:87], a[180:183], a[36:39], v[84:87]\n"
    "v_mfma_f32_16x16x32_bf16 v[84:87], a[176:179], a[32:35], v[84:87]\n"
    "v_mfma_f32_16x16x32_bf16 v[88:91], a[192:195], a[32:35], 4.0\n"
    "v_mfma_f32_16x16x32_bf16 v[88:91], a[196:199], a[36:39], v[88:91]\n"
    "v_mfma_f32_16x16x32_bf16 v[88:91], a[200:203], a[40:43], v[88:91]\n"
    "v_mfma_f32_16x16x32_bf16 v[88:91], a[204:207], a[44:47], v[88:91]\n"
    "v_exp_f32 v80, v80\n"
    "v_mfma_f32_16x16x32_bf16 v[24:27], a[220:223], a[44:47], 4.0\n"
    "v_exp_f32 v81, v81\n"
    "v_mfma_f32_16x16x32_bf16 v[24:27], a[216:219], a[40:43], v[24:27]\n"
    "v_exp_f32 v82, v82\n"
    "v_mfma_f32_16x16x32_bf16 v[24:27], a[212:215], a[36:39], v[24:27]\n"
    "v_exp_f32 v83, v83\n"
    "v_mfma_f32_16x16x32_bf16 v[24:27], a[208:211], a[32:35], v[24:27]\n"
    "v_cvt_pk_bf16_f32 v64, v80, v81\n"
    "v_cvt_pk_bf16_f32 v65, v82, v83\n"
    "v_exp_f32 v84, v84\n"
    "v_exp_f32 v85, v85\n"
    "v_exp_f32 v86, v86\n"
    "v_exp_f32 v87, v87\n"
    "v_cvt_pk_bf16_f32 v66, v84, v85\n"
    "v_cvt_pk_bf16_f32 v67, v86, v87\n"
    "v_exp_f32 v88, v88\n"
    "v_exp_f32 v89, v89\n"
    "v_exp_f32 v90, v90\n"
    "v_exp_f32 v91, v91\n"
    "v_cvt_pk_bf16_f32 v68, v88, v89\n"
    "v_cvt_pk_bf16_f32 v69, v90, v91\n"
    "v_exp_f32 v24, v24\n"
    "v_exp_f32 v25, v25\n"
    "v_cmp_ne_u64 vcc, s[72:73], v[2:3]\n"
    "s_and_b32 s78, vcc_lo, 0xffffffff\n"
    "s_cbranch_scc1 L%=_p2_retry\n"
    "L%=_p2_ok:\n"
    "s_add_u32 m0, s69, 32768\n"
    "s_nop 0\n"
    "global_load_lds_dwordx4 v8, s[62:63] offset:0 sc1\n"
    "global_load_lds_dwordx4 v8, s[62:63] offset:1024 sc1\n"
    "global_load_lds_dwordx4 v8, s[62:63] offset:2048 sc1\n"
    "global_load_lds_dwordx4 v8, s[62:63] offset:3072 sc1\n"
    "s_add_u32 m0, s69, 49152\n"
    "s_nop 0\n"
    "global_load_lds_dwordx4 v9, s[62:63] offset:0 sc1\n"
    "global_load_lds_dwordx4 v9, s[62:63] offset:1024 sc1\n"
    "global_load_lds_dwordx4 v9, s[62:63] offset:2048 sc1\n"
    "global_load_lds_dwordx4 v9, s[62:63] offset:3072 sc1\n"
    "s_add_u32 s62, s62, 0x8000\n"
    "s_add_u32 s38, s38, 0x100\n"
    "L%=_split:\n"
    "s_waitcnt vmcnt(16)\n"
    "ds_read_b128 a[48:51], v16 offset:12288\n"
    "ds_read_b128 a[52:55], v17 offset:12288\n"
    "ds_read_b128 a[56:59], v18 offset:12288\n"
    "ds_read_b128 a[60:63], v19 offset:12288\n"
    "s_waitcnt lgkmcnt(0)\n"
    "s_waitcnt vmcnt(12)\n"
    "s_add_u32 m0, s69, 65536\n"
    "s_barrier\n"
    "v_mfma_f32_16x16x32_bf16 v[96:99], a[160:163], a[48:51], 4.0\n"
    "L%=_w0:\n"
    "s_cbranch_execz L%=_w4\n"
    "ds_read_b128 a[64:67], v4 offset:0\n"
    "v_mfma_f32_16x16x32_bf16 v[96:99], a[164:167], a[52:55], v[96:99]\n"
    "ds_read_b128 a[68:71], v4 offset:1024\n"
    "v_mfma_f32_16x16x32_bf16 v[96:99], a[168:171], a[56:59], v[96:99]\n"
    "ds_read_b128 a[72:75], v4 offset:2048\n"
    "v_mfma_f32_16x16x32_bf16 v[96:99], a[172:175], a[60:63], v[96:99]\n"
    "ds_read_b128 a[76:79], v4 offset:3072\n"
    "v_mfma_f32_16x16x32_bf16 v[100:103], a[188:191], a[60:63], 4.0\n"
    "ds_read_b128 a[80:83], v4 offset:4096\n"
    "v_mfma_f32_16x16x32_bf16 v[100:103], a[184:187], a[56:59], v[100:103]\n"
    "ds_read_b128 a[84:87], v4 offset:5120\n"
    "v_mfma_f32_16x16x32_bf16 v[100:103], a[180:183], a[52:55], v[100:103]\n"
    "ds_read_b128 a[88:91], v4 offset:6144\n"
    "v_mfma_f32_16x16x32_bf16 v[100:103], a[176:179], a[48:51], v[100:103]\n"
    "ds_read_b128 a[92:95], v4 offset:7168\n"
    "v_mfma_f32_16x16x32_bf16 v[104:107], a[192:195], a[48:51], 4.0\n"
    "v_mfma_f32_16x16x32_bf16 v[104:107], a[196:199], a[52:55], v[104:107]\n"
    "v_mfma_f32_16x16x32_bf16 v[104:107], a[200:203], a[56:59], v[104:107]\n"
    "v_mfma_f32_16x16x32_bf16 v[104:107], a[204:207], a[60:63], v[104:107]\n"
    "v_exp_f32 v96, v96\n"
    "v_mfma_f32_16x16x32_bf16 v[28:31], a[220:223], a[60:63], 4.0\n"
    "v_exp_f32 v97, v97\n"
    "v_mfma_f32_16x16x32_bf16 v[28:31], a[216:219], a[56:59], v[28:31]\n"
    "v_exp_f32 v98, v98\n"
    "v_mfma_f32_16x16x32_bf16 v[28:31], a[212:215], a[52:55], v[28:31]\n"
    "v_exp_f32 v99, v99\n"
    "v_mfma_f32_16x16x32_bf16 v[28:31], a[208:211], a[48:51], v[28:31]\n"
    "v_cvt_pk_bf16_f32 v72, v96, v97\n"
    "v_cvt_pk_bf16_f32 v73, v98, v99\n"
    "v_exp_f32 v100, v100\n"
    "v_exp_f32 v101, v101\n"
    "v_exp_f32 v102, v102\n"
    "v_exp_f32 v103, v103\n"
    "v_cvt_pk_bf16_f32 v74, v100, v101\n"
    "v_cvt_pk_bf16_f32 v75, v102, v103\n"
    "v_exp_f32 v104, v104\n"
    "v_exp_f32 v105, v105\n"
    "v_exp_f32 v106, v106\n"
    "v_exp_f32 v107, v107\n"
    "v_cvt_pk_bf16_f32 v76, v104, v105\n"
    "v_cvt_pk_bf16_f32 v77, v106, v107\n"
    "s_waitcnt vmcnt(8)\n"
    "s_barrier\n"
    "ds_read_b128 a[96:99], v6 offset:16384\n"
    "ds_read_b128 a[100:103], v6 offset:17408\n"
    "ds_read_b128 a[104:107], v6 offset:18432\n"
    "ds_read_b128 a[108:111], v6 offset:19456\n"
    "s_waitcnt lgkmcnt(4)\n"
    "L%=_g0:\n"
    "v_mfma_f32_16x16x32_bf16 v[16:19], a[64:67], a[0:3], 4.0\n"
    "v_exp_f32 v26, v26\n"
    "v_mfma_f32_16x16x32_bf16 v[16:19], a[68:71], a[4:7], v[16:19]\n"
    "ds_read_b128 a[112:115], v6 offset:20480\n"
    "v_mfma_f32_16x16x32_bf16 v[16:19], a[72:75], a[8:11], v[16:19]\n"
    "v_exp_f32 v27, v27\n"
    "v_mfma_f32_16x16x32_bf16 v[16:19], a[76:79], a[12:15], v[16:19]\n"
    "v_cvt_pk_bf16_f32 v70, v24, v25\n"
    "v_cvt_pk_bf16_f32 v71, v26, v27\n"
    "v_mfma_f32_16x16x32_bf16 v[20:23], a[76:79], a[28:31], 4.0\n"
    "v_exp_f32 v28, v28\n"
    "v_mfma_f32_16x16x32_bf16 v[20:23], a[72:75], a[24:27], v[20:23]\n"
    "ds_read_b128 a[116:119], v6 offset:21504\n"
    "v_mfma_f32_16x16x32_bf16 v[20:23], a[68:71], a[20:23], v[20:23]\n"
    "global_load_lds_dwordx4 v8, s[62:63] offset:2048 sc1\n"
    "v_mfma_f32_16x16x32_bf16 v[20:23], a[64:67], a[16:19], v[20:23]\n"
    "v_exp_f32 v29, v29\n"
    "v_mfma_f32_16x16x32_bf16 v[24:27], a[64:67], a[32:35], 4.0\n"
    "v_exp_f32 v30, v30\n"
    "v_mfma_f32_16x16x32_bf16 v[24:27], a[68:71], a[36:39], v[24:27]\n"
    "global_load_lds_dwordx4 v8, s[62:63] offset:0 sc1\n"
    "ds_read_b128 a[120:123], v6 offset:22528\n"
    "v_mfma_f32_16x16x32_bf16 v[24:27], a[72:75], a[40:43], v[24:27]\n"
    "v_exp_f32 v31, v31\n"
    "v_mfma_f32_16x16x32_bf16 v[24:27], a[76:79], a[44:47], v[24:27]\n"
    "v_cvt_pk_bf16_f32 v78, v28, v29\n"
    "v_cvt_pk_bf16_f32 v79, v30, v31\n"
    "v_mfma_f32_16x16x32_bf16 v[28:31], a[76:79], a[60:63], 4.0\n"
    "v_exp_f32 v16, v16\n"
    "v_mfma_f32_16x16x32_bf16 v[28:31], a[72:75], a[56:59], v[28:31]\n"
    "ds_read_b128 a[124:127], v6 offset:23552\n"
    "v_mfma_f32_16x16x32_bf16 v[28:31], a[68:71], a[52:55], v[28:31]\n"
    "s_waitcnt lgkmcnt(4)\n"
    "v_exp_f32 v17, v17\n"
    "v_mfma_f32_16x16x32_bf16 v[28:31], a[64:67], a[48:51], v[28:31]\n"
    "global_load_lds_dwordx4 v8, s[62:63] offset:1024 sc1\n"
    "v_mfma_f32_16x16x32_bf16 v[128:131], v[48:51], a[96:99], 0\n"
    "v_exp_f32 v18, v18\n"
    "v_mfma_f32_16x16x32_bf16 v[128:131], v[52:55], a[100:103], v[128:131]\n"
    "ds_read_b128 a[64:67], v4 offset:8192\n"
    "v_mfma_f32_16x16x32_bf16 v[132:135], v[60:63], a[100:103], 0\n"
    "v_exp_f32 v19, v19\n"
    "v_mfma_f32_16x16x32_bf16 v[132:135], v[56:59], a[96:99], v[132:135]\n"
    "v_cvt_pk_bf16_f32 v80, v16, v17\n"
    "v_cvt_pk_bf16_f32 v81, v18, v19\n"
    "v_mfma_f32_16x16x32_bf16 v[136:139], v[64:67], a[96:99], 0\n"
    "v_exp_f32 v20, v20\n"
    "v_mfma_f32_16x16x32_bf16 v[136:139], v[68:71], a[100:103], v[136:139]\n"
    "ds_read_b128 a[68:71], v4 offset:9216\n"
    "v_mfma_f32_16x16x32_bf16 v[140:143], v[76:79], a[100:103], 0\n"
    "global_load_lds_dwordx4 v8, s[62:63] offset:3072 sc1\n"
    "v_mfma_f32_16x16x32_bf16 v[140:143], v[72:75], a[96:99], v[140:143]\n"
    "v_exp_f32 v21, v21\n"
    "v_mfma_f32_16x16x32_bf16 v[156:159], v[72:75], a[104:107], 0\n"
    "v_exp_f32 v22, v22\n"
    "v_mfma_f32_16x16x32_bf16 v[156:159], v[76:79], a[108:111], v[156:159]\n"
    "ds_read_b128 a[72:75], v4 offset:10240\n"
    "v_mfma_f32_16x16x32_bf16 v[152:155], v[68:71], a[108:111], 0\n"
    "v_exp_f32 v23, v23\n"
    "v_mfma_f32_16x16x32_bf16 v[152:155], v[64:67], a[104:107], v[152:155]\n"
    "v_cvt_pk_bf16_f32 v88, v20, v21\n"
    "v_cvt_pk_bf16_f32 v89, v22, v23\n"
    "v_mfma_f32_16x16x32_bf16 v[148:151], v[56:59], a[104:107], 0\n"
    "s_add_u32 m0, s69, 81920\n"
    "v_exp_f32 v24, v24\n"
    "v_mfma_f32_16x16x32_bf16 v[148:151], v[60:63], a[108:111], v[148:151]\n"
    "ds_read_b128 a[76:79], v4 offset:11264\n"
    "v_mfma_f32_16x16x32_bf16 v[144:147], v[52:55], a[108:111], 0\n"
    "v_exp_f32 v25, v25\n"
    "v_mfma_f32_16x16x32_bf16 v[144:147], v[48:51], a[104:107], v[144:147]\n"
    "global_load_dwordx4 v[36:39], v15, s[82:83]\n"
    "v_add_u32 v3, s60, v12\n"
    "v_smfmac_f32_16x16x64_bf16 a[128:131], a[144:147], v[48:55], v10\n"
    "s_waitcnt lgkmcnt(4)\n"
    "global_load_dwordx4 v[44:47], v15, s[84:85]\n"
    "v_mfma_f32_16x16x32_bf16 v[16:19], a[80:83], a[0:3], 4.0\n"
    "v_exp_f32 v26, v26\n"
    "v_mfma_f32_16x16x32_bf16 v[16:19], a[84:87], a[4:7], v[16:19]\n"
    "ds_read_b128 a[96:99], v6 offset:24576\n"
    "v_mfma_f32_16x16x32_bf16 v[16:19], a[88:91], a[8:11], v[16:19]\n"
    "v_exp_f32 v27, v27\n"
    "v_mfma_f32_16x16x32_bf16 v[16:19], a[92:95], a[12:15], v[16:19]\n"
    "v_cvt_pk_bf16_f32 v96, v24, v25\n"
    "v_cvt_pk_bf16_f32 v97, v26, v27\n"
    "v_mfma_f32_16x16x32_bf16 v[20:23], a[92:95], a[28:31], 4.0\n"
    "v_exp_f32 v28, v28\n"
    "v_mfma_f32_16x16x32_bf16 v[20:23], a[88:91], a[24:27], v[20:23]\n"
    "ds_read_b128 a[100:103], v6 offset:25600\n"
    "v_mfma_f32_16x16x32_bf16 v[20:23], a[84:87], a[20:23], v[20:23]\n"
    "global_load_lds_dwordx4 v9, s[62:63] offset:0 sc1\n"
    "v_mfma_f32_16x16x32_bf16 v[20:23], a[80:83], a[16:19], v[20:23]\n"
    "v_exp_f32 v29, v29\n"
    "v_mfma_f32_16x16x32_bf16 v[24:27], a[80:83], a[32:35], 4.0\n"
    "v_exp_f32 v30, v30\n"
    "v_mfma_f32_16x16x32_bf16 v[24:27], a[84:87], a[36:39], v[24:27]\n"
    "global_load_dwordx4 v[112:115], v12, s[80:81] offset:0 nt\n"
    "ds_read_b128 a[104:107], v6 offset:26624\n"
    "v_mfma_f32_16x16x32_bf16 v[24:27], a[88:91], a[40:43], v[24:27]\n"
    "v_exp_f32 v31, v31\n"
    "v_mfma_f32_16x16x32_bf16 v[24:27], a[92:95], a[44:47], v[24:27]\n"
    "v_cvt_pk_bf16_f32 v104, v28, v29\n"
    "v_cvt_pk_bf16_f32 v105, v30, v31\n"
    "v_mfma_f32_16x16x32_bf16 v[28:31], a[92:95], a[60:63], 4.0\n"
    "v_exp_f32 v16, v16\n"
    "v_mfma_f32_16x16x32_bf16 v[28:31], a[88:91], a[56:59], v[28:31]\n"
    "ds_read_b128 a[108:111], v6 offset:27648\n"
    "v_mfma_f32_16x16x32_bf16 v[28:31], a[84:87], a[52:55], v[28:31]\n"
    "v_exp_f32 v17, v17\n"
    "v_mfma_f32_16x16x32_bf16 v[28:31], a[80:83], a[48:51], v[28:31]\n"
    "global_load_dwordx4 v[116:119], v3, s[80:81] offset:0 nt\n"
    "v_mfma_f32_16x16x32_bf16 v[160:163], v[48:51], a[112:115], 0\n"
    "v_exp_f32 v18, v18\n"
    "v_mfma_f32_16x16x32_bf16 v[160:163], v[52:55], a[116:119], v[160:163]\n"
    "ds_read_b128 a[80:83], v4 offset:12288\n"
    "v_mfma_f32_16x16x32_bf16 v[164:167], v[60:63], a[116:119], 0\n"
    "v_exp_f32 v19, v19\n"
    "v_mfma_f32_16x16x32_bf16 v[164:167], v[56:59], a[112:115], v[164:167]\n"
    "v_cvt_pk_bf16_f32 v82, v16, v17\n"
    "v_cvt_pk_bf16_f32 v83, v18, v19\n"
    "v_mfma_f32_16x16x32_bf16 v[168:171], v[64:67], a[112:115], 0\n"
    "v_exp_f32 v20, v20\n"
    "v_mfma_f32_16x16x32_bf16 v[168:171], v[68:71], a[116:119], v[168:171]\n"
    "ds_read_b128 a[84:87], v4 offset:13312\n"
    "v_mfma_f32_16x16x32_bf16 v[172:175], v[76:79], a[116:119], 0\n"
    "global_load_lds_dwordx4 v9, s[62:63] offset:1024 sc1\n"
    "v_mfma_f32_16x16x32_bf16 v[172:175], v[72:75], a[112:115], v[172:175]\n"
    "v_exp_f32 v21, v21\n"
    "v_mfma_f32_16x16x32_bf16 v[188:191], v[72:75], a[120:123], 0\n"
    "v_exp_f32 v22, v22\n"
    "v_mfma_f32_16x16x32_bf16 v[188:191], v[76:79], a[124:127], v[188:191]\n"
    "ds_read_b128 a[88:91], v4 offset:14336\n"
    "v_mfma_f32_16x16x32_bf16 v[184:187], v[68:71], a[124:127], 0\n"
    "v_exp_f32 v23, v23\n"
    "v_mfma_f32_16x16x32_bf16 v[184:187], v[64:67], a[120:123], v[184:187]\n"
    "v_cvt_pk_bf16_f32 v90, v20, v21\n"
    "v_cvt_pk_bf16_f32 v91, v22, v23\n"
    "v_mfma_f32_16x16x32_bf16 v[180:183], v[56:59], a[120:123], 0\n"
    "v_exp_f32 v24, v24\n"
    "v_mfma_f32_16x16x32_bf16 v[180:183], v[60:63], a[124:127], v[180:183]\n"
    "ds_read_b128 a[92:95], v4 offset:15360\n"
    "v_mfma_f32_16x16x32_bf16 v[176:179], v[52:55], a[124:127], 0\n"
    "v_exp_f32 v25, v25\n"
    "v_mfma_f32_16x16x32_bf16 v[176:179], v[48:51], a[120:123], v[176:179]\n"
    "global_load_dwordx4 v[120:123], v12, s[80:81] offset:2048 nt\n"
    "v_smfmac_f32_16x16x64_bf16 a[132:135], a[144:147], v[56:63], v10\n"
    "s_waitcnt lgkmcnt(4)\n"
    "global_load_dwordx4 v[124:127], v3, s[80:81] offset:2048 nt\n"
    "v_mfma_f32_16x16x32_bf16 v[16:19], a[64:67], a[0:3], 4.0\n"
    "v_exp_f32 v26, v26\n"
    "v_mfma_f32_16x16x32_bf16 v[16:19], a[68:71], a[4:7], v[16:19]\n"
    "ds_read_b128 a[112:115], v6 offset:28672\n"
    "v_add_u32 v12, 0x40000, v12\n"
    "v_mfma_f32_16x16x32_bf16 v[16:19], a[72:75], a[8:11], v[16:19]\n"
    "v_exp_f32 v27, v27\n"
    "v_mfma_f32_16x16x32_bf16 v[16:19], a[76:79], a[12:15], v[16:19]\n"
    "v_cvt_pk_bf16_f32 v98, v24, v25\n"
    "v_cvt_pk_bf16_f32 v99, v26, v27\n"
    "v_mfma_f32_16x16x32_bf16 v[20:23], a[76:79], a[28:31], 4.0\n"
    "v_exp_f32 v28, v28\n"
    "v_mfma_f32_16x16x32_bf16 v[20:23], a[72:75], a[24:27], v[20:23]\n"
    "ds_read_b128 a[116:119], v6 offset:29696\n"
    "v_mfma_f32_16x16x32_bf16 v[20:23], a[68:71], a[20:23], v[20:23]\n"
    "global_load_lds_dwordx4 v9, s[62:63] offset:2048 sc1\n"
    "v_mfma_f32_16x16x32_bf16 v[20:23], a[64:67], a[16:19], v[20:23]\n"
    "v_exp_f32 v29, v29\n"
    "v_mfma_f32_16x16x32_bf16 v[24:27], a[64:67], a[32:35], 4.0\n"
    "v_exp_f32 v30, v30\n"
    "v_mfma_f32_16x16x32_bf16 v[24:27], a[68:71], a[36:39], v[24:27]\n"
    "ds_read_b128 a[120:123], v6 offset:30720\n"
    "v_add_u32 v15, 0x400, v15\n"
    "v_mfma_f32_16x16x32_bf16 v[24:27], a[72:75], a[40:43], v[24:27]\n"
    "v_exp_f32 v31, v31\n"
    "v_mfma_f32_16x16x32_bf16 v[24:27], a[76:79], a[44:47], v[24:27]\n"
    "v_cvt_pk_bf16_f32 v106, v28, v29\n"
    "v_cvt_pk_bf16_f32 v107, v30, v31\n"
    "v_mfma_f32_16x16x32_bf16 v[28:31], a[76:79], a[60:63], 4.0\n"
    "v_exp_f32 v16, v16\n"
    "v_mfma_f32_16x16x32_bf16 v[28:31], a[72:75], a[56:59], v[28:31]\n"
    "ds_read_b128 a[124:127], v6 offset:31744\n"
    "v_and_b32 v12, 0xfffff, v12\n"
    "v_mfma_f32_16x16x32_bf16 v[28:31], a[68:71], a[52:55], v[28:31]\n"
    "s_waitcnt vmcnt(13)\n"
    "v_exp_f32 v17, v17\n"
    "v_mfma_f32_16x16x32_bf16 v[28:31], a[64:67], a[48:51], v[28:31]\n"
    "s_barrier\n"
    "v_mfma_f32_16x16x32_bf16 v[192:195], v[48:51], a[96:99], 0\n"
    "v_exp_f32 v18, v18\n"
    "v_mfma_f32_16x16x32_bf16 v[192:195], v[52:55], a[100:103], v[192:195]\n"
    "ds_read_b128 a[64:67], v4 offset:32768\n"
    "v_and_b32 v15, 0xfff, v15\n"
    "v_mfma_f32_16x16x32_bf16 v[196:199], v[60:63], a[100:103], 0\n"
    "v_exp_f32 v19, v19\n"
    "v_mfma_f32_16x16x32_bf16 v[196:199], v[56:59], a[96:99], v[196:199]\n"
    "v_cvt_pk_bf16_f32 v84, v16, v17\n"
    "v_cvt_pk_bf16_f32 v85, v18, v19\n"
    "v_mfma_f32_16x16x32_bf16 v[200:203], v[64:67], a[96:99], 0\n"
    "v_exp_f32 v20, v20\n"
    "v_mfma_f32_16x16x32_bf16 v[200:203], v[68:71], a[100:103], v[200:203]\n"
    "ds_read_b128 a[68:71], v4 offset:33792\n"
    "v_mfma_f32_16x16x32_bf16 v[204:207], v[76:79], a[100:103], 0\n"
    "global_load_lds_dwordx4 v9, s[62:63] offset:3072 sc1\n"
    "v_mfma_f32_16x16x32_bf16 v[204:207], v[72:75], a[96:99], v[204:207]\n"
    "v_exp_f32 v21, v21\n"
    "v_mfma_f32_16x16x32_bf16 v[220:223], v[72:75], a[104:107], 0\n"
    "v_exp_f32 v22, v22\n"
    "v_mfma_f32_16x16x32_bf16 v[220:223], v[76:79], a[108:111], v[220:223]\n"
    "ds_read_b128 a[72:75], v4 offset:34816\n"
    "v_mfma_f32_16x16x32_bf16 v[216:219], v[68:71], a[108:111], 0\n"
    "s_add_u32 s62, s62, 0x8000\n"
    "v_exp_f32 v23, v23\n"
    "v_mfma_f32_16x16x32_bf16 v[216:219], v[64:67], a[104:107], v[216:219]\n"
    "v_cvt_pk_bf16_f32 v92, v20, v21\n"
    "v_cvt_pk_bf16_f32 v93, v22, v23\n"
    "v_mfma_f32_16x16x32_bf16 v[212:215], v[56:59], a[104:107], 0\n"
    "s_add_u32 m0, s69, 98304\n"
    "v_exp_f32 v24, v24\n"
    "v_mfma_f32_16x16x32_bf16 v[212:215], v[60:63], a[108:111], v[212:215]\n"
    "ds_read_b128 a[76:79], v4 offset:35840\n"
    "v_mfma_f32_16x16x32_bf16 v[208:211], v[52:55], a[108:111], 0\n"
    "v_exp_f32 v25, v25\n"
    "v_mfma_f32_16x16x32_bf16 v[208:211], v[48:51], a[104:107], v[208:211]\n"
    "v_smfmac_f32_16x16x64_bf16 a[136:139], a[144:147], v[64:71], v10\n"
    "s_waitcnt lgkmcnt(4)\n"
    "v_mfma_f32_16x16x32_bf16 v[16:19], a[80:83], a[0:3], 4.0\n"
    "v_exp_f32 v26, v26\n"
    "v_mfma_f32_16x16x32_bf16 v[16:19], a[84:87], a[4:7], v[16:19]\n"
    "ds_read_b128 a[96:99], v6 offset:49152\n"
    "v_mfma_f32_16x16x32_bf16 v[16:19], a[88:91], a[8:11], v[16:19]\n"
    "v_exp_f32 v27, v27\n"
    "v_mfma_f32_16x16x32_bf16 v[16:19], a[92:95], a[12:15], v[16:19]\n"
    "v_cvt_pk_bf16_f32 v100, v24, v25\n"
    "v_cvt_pk_bf16_f32 v101, v26, v27\n"
    "v_mfma_f32_16x16x32_bf16 v[20:23], a[92:95], a[28:31], 4.0\n"
    "v_exp_f32 v28, v28\n"
    "v_mfma_f32_16x16x32_bf16 v[20:23], a[88:91], a[24:27], v[20:23]\n"
    "ds_read_b128 a[100:103], v6 offset:50176\n"
    "v_mfma_f32_16x16x32_bf16 v[20:23], a[84:87], a[20:23], v[20:23]\n"
    "global_load_lds_dwordx4 v8, s[62:63] offset:0 sc1\n"
    "v_mfma_f32_16x16x32_bf16 v[20:23], a[80:83], a[16:19], v[20:23]\n"
    "v_exp_f32 v29, v29\n"
    "v_mfma_f32_16x16x32_bf16 v[24:27], a[80:83], a[32:35], 4.0\n"
    "v_exp_f32 v30, v30\n"
    "v_mfma_f32_16x16x32_bf16 v[24:27], a[84:87], a[36:39], v[24:27]\n"
    "ds_read_b128 a[104:107], v6 offset:51200\n"
    "v_mfma_f32_16x16x32_bf16 v[24:27], a[88:91], a[40:43], v[24:27]\n"
    "v_exp_f32 v31, v31\n"
    "v_mfma_f32_16x16x32_bf16 v[24:27], a[92:95], a[44:47], v[24:27]\n"
    "v_cvt_pk_bf16_f32 v108, v28, v29\n"
    "v_cvt_pk_bf16_f32 v109, v30, v31\n"
    "v_mfma_f32_16x16x32_bf16 v[28:31], a[92:95], a[60:63], 4.0\n"
    "v_exp_f32 v16, v16\n"
    "v_mfma_f32_16x16x32_bf16 v[28:31], a[88:91], a[56:59], v[28:31]\n"
    "ds_read_b128 a[108:111], v6 offset:52224\n"
    "v_mfma_f32_16x16x32_bf16 v[28:31], a[84:87], a[52:55], v[28:31]\n"
    "v_exp_f32 v17, v17\n"
    "v_mfma_f32_16x16x32_bf16 v[28:31], a[80:83], a[48:51], v[28:31]\n"
    "v_mfma_f32_16x16x32_bf16 v[224:227], v[48:51], a[112:115], 0\n"
    "v_exp_f32 v18, v18\n"
    "v_mfma_f32_16x16x32_bf16 v[224:227], v[52:55], a[116:119], v[224:227]\n"
    "ds_read_b128 a[80:83], v4 offset:36864\n"
    "v_mfma_f32_16x16x32_bf16 v[228:231], v[60:63], a[116:119], 0\n"
    "v_exp_f32 v19, v19\n"
    "v_mfma_f32_16x16x32_bf16 v[228:231], v[56:59], a[112:115], v[228:231]\n"
    "v_cvt_pk_bf16_f32 v86, v16, v17\n"
    "v_cvt_pk_bf16_f32 v87, v18, v19\n"
    "v_mfma_f32_16x16x32_bf16 v[232:235], v[64:67], a[112:115], 0\n"
    "v_exp_f32 v20, v20\n"
    "v_mfma_f32_16x16x32_bf16 v[232:235], v[68:71], a[116:119], v[232:235]\n"
    "ds_read_b128 a[84:87], v4 offset:37888\n"
    "v_mfma_f32_16x16x32_bf16 v[236:239], v[76:79], a[116:119], 0\n"
    "global_load_lds_dwordx4 v8, s[62:63] offset:1024 sc1\n"
    "v_mfma_f32_16x16x32_bf16 v[236:239], v[72:75], a[112:115], v[236:239]\n"
    "v_exp_f32 v21, v21\n"
    "v_mfma_f32_16x16x32_bf16 v[252:255], v[72:75], a[120:123], 0\n"
    "v_exp_f32 v22, v22\n"
    "v_mfma_f32_16x16x32_bf16 v[252:255], v[76:79], a[124:127], v[252:255]\n"
    "ds_read_b128 a[88:91], v4 offset:38912\n"
    "v_mfma_f32_16x16x32_bf16 v[248:251], v[68:71], a[124:127], 0\n"
    "v_exp_f32 v23, v23\n"
    "v_mfma_f32_16x16x32_bf16 v[248:251], v[64:67], a[120:123], v[248:251]\n"
    "v_cvt_pk_bf16_f32 v94, v20, v21\n"
    "v_cvt_pk_bf16_f32 v95, v22, v23\n"
    "v_mfma_f32_16x16x32_bf16 v[244:247], v[56:59], a[120:123], 0\n"
    "v_exp_f32 v24, v24\n"
    "v_mfma_f32_16x16x32_bf16 v[244:247], v[60:63], a[124:127], v[244:247]\n"
    "ds_read_b128 a[92:95], v4 offset:39936\n"
    "v_mfma_f32_16x16x32_bf16 v[240:243], v[52:55], a[124:127], 0\n"
    "s_waitcnt vmcnt(4)\n"
    "v_exp_f32 v25, v25\n"
    "v_mfma_f32_16x16x32_bf16 v[240:243], v[48:51], a[120:123], v[240:243]\n"
    "v_smfmac_f32_16x16x64_bf16 a[140:143], a[144:147], v[72:79], v10\n"
    "s_waitcnt lgkmcnt(4)\n"
    "s_cmp_lt_u32 %3, 2\n"
    "s_cbranch_scc1 L%=_uv\n"
    "L%=_uk:\n"
    "v_mfma_f32_16x16x32_bf16 v[16:19], a[64:67], a[0:3], 4.0\n"
    "v_exp_f32 v26, v26\n"
    "v_mfma_f32_16x16x32_bf16 v[16:19], a[68:71], a[4:7], v[16:19]\n"
    "ds_read_b128 a[112:115], v6 offset:53248\n"
    "v_lshlrev_b32 v32, 16, v36\n"
    "v_mfma_f32_16x16x32_bf16 v[16:19], a[72:75], a[8:11], v[16:19]\n"
    "v_exp_f32 v27, v27\n"
    "v_mfma_f32_16x16x32_bf16 v[16:19], a[76:79], a[12:15], v[16:19]\n"
    "v_cvt_pk_bf16_f32 v102, v24, v25\n"
    "v_cvt_pk_bf16_f32 v103, v26, v27\n"
    "v_mfma_f32_16x16x32_bf16 v[20:23], a[76:79], a[28:31], 4.0\n"
    "v_exp_f32 v28, v28\n"
    "v_mfma_f32_16x16x32_bf16 v[20:23], a[72:75], a[24:27], v[20:23]\n"
    "ds_read_b128 a[116:119], v6 offset:54272\n"
    "v_mfma_f32_16x16x32_bf16 v[20:23], a[68:71], a[20:23], v[20:23]\n"
    "global_load_lds_dwordx4 v8, s[62:63] offset:2048 sc1\n"
    "v_and_b32 v33, 0xffff0000, v36\n"
    "v_mfma_f32_16x16x32_bf16 v[20:23], a[64:67], a[16:19], v[20:23]\n"
    "v_exp_f32 v29, v29\n"
    "v_mfma_f32_16x16x32_bf16 v[24:27], a[64:67], a[32:35], 4.0\n"
    "v_exp_f32 v30, v30\n"
    "v_mfma_f32_16x16x32_bf16 v[24:27], a[68:71], a[36:39], v[24:27]\n"
    "ds_read_b128 a[120:123], v6 offset:55296\n"
    "v_lshlrev_b32 v34, 16, v37\n"
    "v_mfma_f32_16x16x32_bf16 v[24:27], a[72:75], a[40:43], v[24:27]\n"
    "v_exp_f32 v31, v31\n"
    "v_mfma_f32_16x16x32_bf16 v[24:27], a[76:79], a[44:47], v[24:27]\n"
    "v_cvt_pk_bf16_f32 v110, v28, v29\n"
    "v_cvt_pk_bf16_f32 v111, v30, v31\n"
    "v_mfma_f32_16x16x32_bf16 v[28:31], a[76:79], a[60:63], 4.0\n"
    "v_exp_f32 v16, v16\n"
    "v_mfma_f32_16x16x32_bf16 v[28:31], a[72:75], a[56:59], v[28:31]\n"
    "ds_read_b128 a[124:127], v6 offset:56320\n"
    "v_and_b32 v35, 0xffff0000, v37\n"
    "v_mfma_f32_16x16x32_bf16 v[28:31], a[68:71], a[52:55], v[28:31]\n"
    "v_exp_f32 v17, v17\n"
    "v_mfma_f32_16x16x32_bf16 v[28:31], a[64:67], a[48:51], v[28:31]\n"
    "v_lshlrev_b32 v36, 16, v38\n"
    "v_and_b32 v37, 0xffff0000, v38\n"
    "v_mfma_f32_16x16x32_bf16 v[128:131], v[80:83], a[96:99], v[128:131]\n"
    "v_exp_f32 v18, v18\n"
    "v_mfma_f32_16x16x32_bf16 v[128:131], v[84:87], a[100:103], v[128:131]\n"
    "ds_read_b128 a[64:67], v4 offset:40960\n"
    "v_lshlrev_b32 v38, 16, v39\n"
    "v_mfma_f32_16x16x32_bf16 v[132:135], v[92:95], a[100:103], v[132:135]\n"
    "v_exp_f32 v19, v19\n"
    "v_mfma_f32_16x16x32_bf16 v[132:135], v[88:91], a[96:99], v[132:135]\n"
    "v_cvt_pk_bf16_f32 v48, v16, v17\n"
    "v_cvt_pk_bf16_f32 v49, v18, v19\n"
    "v_mfma_f32_16x16x32_bf16 v[136:139], v[96:99], a[96:99], v[136:139]\n"
    "v_exp_f32 v20, v20\n"
    "v_mfma_f32_16x16x32_bf16 v[136:139], v[100:103], a[100:103], v[136:139]\n"
    "ds_read_b128 a[68:71], v4 offset:41984\n"
    "v_mfma_f32_16x16x32_bf16 v[140:143], v[108:111], a[100:103], v[140:143]\n"
    "global_load_lds_dwordx4 v8, s[62:63] offset:3072 sc1\n"
    "v_and_b32 v39, 0xffff0000, v39\n"
    "v_mfma_f32_16x16x32_bf16 v[140:143], v[104:107], a[96:99], v[140:143]\n"
    "v_exp_f32 v21, v21\n"
    "v_mfma_f32_16x16x32_bf16 v[156:159], v[104:107], a[104:107], v[156:159]\n"
    "v_exp_f32 v22, v22\n"
    "v_mfma_f32_16x16x32_bf16 v[156:159], v[108:111], a[108:111], v[156:159]\n"
    "ds_read_b128 a[72:75], v4 offset:43008\n"
    "v_lshlrev_b32 v40, 16, v44\n"
    "v_mfma_f32_16x16x32_bf16 v[152:155], v[100:103], a[108:111], v[152:155]\n"
    "v_exp_f32 v23, v23\n"
    "v_mfma_f32_16x16x32_bf16 v[152:155], v[96:99], a[104:107], v[152:155]\n"
    "v_cvt_pk_bf16_f32 v56, v20, v21\n"
    "v_cvt_pk_bf16_f32 v57, v22, v23\n"
    "v_mfma_f32_16x16x32_bf16 v[148:151], v[88:91], a[104:107], v[148:151]\n"
    "s_add_u32 m0, s69, 114688\n"
    "v_exp_f32 v24, v24\n"
    "v_mfma_f32_16x16x32_bf16 v[148:151], v[92:95], a[108:111], v[148:151]\n"
    "ds_read_b128 a[76:79], v4 offset:44032\n"
    "v_and_b32 v41, 0xffff0000, v44\n"
    "v_mfma_f32_16x16x32_bf16 v[144:147], v[84:87], a[108:111], v[144:147]\n"
    "v_exp_f32 v25, v25\n"
    "v_mfma_f32_16x16x32_bf16 v[144:147], v[80:83], a[104:107], v[144:147]\n"
    "v_lshlrev_b32 v42, 16, v45\n"
    "v_and_b32 v43, 0xffff0000, v45\n"
    "v_smfmac_f32_16x16x64_bf16 a[128:131], a[144:147], v[80:87], v10\n"
    "s_waitcnt lgkmcnt(4)\n"
    "v_lshlrev_b32 v44, 16, v46\n"
    "v_and_b32 v45, 0xffff0000, v46\n"
    "v_mfma_f32_16x16x32_bf16 v[16:19], a[80:83], a[0:3], 4.0\n"
    "v_exp_f32 v26, v26\n"
    "v_mfma_f32_16x16x32_bf16 v[16:19], a[84:87], a[4:7], v[16:19]\n"
    "ds_read_b128 a[96:99], v6 offset:57344\n"
    "v_lshlrev_b32 v46, 16, v47\n"
    "v_mfma_f32_16x16x32_bf16 v[16:19], a[88:91], a[8:11], v[16:19]\n"
    "v_exp_f32 v27, v27\n"
    "v_mfma_f32_16x16x32_bf16 v[16:19], a[92:95], a[12:15], v[16:19]\n"
    "v_cvt_pk_bf16_f32 v64, v24, v25\n"
    "v_cvt_pk_bf16_f32 v65, v26, v27\n"
    "v_mfma_f32_16x16x32_bf16 v[20:23], a[92:95], a[28:31], 4.0\n"
    "v_exp_f32 v28, v28\n"
    "v_mfma_f32_16x16x32_bf16 v[20:23], a[88:91], a[24:27], v[20:23]\n"
    "ds_read_b128 a[100:103], v6 offset:58368\n"
    "v_mfma_f32_16x16x32_bf16 v[20:23], a[84:87], a[20:23], v[20:23]\n"
    "global_load_lds_dwordx4 v9, s[62:63] offset:0 sc1\n"
    "v_and_b32 v47, 0xffff0000, v47\n"
    "v_mfma_f32_16x16x32_bf16 v[20:23], a[80:83], a[16:19], v[20:23]\n"
    "v_exp_f32 v29, v29\n"
    "v_mfma_f32_16x16x32_bf16 v[24:27], a[80:83], a[32:35], 4.0\n"
    "v_exp_f32 v30, v30\n"
    "v_mfma_f32_16x16x32_bf16 v[24:27], a[84:87], a[36:39], v[24:27]\n"
    "ds_read_b128 a[104:107], v6 offset:59392\n"
    "v_mul_f32 v40, s36, v40\n"
    "v_mfma_f32_16x16x32_bf16 v[24:27], a[88:91], a[40:43], v[24:27]\n"
    "v_exp_f32 v31, v31\n"
    "v_mfma_f32_16x16x32_bf16 v[24:27], a[92:95], a[44:47], v[24:27]\n"
    "v_cvt_pk_bf16_f32 v72, v28, v29\n"
    "v_cvt_pk_bf16_f32 v73, v30, v31\n"
    "v_mfma_f32_16x16x32_bf16 v[28:31], a[92:95], a[60:63], 4.0\n"
    "v_exp_f32 v16, v16\n"
    "v_mfma_f32_16x16x32_bf16 v[28:31], a[88:91], a[56:59], v[28:31]\n"
    "ds_read_b128 a[108:111], v6 offset:60416\n"
    "v_mul_f32 v41, s36, v41\n"
    "v_mfma_f32_16x16x32_bf16 v[28:31], a[84:87], a[52:55], v[28:31]\n"
    "v_exp_f32 v17, v17\n"
    "v_mfma_f32_16x16x32_bf16 v[28:31], a[80:83], a[48:51], v[28:31]\n"
    "v_mul_f32 v42, s36, v42\n"
    "v_mul_f32 v43, s36, v43\n"
    "v_mfma_f32_16x16x32_bf16 v[160:163], v[80:83], a[112:115], v[160:163]\n"
    "v_exp_f32 v18, v18\n"
    "v_mfma_f32_16x16x32_bf16 v[160:163], v[84:87], a[116:119], v[160:163]\n"
    "ds_read_b128 a[80:83], v4 offset:45056\n"
    "v_mul_f32 v44, s36, v44\n"
    "v_mfma_f32_16x16x32_bf16 v[164:167], v[92:95], a[116:119], v[164:167]\n"
    "v_exp_f32 v19, v19\n"
    "v_mfma_f32_16x16x32_bf16 v[164:167], v[88:91], a[112:115], v[164:167]\n"
    "v_cvt_pk_bf16_f32 v50, v16, v17\n"
    "v_cvt_pk_bf16_f32 v51, v18, v19\n"
    "v_mfma_f32_16x16x32_bf16 v[168:171], v[96:99], a[112:115], v[168:171]\n"
    "v_exp_f32 v20, v20\n"
    "v_mfma_f32_16x16x32_bf16 v[168:171], v[100:103], a[116:119], v[168:171]\n"
    "ds_read_b128 a[84:87], v4 offset:46080\n"
    "v_mfma_f32_16x16x32_bf16 v[172:175], v[108:111], a[116:119], v[172:175]\n"
    "global_load_lds_dwordx4 v9, s[62:63] offset:1024 sc1\n"
    "v_mul_f32 v45, s36, v45\n"
    "v_mfma_f32_16x16x32_bf16 v[172:175], v[104:107], a[112:115], v[172:175]\n"
    "v_exp_f32 v21, v21\n"
    "v_mfma_f32_16x16x32_bf16 v[188:191], v[104:107], a[120:123], v[188:191]\n"
    "v_exp_f32 v22, v22\n"
    "v_mfma_f32_16x16x32_bf16 v[188:191], v[108:111], a[124:127], v[188:191]\n"
    "ds_read_b128 a[88:91], v4 offset:47104\n"
    "v_mul_f32 v46, s36, v46\n"
    "v_mfma_f32_16x16x32_bf16 v[184:187], v[100:103], a[124:127], v[184:187]\n"
    "v_exp_f32 v23, v23\n"
    "v_mfma_f32_16x16x32_bf16 v[184:187], v[96:99], a[120:123], v[184:187]\n"
    "v_cvt_pk_bf16_f32 v58, v20, v21\n"
    "v_cvt_pk_bf16_f32 v59, v22, v23\n"
    "v_mfma_f32_16x16x32_bf16 v[180:183], v[88:91], a[120:123], v[180:183]\n"
    "v_exp_f32 v24, v24\n"
    "v_mfma_f32_16x16x32_bf16 v[180:183], v[92:95], a[124:127], v[180:183]\n"
    "ds_read_b128 a[92:95], v4 offset:48128\n"
    "v_mul_f32 v47, s36, v47\n"
    "v_mfma_f32_16x16x32_bf16 v[176:179], v[84:87], a[124:127], v[176:179]\n"
    "v_exp_f32 v25, v25\n"
    "v_mfma_f32_16x16x32_bf16 v[176:179], v[80:83], a[120:123], v[176:179]\n"
    "v_mul_f32 v32, v32, v40\n"
    "v_mul_f32 v33, v33, v41\n"
    "v_smfmac_f32_16x16x64_bf16 a[132:135], a[144:147], v[88:95], v10\n"
    "s_waitcnt lgkmcnt(4)\n"
    "v_mul_f32 v34, v34, v42\n"
    "v_mul_f32 v35, v35, v43\n"
    "v_mfma_f32_16x16x32_bf16 v[16:19], a[64:67], a[0:3], 4.0\n"
    "v_exp_f32 v26, v26\n"
    "v_mfma_f32_16x16x32_bf16 v[16:19], a[68:71], a[4:7], v[16:19]\n"
    "ds_read_b128 a[112:115], v6 offset:61440\n"
    "v_mul_f32 v36, v36, v44\n"
    "v_mfma_f32_16x16x32_bf16 v[16:19], a[72:75], a[8:11], v[16:19]\n"
    "v_exp_f32 v27, v27\n"
    "v_mfma_f32_16x16x32_bf16 v[16:19], a[76:79], a[12:15], v[16:19]\n"
    "v_cvt_pk_bf16_f32 v66, v24, v25\n"
    "v_cvt_pk_bf16_f32 v67, v26, v27\n"
    "v_mfma_f32_16x16x32_bf16 v[20:23], a[76:79], a[28:31], 4.0\n"
    "v_exp_f32 v28, v28\n"
    "v_mfma_f32_16x16x32_bf16 v[20:23], a[72:75], a[24:27], v[20:23]\n"
    "ds_read_b128 a[116:119], v6 offset:62464\n"
    "v_mfma_f32_16x16x32_bf16 v[20:23], a[68:71], a[20:23], v[20:23]\n"
    "global_load_lds_dwordx4 v9, s[62:63] offset:2048 sc1\n"
    "v_mul_f32 v37, v37, v45\n"
    "v_mfma_f32_16x16x32_bf16 v[20:23], a[64:67], a[16:19], v[20:23]\n"
    "v_exp_f32 v29, v29\n"
    "v_mfma_f32_16x16x32_bf16 v[24:27], a[64:67], a[32:35], 4.0\n"
    "v_exp_f32 v30, v30\n"
    "v_mfma_f32_16x16x32_bf16 v[24:27], a[68:71], a[36:39], v[24:27]\n"
    "ds_read_b128 a[120:123], v6 offset:63488\n"
    "v_mul_f32 v38, v38, v46\n"
    "v_mfma_f32_16x16x32_bf16 v[24:27], a[72:75], a[40:43], v[24:27]\n"
    "v_exp_f32 v31, v31\n"
    "v_mfma_f32_16x16x32_bf16 v[24:27], a[76:79], a[44:47], v[24:27]\n"
    "v_cvt_pk_bf16_f32 v74, v28, v29\n"
    "v_cvt_pk_bf16_f32 v75, v30, v31\n"
    "v_mfma_f32_16x16x32_bf16 v[28:31], a[76:79], a[60:63], 4.0\n"
    "v_exp_f32 v16, v16\n"
    "v_mfma_f32_16x16x32_bf16 v[28:31], a[72:75], a[56:59], v[28:31]\n"
    "ds_read_b128 a[124:127], v6 offset:64512\n"
    "v_mul_f32 v39, v39, v47\n"
    "v_mfma_f32_16x16x32_bf16 v[28:31], a[68:71], a[52:55], v[28:31]\n"
    "s_waitcnt vmcnt(7)\n"
    "v_exp_f32 v17, v17\n"
    "v_mfma_f32_16x16x32_bf16 v[28:31], a[64:67], a[48:51], v[28:31]\n"
    "s_barrier\n"
    "v_cvt_f32_i32 v112, v112\n"
    "v_cvt_f32_i32 v113, v113\n"
    "v_mfma_f32_16x16x32_bf16 v[192:195], v[80:83], a[96:99], v[192:195]\n"
    "v_exp_f32 v18, v18\n"
    "v_mfma_f32_16x16x32_bf16 v[192:195], v[84:87], a[100:103], v[192:195]\n"
    "ds_read_b128 a[64:67], v5 offset:0\n"
    "v_cvt_f32_i32 v114, v114\n"
    "v_mfma_f32_16x16x32_bf16 v[196:199], v[92:95], a[100:103], v[196:199]\n"
    "v_exp_f32 v19, v19\n"
    "v_mfma_f32_16x16x32_bf16 v[196:199], v[88:91], a[96:99], v[196:199]\n"
    "v_cvt_pk_bf16_f32 v52, v16, v17\n"
    "v_cvt_pk_bf16_f32 v53, v18, v19\n"
    "v_mfma_f32_16x16x32_bf16 v[200:203], v[96:99], a[96:99], v[200:203]\n"
    "v_exp_f32 v20, v20\n"
    "v_mfma_f32_16x16x32_bf16 v[200:203], v[100:103], a[100:103], v[200:203]\n"
    "ds_read_b128 a[68:71], v5 offset:1024\n"
    "v_mfma_f32_16x16x32_bf16 v[204:207], v[108:111], a[100:103], v[204:207]\n"
    "global_load_lds_dwordx4 v9, s[62:63] offset:3072 sc1\n"
    "v_cvt_f32_i32 v115, v115\n"
    "v_mfma_f32_16x16x32_bf16 v[204:207], v[104:107], a[96:99], v[204:207]\n"
    "v_exp_f32 v21, v21\n"
    "v_mfma_f32_16x16x32_bf16 v[220:223], v[104:107], a[104:107], v[220:223]\n"
    "v_exp_f32 v22, v22\n"
    "v_mfma_f32_16x16x32_bf16 v[220:223], v[108:111], a[108:111], v[220:223]\n"
    "L%=_w1:\n"
    "s_cbranch_execz L%=_w5\n"
    "ds_read_b128 a[72:75], v5 offset:2048\n"
    "v_cvt_f32_i32 v116, v116\n"
    "v_mfma_f32_16x16x32_bf16 v[216:219], v[100:103], a[108:111], v[216:219]\n"
    "s_add_u32 s62, s62, 0x8000\n"
    "v_exp_f32 v23, v23\n"
    "v_mfma_f32_16x16x32_bf16 v[216:219], v[96:99], a[104:107], v[216:219]\n"
    "v_cvt_pk_bf16_f32 v60, v20, v21\n"
    "v_cvt_pk_bf16_f32 v61, v22, v23\n"
    "v_mfma_f32_16x16x32_bf16 v[212:215], v[88:91], a[104:107], v[212:215]\n"
    "s_add_u32 m0, s69, 0\n"
    "v_exp_f32 v24, v24\n"
    "v_mfma_f32_16x16x32_bf16 v[212:215], v[92:95], a[108:111], v[212:215]\n"
    "ds_read_b128 a[76:79], v5 offset:3072\n"
    "v_cvt_f32_i32 v117, v117\n"
    "v_mfma_f32_16x16x32_bf16 v[208:211], v[84:87], a[108:111], v[208:211]\n"
    "v_exp_f32 v25, v25\n"
    "v_mfma_f32_16x16x32_bf16 v[208:211], v[80:83], a[104:107], v[208:211]\n"
    "v_cvt_f32_i32 v118, v118\n"
    "v_cvt_f32_i32 v119, v119\n"
    "v_smfmac_f32_16x16x64_bf16 a[136:139], a[144:147], v[96:103], v10\n"
    "s_waitcnt lgkmcnt(4)\n"
    "v_fma_f32 v112, v112, v40, -v32\n"
    "v_fma_f32 v113, v113, v41, -v33\n"
    "v_mfma_f32_16x16x32_bf16 v[16:19], a[80:83], a[0:3], 4.0\n"
    "v_exp_f32 v26, v26\n"
    "v_mfma_f32_16x16x32_bf16 v[16:19], a[84:87], a[4:7], v[16:19]\n"
    "ds_read_b128 a[96:99], v7 offset:16384\n"
    "v_fma_f32 v114, v114, v42, -v34\n"
    "v_mfma_f32_16x16x32_bf16 v[16:19], a[88:91], a[8:11], v[16:19]\n"
    "v_exp_f32 v27, v27\n"
    "v_mfma_f32_16x16x32_bf16 v[16:19], a[92:95], a[12:15], v[16:19]\n"
    "v_cvt_pk_bf16_f32 v68, v24, v25\n"
    "v_cvt_pk_bf16_f32 v69, v26, v27\n"
    "v_mfma_f32_16x16x32_bf16 v[20:23], a[92:95], a[28:31], 4.0\n"
    "v_exp_f32 v28, v28\n"
    "v_mfma_f32_16x16x32_bf16 v[20:23], a[88:91], a[24:27], v[20:23]\n"
    "ds_read_b128 a[100:103], v7 offset:17408\n"
    "v_mfma_f32_16x16x32_bf16 v[20:23], a[84:87], a[20:23], v[20:23]\n"
    "global_load_lds_dwordx4 v8, s[62:63] offset:0 sc1\n"
    "v_fma_f32 v115, v115, v43, -v35\n"
    "v_mfma_f32_16x16x32_bf16 v[20:23], a[80:83], a[16:19], v[20:23]\n"
    "v_exp_f32 v29, v29\n"
    "v_mfma_f32_16x16x32_bf16 v[24:27], a[80:83], a[32:35], 4.0\n"
    "v_exp_f32 v30, v30\n"
    "v_mfma_f32_16x16x32_bf16 v[24:27], a[84:87], a[36:39], v[24:27]\n"
    "ds_read_b128 a[104:107], v7 offset:18432\n"
    "v_fma_f32 v116, v116, v44, -v36\n"
    "v_mfma_f32_16x16x32_bf16 v[24:27], a[88:91], a[40:43], v[24:27]\n"
    "v_exp_f32 v31, v31\n"
    "v_mfma_f32_16x16x32_bf16 v[24:27], a[92:95], a[44:47], v[24:27]\n"
    "v_cvt_pk_bf16_f32 v76, v28, v29\n"
    "v_cvt_pk_bf16_f32 v77, v30, v31\n"
    "v_mfma_f32_16x16x32_bf16 v[28:31], a[92:95], a[60:63], 4.0\n"
    "v_exp_f32 v16, v16\n"
    "v_mfma_f32_16x16x32_bf16 v[28:31], a[88:91], a[56:59], v[28:31]\n"
    "ds_read_b128 a[108:111], v7 offset:19456\n"
    "v_fma_f32 v117, v117, v45, -v37\n"
    "v_mfma_f32_16x16x32_bf16 v[28:31], a[84:87], a[52:55], v[28:31]\n"
    "v_exp_f32 v17, v17\n"
    "v_mfma_f32_16x16x32_bf16 v[28:31], a[80:83], a[48:51], v[28:31]\n"
    "v_fma_f32 v118, v118, v46, -v38\n"
    "v_fma_f32 v119, v119, v47, -v39\n"
    "v_mfma_f32_16x16x32_bf16 v[224:227], v[80:83], a[112:115], v[224:227]\n"
    "v_exp_f32 v18, v18\n"
    "v_mfma_f32_16x16x32_bf16 v[224:227], v[84:87], a[116:119], v[224:227]\n"
    "ds_read_b128 a[80:83], v5 offset:4096\n"
    "v_cvt_pk_bf16_f32 v112, v112, v113\n"
    "v_mfma_f32_16x16x32_bf16 v[228:231], v[92:95], a[116:119], v[228:231]\n"
    "v_exp_f32 v19, v19\n"
    "v_mfma_f32_16x16x32_bf16 v[228:231], v[88:91], a[112:115], v[228:231]\n"
    "v_cvt_pk_bf16_f32 v54, v16, v17\n"
    "v_cvt_pk_bf16_f32 v55, v18, v19\n"
    "v_mfma_f32_16x16x32_bf16 v[232:235], v[96:99], a[112:115], v[232:235]\n"
    "v_exp_f32 v20, v20\n"
    "v_mfma_f32_16x16x32_bf16 v[232:235], v[100:103], a[116:119], v[232:235]\n"
    "ds_read_b128 a[84:87], v5 offset:5120\n"
    "v_mfma_f32_16x16x32_bf16 v[236:239], v[108:111], a[116:119], v[236:239]\n"
    "global_load_lds_dwordx4 v8, s[62:63] offset:1024 sc1\n"
    "v_cvt_pk_bf16_f32 v113, v114, v115\n"
    "v_mfma_f32_16x16x32_bf16 v[236:239], v[104:107], a[112:115], v[236:239]\n"
    "v_exp_f32 v21, v21\n"
    "v_mfma_f32_16x16x32_bf16 v[252:255], v[104:107], a[120:123], v[252:255]\n"
    "v_exp_f32 v22, v22\n"
    "v_mfma_f32_16x16x32_bf16 v[252:255], v[108:111], a[124:127], v[252:255]\n"
    "ds_read_b128 a[88:91], v5 offset:6144\n"
    "v_cvt_pk_bf16_f32 v114, v116, v117\n"
    "v_mfma_f32_16x16x32_bf16 v[248:251], v[100:103], a[124:127], v[248:251]\n"
    "v_exp_f32 v23, v23\n"
    "v_mfma_f32_16x16x32_bf16 v[248:251], v[96:99], a[120:123], v[248:251]\n"
    "v_cvt_pk_bf16_f32 v62, v20, v21\n"
    "v_cvt_pk_bf16_f32 v63, v22, v23\n"
    "v_mfma_f32_16x16x32_bf16 v[244:247], v[88:91], a[120:123], v[244:247]\n"
    "v_exp_f32 v24, v24\n"
    "v_mfma_f32_16x16x32_bf16 v[244:247], v[92:95], a[124:127], v[244:247]\n"
    "ds_read_b128 a[92:95], v5 offset:7168\n"
    "v_cvt_pk_bf16_f32 v115, v118, v119\n"
    "v_mfma_f32_16x16x32_bf16 v[240:243], v[84:87], a[124:127], v[240:243]\n"
    "v_exp_f32 v25, v25\n"
    "v_mfma_f32_16x16x32_bf16 v[240:243], v[80:83], a[120:123], v[240:243]\n"
    "global_store_dwordx4 v14, v[112:115], s[64:65] offset:-4096 sc1\n"
    "v_cvt_f32_i32 v120, v120\n"
    "v_smfmac_f32_16x16x64_bf16 a[140:143], a[144:147], v[104:111], v10\n"
    "s_waitcnt lgkmcnt(4)\n"
    "v_cvt_f32_i32 v121, v121\n"
    "v_cvt_f32_i32 v122, v122\n"
    "v_mfma_f32_16x16x32_bf16 v[16:19], a[64:67], a[0:3], 4.0\n"
    "v_exp_f32 v26, v26\n"
    "v_mfma_f32_16x16x32_bf16 v[16:19], a[68:71], a[4:7], v[16:19]\n"
    "ds_read_b128 a[112:115], v7 offset:20480\n"
    "v_cvt_f32_i32 v123, v123\n"
    "v_mfma_f32_16x16x32_bf16 v[16:19], a[72:75], a[8:11], v[16:19]\n"
    "v_exp_f32 v27, v27\n"
    "v_mfma_f32_16x16x32_bf16 v[16:19], a[76:79], a[12:15], v[16:19]\n"
    "v_cvt_pk_bf16_f32 v70, v24, v25\n"
    "v_cvt_pk_bf16_f32 v71, v26, v27\n"
    "v_mfma_f32_16x16x32_bf16 v[20:23], a[76:79], a[28:31], 4.0\n"
    "v_exp_f32 v28, v28\n"
    "v_mfma_f32_16x16x32_bf16 v[20:23], a[72:75], a[24:27], v[20:23]\n"
    "ds_read_b128 a[116:119], v7 offset:21504\n"
    "v_mfma_f32_16x16x32_bf16 v[20:23], a[68:71], a[20:23], v[20:23]\n"
    "global_load_lds_dwordx4 v8, s[62:63] offset:2048 sc1\n"
    "v_cvt_f32_i32 v124, v124\n"
    "v_mfma_f32_16x16x32_bf16 v[20:23], a[64:67], a[16:19], v[20:23]\n"
    "v_exp_f32 v29, v29\n"
    "v_mfma_f32_16x16x32_bf16 v[24:27], a[64:67], a[32:35], 4.0\n"
    "v_exp_f32 v30, v30\n"
    "v_mfma_f32_16x16x32_bf16 v[24:27], a[68:71], a[36:39], v[24:27]\n"
    "ds_read_b128 a[120:123], v7 offset:22528\n"
    "v_cvt_f32_i32 v125, v125\n"
    "v_mfma_f32_16x16x32_bf16 v[24:27], a[72:75], a[40:43], v[24:27]\n"
    "v_exp_f32 v31, v31\n"
    "v_mfma_f32_16x16x32_bf16 v[24:27], a[76:79], a[44:47], v[24:27]\n"
    "v_cvt_pk_bf16_f32 v78, v28, v29\n"
    "v_cvt_pk_bf16_f32 v79, v30, v31\n"
    "v_mfma_f32_16x16x32_bf16 v[28:31], a[76:79], a[60:63], 4.0\n"
    "v_exp_f32 v16, v16\n"
    "v_mfma_f32_16x16x32_bf16 v[28:31], a[72:75], a[56:59], v[28:31]\n"
    "ds_read_b128 a[124:127], v7 offset:23552\n"
    "v_cvt_f32_i32 v126, v126\n"
    "v_mfma_f32_16x16x32_bf16 v[28:31], a[68:71], a[52:55], v[28:31]\n"
    "v_exp_f32 v17, v17\n"
    "v_mfma_f32_16x16x32_bf16 v[28:31], a[64:67], a[48:51], v[28:31]\n"
    "v_cvt_f32_i32 v127, v127\n"
    "v_fma_f32 v120, v120, v40, -v32\n"
    "v_mfma_f32_16x16x32_bf16 v[128:131], v[48:51], a[96:99], v[128:131]\n"
    "v_exp_f32 v18, v18\n"
    "v_mfma_f32_16x16x32_bf16 v[128:131], v[52:55], a[100:103], v[128:131]\n"
    "ds_read_b128 a[64:67], v5 offset:8192\n"
    "v_fma_f32 v121, v121, v41, -v33\n"
    "v_mfma_f32_16x16x32_bf16 v[132:135], v[60:63], a[100:103], v[132:135]\n"
    "v_exp_f32 v19, v19\n"
    "v_mfma_f32_16x16x32_bf16 v[132:135], v[56:59], a[96:99], v[132:135]\n"
    "v_cvt_pk_bf16_f32 v80, v16, v17\n"
    "v_cvt_pk_bf16_f32 v81, v18, v19\n"
    "v_mfma_f32_16x16x32_bf16 v[136:139], v[64:67], a[96:99], v[136:139]\n"
    "v_exp_f32 v20, v20\n"
    "v_mfma_f32_16x16x32_bf16 v[136:139], v[68:71], a[100:103], v[136:139]\n"
    "ds_read_b128 a[68:71], v5 offset:9216\n"
    "v_mfma_f32_16x16x32_bf16 v[140:143], v[76:79], a[100:103], v[140:143]\n"
    "global_load_lds_dwordx4 v8, s[62:63] offset:3072 sc1\n"
    "v_fma_f32 v122, v122, v42, -v34\n"
    "v_mfma_f32_16x16x32_bf16 v[140:143], v[72:75], a[96:99], v[140:143]\n"
    "v_exp_f32 v21, v21\n"
    "v_mfma_f32_16x16x32_bf16 v[156:159], v[72:75], a[104:107], v[156:159]\n"
    "v_exp_f32 v22, v22\n"
    "v_mfma_f32_16x16x32_bf16 v[156:159], v[76:79], a[108:111], v[156:159]\n"
    "ds_read_b128 a[72:75], v5 offset:10240\n"
    "v_fma_f32 v123, v123, v43, -v35\n"
    "v_mfma_f32_16x16x32_bf16 v[152:155], v[68:71], a[108:111], v[152:155]\n"
    "v_exp_f32 v23, v23\n"
    "v_mfma_f32_16x16x32_bf16 v[152:155], v[64:67], a[104:107], v[152:155]\n"
    "v_cvt_pk_bf16_f32 v88, v20, v21\n"
    "v_cvt_pk_bf16_f32 v89, v22, v23\n"
    "v_mfma_f32_16x16x32_bf16 v[148:151], v[56:59], a[104:107], v[148:151]\n"
    "s_add_u32 m0, s69, 16384\n"
    "v_exp_f32 v24, v24\n"
    "v_mfma_f32_16x16x32_bf16 v[148:151], v[60:63], a[108:111], v[148:151]\n"
    "ds_read_b128 a[76:79], v5 offset:11264\n"
    "v_fma_f32 v124, v124, v44, -v36\n"
    "v_mfma_f32_16x16x32_bf16 v[144:147], v[52:55], a[108:111], v[144:147]\n"
    "v_exp_f32 v25, v25\n"
    "v_mfma_f32_16x16x32_bf16 v[144:147], v[48:51], a[104:107], v[144:147]\n"
    "v_fma_f32 v125, v125, v45, -v37\n"
    "v_fma_f32 v126, v126, v46, -v38\n"
    "v_smfmac_f32_16x16x64_bf16 a[128:131], a[144:147], v[48:55], v10\n"
    "s_waitcnt lgkmcnt(4)\n"
    "v_fma_f32 v127, v127, v47, -v39\n"
    "v_cvt_pk_bf16_f32 v120, v120, v121\n"
    "v_mfma_f32_16x16x32_bf16 v[16:19], a[80:83], a[0:3], 4.0\n"
    "v_exp_f32 v26, v26\n"
    "v_mfma_f32_16x16x32_bf16 v[16:19], a[84:87], a[4:7], v[16:19]\n"
    "ds_read_b128 a[96:99], v7 offset:24576\n"
    "v_cvt_pk_bf16_f32 v121, v122, v123\n"
    "v_mfma_f32_16x16x32_bf16 v[16:19], a[88:91], a[8:11], v[16:19]\n"
    "v_exp_f32 v27, v27\n"
    "v_mfma_f32_16x16x32_bf16 v[16:19], a[92:95], a[12:15], v[16:19]\n"
    "v_cvt_pk_bf16_f32 v96, v24, v25\n"
    "v_cvt_pk_bf16_f32 v97, v26, v27\n"
    "v_mfma_f32_16x16x32_bf16 v[20:23], a[92:95], a[28:31], 4.0\n"
    "v_exp_f32 v28, v28\n"
    "v_mfma_f32_16x16x32_bf16 v[20:23], a[88:91], a[24:27], v[20:23]\n"
    "ds_read_b128 a[100:103], v7 offset:25600\n"
    "v_mfma_f32_16x16x32_bf16 v[20:23], a[84:87], a[20:23], v[20:23]\n"
    "global_load_lds_dwordx4 v9, s[62:63] offset:0 sc1\n"
    "v_cvt_pk_bf16_f32 v122, v124, v125\n"
    "v_mfma_f32_16x16x32_bf16 v[20:23], a[80:83], a[16:19], v[20:23]\n"
    "v_exp_f32 v29, v29\n"
    "v_mfma_f32_16x16x32_bf16 v[24:27], a[80:83], a[32:35], 4.0\n"
    "v_exp_f32 v30, v30\n"
    "v_mfma_f32_16x16x32_bf16 v[24:27], a[84:87], a[36:39], v[24:27]\n"
    "ds_read_b128 a[104:107], v7 offset:26624\n"
    "v_cvt_pk_bf16_f32 v123, v126, v127\n"
    "v_mfma_f32_16x16x32_bf16 v[24:27], a[88:91], a[40:43], v[24:27]\n"
    "v_exp_f32 v31, v31\n"
    "v_mfma_f32_16x16x32_bf16 v[24:27], a[92:95], a[44:47], v[24:27]\n"
    "v_cvt_pk_bf16_f32 v104, v28, v29\n"
    "v_cvt_pk_bf16_f32 v105, v30, v31\n"
    "v_mfma_f32_16x16x32_bf16 v[28:31], a[92:95], a[60:63], 4.0\n"
    "v_exp_f32 v16, v16\n"
    "v_mfma_f32_16x16x32_bf16 v[28:31], a[88:91], a[56:59], v[28:31]\n"
    "ds_read_b128 a[108:111], v7 offset:27648\n"
    "v_mfma_f32_16x16x32_bf16 v[28:31], a[84:87], a[52:55], v[28:31]\n"
    "v_exp_f32 v17, v17\n"
    "v_mfma_f32_16x16x32_bf16 v[28:31], a[80:83], a[48:51], v[28:31]\n"
    "global_store_dwordx4 v14, v[120:123], s[64:65] offset:0 sc1\n"
    "v_mfma_f32_16x16x32_bf16 v[160:163], v[48:51], a[112:115], v[160:163]\n"
    "v_exp_f32 v18, v18\n"
    "v_mfma_f32_16x16x32_bf16 v[160:163], v[52:55], a[116:119], v[160:163]\n"
    "ds_read_b128 a[80:83], v5 offset:12288\n"
    "v_mfma_f32_16x16x32_bf16 v[164:167], v[60:63], a[116:119], v[164:167]\n"
    "v_exp_f32 v19, v19\n"
    "v_mfma_f32_16x16x32_bf16 v[164:167], v[56:59], a[112:115], v[164:167]\n"
    "v_cvt_pk_bf16_f32 v82, v16, v17\n"
    "v_cvt_pk_bf16_f32 v83, v18, v19\n"
    "v_mfma_f32_16x16x32_bf16 v[168:171], v[64:67], a[112:115], v[168:171]\n"
    "v_exp_f32 v20, v20\n"
    "v_mfma_f32_16x16x32_bf16 v[168:171], v[68:71], a[116:119], v[168:171]\n"
    "ds_read_b128 a[84:87], v5 offset:13312\n"
    "v_mfma_f32_16x16x32_bf16 v[172:175], v[76:79], a[116:119], v[172:175]\n"
    "global_load_lds_dwordx4 v9, s[62:63] offset:1024 sc1\n"
    "v_mfma_f32_16x16x32_bf16 v[172:175], v[72:75], a[112:115], v[172:175]\n"
    "v_exp_f32 v21, v21\n"
    "v_mfma_f32_16x16x32_bf16 v[188:191], v[72:75], a[120:123], v[188:191]\n"
    "v_exp_f32 v22, v22\n"
    "v_mfma_f32_16x16x32_bf16 v[188:191], v[76:79], a[124:127], v[188:191]\n"
    "ds_read_b128 a[88:91], v5 offset:14336\n"
    "v_mfma_f32_16x16x32_bf16 v[184:187], v[68:71], a[124:127], v[184:187]\n"
    "v_exp_f32 v23, v23\n"
    "v_mfma_f32_16x16x32_bf16 v[184:187], v[64:67], a[120:123], v[184:187]\n"
    "v_cvt_pk_bf16_f32 v90, v20, v21\n"
    "v_cvt_pk_bf16_f32 v91, v22, v23\n"
    "v_mfma_f32_16x16x32_bf16 v[180:183], v[56:59], a[120:123], v[180:183]\n"
    "v_exp_f32 v24, v24\n"
    "v_mfma_f32_16x16x32_bf16 v[180:183], v[60:63], a[124:127], v[180:183]\n"
    "ds_read_b128 a[92:95], v5 offset:15360\n"
    "v_mfma_f32_16x16x32_bf16 v[176:179], v[52:55], a[124:127], v[176:179]\n"
    "v_exp_f32 v25, v25\n"
    "v_mfma_f32_16x16x32_bf16 v[176:179], v[48:51], a[120:123], v[176:179]\n"
    "v_smfmac_f32_16x16x64_bf16 a[132:135], a[144:147], v[56:63], v10\n"
    "s_waitcnt lgkmcnt(4)\n"
    "L%=_body:\n"
    "v_mfma_f32_16x16x32_bf16 v[16:19], a[64:67], a[0:3], 4.0\n"
    "L%=_w2:\n"
    "s_cbranch_execz L%=_w6\n"
    "v_exp_f32 v26, v26\n"
    "v_mfma_f32_16x16x32_bf16 v[16:19], a[68:71], a[4:7], v[16:19]\n"
    "ds_read_b128 a[112:115], v7 offset:28672\n"
    "v_mfma_f32_16x16x32_bf16 v[16:19], a[72:75], a[8:11], v[16:19]\n"
    "s_add_u32 s64, s64, 0x40000\n"
    "v_exp_f32 v27, v27\n"
    "v_mfma_f32_16x16x32_bf16 v[16:19], a[76:79], a[12:15], v[16:19]\n"
    "v_cvt_pk_bf16_f32 v98, v24, v25\n"
    "v_cvt_pk_bf16_f32 v99, v26, v27\n"
    "v_mfma_f32_16x16x32_bf16 v[20:23], a[76:79], a[28:31], 4.0\n"
    "v_exp_f32 v28, v28\n"
    "v_mfma_f32_16x16x32_bf16 v[20:23], a[72:75], a[24:27], v[20:23]\n"
    "ds_read_b128 a[116:119], v7 offset:29696\n"
    "v_mfma_f32_16x16x32_bf16 v[20:23], a[68:71], a[20:23], v[20:23]\n"
    "global_load_lds_dwordx4 v9, s[62:63] offset:2048 sc1\n"
    "v_mfma_f32_16x16x32_bf16 v[20:23], a[64:67], a[16:19], v[20:23]\n"
    "v_exp_f32 v29, v29\n"
    "v_mfma_f32_16x16x32_bf16 v[24:27], a[64:67], a[32:35], 4.0\n"
    "v_exp_f32 v30, v30\n"
    "v_mfma_f32_16x16x32_bf16 v[24:27], a[68:71], a[36:39], v[24:27]\n"
    "ds_read_b128 a[120:123], v7 offset:30720\n"
    "v_mfma_f32_16x16x32_bf16 v[24:27], a[72:75], a[40:43], v[24:27]\n"
    "v_exp_f32 v31, v31\n"
    "v_mfma_f32_16x16x32_bf16 v[24:27], a[76:79], a[44:47], v[24:27]\n"
    "v_cvt_pk_bf16_f32 v106, v28, v29\n"
    "v_cvt_pk_bf16_f32 v107, v30, v31\n"
    "v_mfma_f32_16x16x32_bf16 v[28:31], a[76:79], a[60:63], 4.0\n"
    "v_exp_f32 v16, v16\n"
    "v_mfma_f32_16x16x32_bf16 v[28:31], a[72:75], a[56:59], v[28:31]\n"
    "ds_read_b128 a[124:127], v7 offset:31744\n"
    "v_mfma_f32_16x16x32_bf16 v[28:31], a[68:71], a[52:55], v[28:31]\n"
    "s_waitcnt vmcnt(7)\n"
    "v_exp_f32 v17, v17\n"
    "v_mfma_f32_16x16x32_bf16 v[28:31], a[64:67], a[48:51], v[28:31]\n"
    "s_barrier\n"
    "global_load_dwordx4 v[36:39], v15, s[82:83]\n"
    "v_add_u32 v3, s60, v12\n"
    "v_mfma_f32_16x16x32_bf16 v[192:195], v[48:51], a[96:99], v[192:195]\n"
    "v_exp_f32 v18, v18\n"
    "v_mfma_f32_16x16x32_bf16 v[192:195], v[52:55], a[100:103], v[192:195]\n"
    "ds_read_b128 a[64:67], v5 offset:32768\n"
    "v_mfma_f32_16x16x32_bf16 v[196:199], v[60:63], a[100:103], v[196:199]\n"
    "v_exp_f32 v19, v19\n"
    "v_mfma_f32_16x16x32_bf16 v[196:199], v[56:59], a[96:99], v[196:199]\n"
    "v_cvt_pk_bf16_f32 v84, v16, v17\n"
    "v_cvt_pk_bf16_f32 v85, v18, v19\n"
    "v_mfma_f32_16x16x32_bf16 v[200:203], v[64:67], a[96:99], v[200:203]\n"
    "v_exp_f32 v20, v20\n"
    "v_mfma_f32_16x16x32_bf16 v[200:203], v[68:71], a[100:103], v[200:203]\n"
    "ds_read_b128 a[68:71], v5 offset:33792\n"
    "v_mfma_f32_16x16x32_bf16 v[204:207], v[76:79], a[100:103], v[204:207]\n"
    "global_load_lds_dwordx4 v9, s[62:63] offset:3072 sc1\n"
    "v_mfma_f32_16x16x32_bf16 v[204:207], v[72:75], a[96:99], v[204:207]\n"
    "v_exp_f32 v21, v21\n"
    "v_mfma_f32_16x16x32_bf16 v[220:223], v[72:75], a[104:107], v[220:223]\n"
    "v_exp_f32 v22, v22\n"
    "v_mfma_f32_16x16x32_bf16 v[220:223], v[76:79], a[108:111], v[220:223]\n"
    "ds_read_b128 a[72:75], v5 offset:34816\n"
    "v_mfma_f32_16x16x32_bf16 v[216:219], v[68:71], a[108:111], v[216:219]\n"
    "s_add_u32 s62, s62, 0x8000\n"
    "v_exp_f32 v23, v23\n"
    "v_mfma_f32_16x16x32_bf16 v[216:219], v[64:67], a[104:107], v[216:219]\n"
    "v_cvt_pk_bf16_f32 v92, v20, v21\n"
    "v_cvt_pk_bf16_f32 v93, v22, v23\n"
    "v_mfma_f32_16x16x32_bf16 v[212:215], v[56:59], a[104:107], v[212:215]\n"
    "s_add_u32 m0, s69, 32768\n"
    "v_exp_f32 v24, v24\n"
    "v_mfma_f32_16x16x32_bf16 v[212:215], v[60:63], a[108:111], v[212:215]\n"
    "ds_read_b128 a[76:79], v5 offset:35840\n"
    "v_mfma_f32_16x16x32_bf16 v[208:211], v[52:55], a[108:111], v[208:211]\n"
    "v_exp_f32 v25, v25\n"
    "v_mfma_f32_16x16x32_bf16 v[208:211], v[48:51], a[104:107], v[208:211]\n"
    "global_load_dwordx4 v[44:47], v15, s[84:85]\n"
    "v_smfmac_f32_16x16x64_bf16 a[136:139], a[144:147], v[64:71], v10\n"
    "s_waitcnt lgkmcnt(4)\n"
    "global_load_dwordx4 v[112:115], v12, s[80:81] offset:0 nt\n"
    "v_mfma_f32_16x16x32_bf16 v[16:19], a[80:83], a[0:3], 4.0\n"
    "v_exp_f32 v26, v26\n"
    "v_mfma_f32_16x16x32_bf16 v[16:19], a[84:87], a[4:7], v[16:19]\n"
    "ds_read_b128 a[96:99], v7 offset:49152\n"
    "v_mfma_f32_16x16x32_bf16 v[16:19], a[88:91], a[8:11], v[16:19]\n"
    "v_exp_f32 v27, v27\n"
    "v_mfma_f32_16x16x32_bf16 v[16:19], a[92:95], a[12:15], v[16:19]\n"
    "v_cvt_pk_bf16_f32 v100, v24, v25\n"
    "v_cvt_pk_bf16_f32 v101, v26, v27\n"
    "v_mfma_f32_16x16x32_bf16 v[20:23], a[92:95], a[28:31], 4.0\n"
    "v_exp_f32 v28, v28\n"
    "v_mfma_f32_16x16x32_bf16 v[20:23], a[88:91], a[24:27], v[20:23]\n"
    "ds_read_b128 a[100:103], v7 offset:50176\n"
    "v_mfma_f32_16x16x32_bf16 v[20:23], a[84:87], a[20:23], v[20:23]\n"
    "global_load_lds_dwordx4 v8, s[62:63] offset:0 sc1\n"
    "v_mfma_f32_16x16x32_bf16 v[20:23], a[80:83], a[16:19], v[20:23]\n"
    "v_exp_f32 v29, v29\n"
    "v_mfma_f32_16x16x32_bf16 v[24:27], a[80:83], a[32:35], 4.0\n"
    "v_exp_f32 v30, v30\n"
    "v_mfma_f32_16x16x32_bf16 v[24:27], a[84:87], a[36:39], v[24:27]\n"
    "ds_read_b128 a[104:107], v7 offset:51200\n"
    "v_mfma_f32_16x16x32_bf16 v[24:27], a[88:91], a[40:43], v[24:27]\n"
    "v_exp_f32 v31, v31\n"
    "v_mfma_f32_16x16x32_bf16 v[24:27], a[92:95], a[44:47], v[24:27]\n"
    "v_cvt_pk_bf16_f32 v108, v28, v29\n"
    "v_cvt_pk_bf16_f32 v109, v30, v31\n"
    "v_mfma_f32_16x16x32_bf16 v[28:31], a[92:95], a[60:63], 4.0\n"
    "v_exp_f32 v16, v16\n"
    "v_mfma_f32_16x16x32_bf16 v[28:31], a[88:91], a[56:59], v[28:31]\n"
    "ds_read_b128 a[108:111], v7 offset:52224\n"
    "v_mfma_f32_16x16x32_bf16 v[28:31], a[84:87], a[52:55], v[28:31]\n"
    "v_exp_f32 v17, v17\n"
    "v_mfma_f32_16x16x32_bf16 v[28:31], a[80:83], a[48:51], v[28:31]\n"
    "global_load_dwordx4 v[116:119], v3, s[80:81] offset:0 nt\n"
    "v_mfma_f32_16x16x32_bf16 v[224:227], v[48:51], a[112:115], v[224:227]\n"
    "v_exp_f32 v18, v18\n"
    "v_mfma_f32_16x16x32_bf16 v[224:227], v[52:55], a[116:119], v[224:227]\n"
    "ds_read_b128 a[80:83], v5 offset:36864\n"
    "v_mfma_f32_16x16x32_bf16 v[228:231], v[60:63], a[116:119], v[228:231]\n"
    "v_exp_f32 v19, v19\n"
    "v_mfma_f32_16x16x32_bf16 v[228:231], v[56:59], a[112:115], v[228:231]\n"
    "v_cvt_pk_bf16_f32 v86, v16, v17\n"
    "v_cvt_pk_bf16_f32 v87, v18, v19\n"
    "v_mfma_f32_16x16x32_bf16 v[232:235], v[64:67], a[112:115], v[232:235]\n"
    "v_exp_f32 v20, v20\n"
    "v_mfma_f32_16x16x32_bf16 v[232:235], v[68:71], a[116:119], v[232:235]\n"
    "ds_read_b128 a[84:87], v5 offset:37888\n"
    "v_mfma_f32_16x16x32_bf16 v[236:239], v[76:79], a[116:119], v[236:239]\n"
    "global_load_lds_dwordx4 v8, s[62:63] offset:1024 sc1\n"
    "v_mfma_f32_16x16x32_bf16 v[236:239], v[72:75], a[112:115], v[236:239]\n"
    "v_exp_f32 v21, v21\n"
    "v_mfma_f32_16x16x32_bf16 v[252:255], v[72:75], a[120:123], v[252:255]\n"
    "v_exp_f32 v22, v22\n"
    "v_mfma_f32_16x16x32_bf16 v[252:255], v[76:79], a[124:127], v[252:255]\n"
    "ds_read_b128 a[88:91], v5 offset:38912\n"
    "v_mfma_f32_16x16x32_bf16 v[248:251], v[68:71], a[124:127], v[248:251]\n"
    "v_exp_f32 v23, v23\n"
    "v_mfma_f32_16x16x32_bf16 v[248:251], v[64:67], a[120:123], v[248:251]\n"
    "v_cvt_pk_bf16_f32 v94, v20, v21\n"
    "v_cvt_pk_bf16_f32 v95, v22, v23\n"
    "v_mfma_f32_16x16x32_bf16 v[244:247], v[56:59], a[120:123], v[244:247]\n"
    "v_exp_f32 v24, v24\n"
    "v_mfma_f32_16x16x32_bf16 v[244:247], v[60:63], a[124:127], v[244:247]\n"
    "ds_read_b128 a[92:95], v5 offset:39936\n"
    "v_mfma_f32_16x16x32_bf16 v[240:243], v[52:55], a[124:127], v[240:243]\n"
    "v_exp_f32 v25, v25\n"
    "v_mfma_f32_16x16x32_bf16 v[240:243], v[48:51], a[120:123], v[240:243]\n"
    "global_load_dwordx4 v[120:123], v12, s[80:81] offset:2048 nt\n"
    "v_smfmac_f32_16x16x64_bf16 a[140:143], a[144:147], v[72:79], v10\n"
    "s_waitcnt lgkmcnt(4)\n"
    "global_load_dwordx4 v[124:127], v3, s[80:81] offset:2048 nt\n"
    "v_add_u32 v12, 0x40000, v12\n"
    "v_mfma_f32_16x16x32_bf16 v[16:19], a[64:67], a[0:3], 4.0\n"
    "v_exp_f32 v26, v26\n"
    "v_mfma_f32_16x16x32_bf16 v[16:19], a[68:71], a[4:7], v[16:19]\n"
    "ds_read_b128 a[112:115], v7 offset:53248\n"
    "v_accvgpr_read_b32 v2, a158\n"
    "v_mfma_f32_16x16x32_bf16 v[16:19], a[72:75], a[8:11], v[16:19]\n"
    "v_exp_f32 v27, v27\n"
    "v_mfma_f32_16x16x32_bf16 v[16:19], a[76:79], a[12:15], v[16:19]\n"
    "v_cvt_pk_bf16_f32 v102, v24, v25\n"
    "v_cvt_pk_bf16_f32 v103, v26, v27\n"
    "v_mfma_f32_16x16x32_bf16 v[20:23], a[76:79], a[28:31], 4.0\n"
    "v_exp_f32 v28, v28\n"
    "v_mfma_f32_16x16x32_bf16 v[20:23], a[72:75], a[24:27], v[20:23]\n"
    "ds_read_b128 a[116:119], v7 offset:54272\n"
    "v_mfma_f32_16x16x32_bf16 v[20:23], a[68:71], a[20:23], v[20:23]\n"
    "global_load_lds_dwordx4 v8, s[62:63] offset:2048 sc1\n"
    "v_add_u32 v15, 0x400, v15\n"
    "v_mfma_f32_16x16x32_bf16 v[20:23], a[64:67], a[16:19], v[20:23]\n"
    "v_exp_f32 v29, v29\n"
    "v_mfma_f32_16x16x32_bf16 v[24:27], a[64:67], a[32:35], 4.0\n"
    "v_exp_f32 v30, v30\n"
    "v_mfma_f32_16x16x32_bf16 v[24:27], a[68:71], a[36:39], v[24:27]\n"
    "ds_read_b128 a[120:123], v7 offset:55296\n"
    "v_and_b32 v12, 0xfffff, v12\n"
    "v_mfma_f32_16x16x32_bf16 v[24:27], a[72:75], a[40:43], v[24:27]\n"
    "v_exp_f32 v31, v31\n"
    "v_mfma_f32_16x16x32_bf16 v[24:27], a[76:79], a[44:47], v[24:27]\n"
    "v_cvt_pk_bf16_f32 v110, v28, v29\n"
    "v_cvt_pk_bf16_f32 v111, v30, v31\n"
    "v_mfma_f32_16x16x32_bf16 v[28:31], a[76:79], a[60:63], 4.0\n"
    "v_exp_f32 v16, v16\n"
    "v_mfma_f32_16x16x32_bf16 v[28:31], a[72:75], a[56:59], v[28:31]\n"
    "ds_read_b128 a[124:127], v7 offset:56320\n"
    "v_and_b32 v15, 0xfff, v15\n"
    "v_mfma_f32_16x16x32_bf16 v[28:31], a[68:71], a[52:55], v[28:31]\n"
    "v_exp_f32 v17, v17\n"
    "v_mfma_f32_16x16x32_bf16 v[28:31], a[64:67], a[48:51], v[28:31]\n"
    "v_mfma_f32_16x16x32_bf16 v[128:131], v[80:83], a[96:99], v[128:131]\n"
    "v_exp_f32 v18, v18\n"
    "v_mfma_f32_16x16x32_bf16 v[128:131], v[84:87], a[100:103], v[128:131]\n"
    "ds_read_b128 a[64:67], v5 offset:40960\n"
    "v_mfma_f32_16x16x32_bf16 v[132:135], v[92:95], a[100:103], v[132:135]\n"
    "v_exp_f32 v19, v19\n"
    "v_mfma_f32_16x16x32_bf16 v[132:135], v[88:91], a[96:99], v[132:135]\n"
    "v_cvt_pk_bf16_f32 v48, v16, v17\n"
    "v_cvt_pk_bf16_f32 v49, v18, v19\n"
    "v_mfma_f32_16x16x32_bf16 v[136:139], v[96:99], a[96:99], v[136:139]\n"
    "v_exp_f32 v20, v20\n"
    "v_mfma_f32_16x16x32_bf16 v[136:139], v[100:103], a[100:103], v[136:139]\n"
    "ds_read_b128 a[68:71], v5 offset:41984\n"
    "v_mfma_f32_16x16x32_bf16 v[140:143], v[108:111], a[100:103], v[140:143]\n"
    "global_load_lds_dwordx4 v8, s[62:63] offset:3072 sc1\n"
    "v_mfma_f32_16x16x32_bf16 v[140:143], v[104:107], a[96:99], v[140:143]\n"
    "v_exp_f32 v21, v21\n"
    "v_mfma_f32_16x16x32_bf16 v[156:159], v[104:107], a[104:107], v[156:159]\n"
    "v_exp_f32 v22, v22\n"
    "v_mfma_f32_16x16x32_bf16 v[156:159], v[108:111], a[108:111], v[156:159]\n"
    "ds_read_b128 a[72:75], v5 offset:43008\n"
    "v_mfma_f32_16x16x32_bf16 v[152:155], v[100:103], a[108:111], v[152:155]\n"
    "v_exp_f32 v23, v23\n"
    "v_mfma_f32_16x16x32_bf16 v[152:155], v[96:99], a[104:107], v[152:155]\n"
    "v_cvt_pk_bf16_f32 v56, v20, v21\n"
    "v_cvt_pk_bf16_f32 v57, v22, v23\n"
    "v_mfma_f32_16x16x32_bf16 v[148:151], v[88:91], a[104:107], v[148:151]\n"
    "s_add_u32 m0, s69, 49152\n"
    "v_exp_f32 v24, v24\n"
    "v_mfma_f32_16x16x32_bf16 v[148:151], v[92:95], a[108:111], v[148:151]\n"
    "ds_read_b128 a[76:79], v5 offset:44032\n"
    "v_mfma_f32_16x16x32_bf16 v[144:147], v[84:87], a[108:111], v[144:147]\n"
    "s_waitcnt vmcnt(12)\n"
    "v_exp_f32 v25, v25\n"
    "v_mfma_f32_16x16x32_bf16 v[144:147], v[80:83], a[104:107], v[144:147]\n"
    "s_barrier\n"
    "v_smfmac_f32_16x16x64_bf16 a[128:131], a[144:147], v[80:87], v10\n"
    "s_mov_b64 exec, 0xff\n"
    "global_store_dwordx2 v2, a[156:157], s[38:39] sc1\n"
    "s_mov_b64 exec, -1\n"
    "s_waitcnt lgkmcnt(4)\n"
    "v_mfma_f32_16x16x32_bf16 v[16:19], a[80:83], a[0:3], 4.0\n"
    "v_exp_f32 v26, v26\n"
    "v_mfma_f32_16x16x32_bf16 v[16:19], a[84:87], a[4:7], v[16:19]\n"
    "ds_read_b128 a[96:99], v7 offset:57344\n"
    "v_mfma_f32_16x16x32_bf16 v[16:19], a[88:91], a[8:11], v[16:19]\n"
    "v_exp_f32 v27, v27\n"
    "v_mfma_f32_16x16x32_bf16 v[16:19], a[92:95], a[12:15], v[16:19]\n"
    "v_cvt_pk_bf16_f32 v64, v24, v25\n"
    "v_cvt_pk_bf16_f32 v65, v26, v27\n"
    "v_mfma_f32_16x16x32_bf16 v[20:23], a[92:95], a[28:31], 4.0\n"
    "v_exp_f32 v28, v28\n"
    "v_mfma_f32_16x16x32_bf16 v[20:23], a[88:91], a[24:27], v[20:23]\n"
    "ds_read_b128 a[100:103], v7 offset:58368\n"
    "v_mfma_f32_16x16x32_bf16 v[20:23], a[84:87], a[20:23], v[20:23]\n"
    "global_load_lds_dwordx4 v9, s[62:63] offset:0 sc1\n"
    "v_mfma_f32_16x16x32_bf16 v[20:23], a[80:83], a[16:19], v[20:23]\n"
    "v_exp_f32 v29, v29\n"
    "v_mfma_f32_16x16x32_bf16 v[24:27], a[80:83], a[32:35], 4.0\n"
    "v_exp_f32 v30, v30\n"
    "v_mfma_f32_16x16x32_bf16 v[24:27], a[84:87], a[36:39], v[24:27]\n"
    "ds_read_b128 a[104:107], v7 offset:59392\n"
    "v_mfma_f32_16x16x32_bf16 v[24:27], a[88:91], a[40:43], v[24:27]\n"
    "v_exp_f32 v31, v31\n"
    "v_mfma_f32_16x16x32_bf16 v[24:27], a[92:95], a[44:47], v[24:27]\n"
    "v_cvt_pk_bf16_f32 v72, v28, v29\n"
    "v_cvt_pk_bf16_f32 v73, v30, v31\n"
    "v_mfma_f32_16x16x32_bf16 v[28:31], a[92:95], a[60:63], 4.0\n"
    "v_exp_f32 v16, v16\n"
    "v_mfma_f32_16x16x32_bf16 v[28:31], a[88:91], a[56:59], v[28:31]\n"
    "ds_read_b128 a[108:111], v7 offset:60416\n"
    "v_mfma_f32_16x16x32_bf16 v[28:31], a[84:87], a[52:55], v[28:31]\n"
    "v_exp_f32 v17, v17\n"
    "v_mfma_f32_16x16x32_bf16 v[28:31], a[80:83], a[48:51], v[28:31]\n"
    "v_mfma_f32_16x16x32_bf16 v[160:163], v[80:83], a[112:115], v[160:163]\n"
    "v_exp_f32 v18, v18\n"
    "v_mfma_f32_16x16x32_bf16 v[160:163], v[84:87], a[116:119], v[160:163]\n"
    "ds_read_b128 a[80:83], v5 offset:45056\n"
    "v_mfma_f32_16x16x32_bf16 v[164:167], v[92:95], a[116:119], v[164:167]\n"
    "v_exp_f32 v19, v19\n"
    "v_mfma_f32_16x16x32_bf16 v[164:167], v[88:91], a[112:115], v[164:167]\n"
    "v_cvt_pk_bf16_f32 v50, v16, v17\n"
    "v_cvt_pk_bf16_f32 v51, v18, v19\n"
    "v_mfma_f32_16x16x32_bf16 v[168:171], v[96:99], a[112:115], v[168:171]\n"
    "v_exp_f32 v20, v20\n"
    "v_mfma_f32_16x16x32_bf16 v[168:171], v[100:103], a[116:119], v[168:171]\n"
    "ds_read_b128 a[84:87], v5 offset:46080\n"
    "v_mfma_f32_16x16x32_bf16 v[172:175], v[108:111], a[116:119], v[172:175]\n"
    "global_load_lds_dwordx4 v9, s[62:63] offset:1024 sc1\n"
    "v_mfma_f32_16x16x32_bf16 v[172:175], v[104:107], a[112:115], v[172:175]\n"
    "v_exp_f32 v21, v21\n"
    "v_mfma_f32_16x16x32_bf16 v[188:191], v[104:107], a[120:123], v[188:191]\n"
    "v_exp_f32 v22, v22\n"
    "v_mfma_f32_16x16x32_bf16 v[188:191], v[108:111], a[124:127], v[188:191]\n"
    "ds_read_b128 a[88:91], v5 offset:47104\n"
    "v_mfma_f32_16x16x32_bf16 v[184:187], v[100:103], a[124:127], v[184:187]\n"
    "v_exp_f32 v23, v23\n"
    "v_mfma_f32_16x16x32_bf16 v[184:187], v[96:99], a[120:123], v[184:187]\n"
    "v_cvt_pk_bf16_f32 v58, v20, v21\n"
    "v_cvt_pk_bf16_f32 v59, v22, v23\n"
    "v_mfma_f32_16x16x32_bf16 v[180:183], v[88:91], a[120:123], v[180:183]\n"
    "v_exp_f32 v24, v24\n"
    "v_mfma_f32_16x16x32_bf16 v[180:183], v[92:95], a[124:127], v[180:183]\n"
    "ds_read_b128 a[92:95], v5 offset:48128\n"
    "v_mfma_f32_16x16x32_bf16 v[176:179], v[84:87], a[124:127], v[176:179]\n"
    "v_exp_f32 v25, v25\n"
    "v_mfma_f32_16x16x32_bf16 v[176:179], v[80:83], a[120:123], v[176:179]\n"
    "v_smfmac_f32_16x16x64_bf16 a[132:135], a[144:147], v[88:95], v10\n"
    "s_waitcnt lgkmcnt(4)\n"
    "v_mfma_f32_16x16x32_bf16 v[16:19], a[64:67], a[0:3], 4.0\n"
    "v_exp_f32 v26, v26\n"
    "v_mfma_f32_16x16x32_bf16 v[16:19], a[68:71], a[4:7], v[16:19]\n"
    "ds_read_b128 a[112:115], v7 offset:61440\n"
    "v_mfma_f32_16x16x32_bf16 v[16:19], a[72:75], a[8:11], v[16:19]\n"
    "v_exp_f32 v27, v27\n"
    "v_mfma_f32_16x16x32_bf16 v[16:19], a[76:79], a[12:15], v[16:19]\n"
    "v_cvt_pk_bf16_f32 v66, v24, v25\n"
    "v_cvt_pk_bf16_f32 v67, v26, v27\n"
    "v_mfma_f32_16x16x32_bf16 v[20:23], a[76:79], a[28:31], 4.0\n"
    "v_exp_f32 v28, v28\n"
    "v_mfma_f32_16x16x32_bf16 v[20:23], a[72:75], a[24:27], v[20:23]\n"
    "ds_read_b128 a[116:119], v7 offset:62464\n"
    "v_mfma_f32_16x16x32_bf16 v[20:23], a[68:71], a[20:23], v[20:23]\n"
    "global_load_lds_dwordx4 v9, s[62:63] offset:2048 sc1\n"
    "v_mfma_f32_16x16x32_bf16 v[20:23], a[64:67], a[16:19], v[20:23]\n"
    "v_exp_f32 v29, v29\n"
    "v_mfma_f32_16x16x32_bf16 v[24:27], a[64:67], a[32:35], 4.0\n"
    "v_exp_f32 v30, v30\n"
    "v_mfma_f32_16x16x32_bf16 v[24:27], a[68:71], a[36:39], v[24:27]\n"
    "ds_read_b128 a[120:123], v7 offset:63488\n"
    "v_mfma_f32_16x16x32_bf16 v[24:27], a[72:75], a[40:43], v[24:27]\n"
    "v_exp_f32 v31, v31\n"
    "v_mfma_f32_16x16x32_bf16 v[24:27], a[76:79], a[44:47], v[24:27]\n"
    "v_cvt_pk_bf16_f32 v74, v28, v29\n"
    "v_cvt_pk_bf16_f32 v75, v30, v31\n"
    "v_mfma_f32_16x16x32_bf16 v[28:31], a[76:79], a[60:63], 4.0\n"
    "v_exp_f32 v16, v16\n"
    "v_mfma_f32_16x16x32_bf16 v[28:31], a[72:75], a[56:59], v[28:31]\n"
    "ds_read_b128 a[124:127], v7 offset:64512\n"
    "v_mfma_f32_16x16x32_bf16 v[28:31], a[68:71], a[52:55], v[28:31]\n"
    "L%=_w3:\n"
    "s_cbranch_execz L%=_w7\n"
    "s_waitcnt vmcnt(12)\n"
    "v_exp_f32 v17, v17\n"
    "v_mfma_f32_16x16x32_bf16 v[28:31], a[64:67], a[48:51], v[28:31]\n"
    "s_barrier\n"
    "v_mfma_f32_16x16x32_bf16 v[192:195], v[80:83], a[96:99], v[192:195]\n"
    "v_exp_f32 v18, v18\n"
    "v_mfma_f32_16x16x32_bf16 v[192:195], v[84:87], a[100:103], v[192:195]\n"
    "ds_read_b128 a[64:67], v4 offset:0\n"
    "v_mfma_f32_16x16x32_bf16 v[196:199], v[92:95], a[100:103], v[196:199]\n"
    "v_exp_f32 v19, v19\n"
    "v_mfma_f32_16x16x32_bf16 v[196:199], v[88:91], a[96:99], v[196:199]\n"
    "v_cvt_pk_bf16_f32 v52, v16, v17\n"
    "v_cvt_pk_bf16_f32 v53, v18, v19\n"
    "v_mfma_f32_16x16x32_bf16 v[200:203], v[96:99], a[96:99], v[200:203]\n"
    "v_exp_f32 v20, v20\n"
    "v_mfma_f32_16x16x32_bf16 v[200:203], v[100:103], a[100:103], v[200:203]\n"
    "ds_read_b128 a[68:71], v4 offset:1024\n"
    "v_mfma_f32_16x16x32_bf16 v[204:207], v[108:111], a[100:103], v[204:207]\n"
    "global_load_lds_dwordx4 v9, s[62:63] offset:3072 sc1\n"
    "v_mfma_f32_16x16x32_bf16 v[204:207], v[104:107], a[96:99], v[204:207]\n"
    "v_exp_f32 v21, v21\n"
    "v_mfma_f32_16x16x32_bf16 v[220:223], v[104:107], a[104:107], v[220:223]\n"
    "v_exp_f32 v22, v22\n"
    "v_mfma_f32_16x16x32_bf16 v[220:223], v[108:111], a[108:111], v[220:223]\n"
    "ds_read_b128 a[72:75], v4 offset:2048\n"
    "v_mfma_f32_16x16x32_bf16 v[216:219], v[100:103], a[108:111], v[216:219]\n"
    "s_add_u32 s62, s62, 0x8000\n"
    "v_exp_f32 v23, v23\n"
    "v_mfma_f32_16x16x32_bf16 v[216:219], v[96:99], a[104:107], v[216:219]\n"
    "v_cvt_pk_bf16_f32 v60, v20, v21\n"
    "v_cvt_pk_bf16_f32 v61, v22, v23\n"
    "v_mfma_f32_16x16x32_bf16 v[212:215], v[88:91], a[104:107], v[212:215]\n"
    "s_add_u32 m0, s69, 65536\n"
    "v_exp_f32 v24, v24\n"
    "v_mfma_f32_16x16x32_bf16 v[212:215], v[92:95], a[108:111], v[212:215]\n"
    "ds_read_b128 a[76:79], v4 offset:3072\n"
    "v_mfma_f32_16x16x32_bf16 v[208:211], v[84:87], a[108:111], v[208:211]\n"
    "v_exp_f32 v25, v25\n"
    "v_mfma_f32_16x16x32_bf16 v[208:211], v[80:83], a[104:107], v[208:211]\n"
    "v_smfmac_f32_16x16x64_bf16 a[136:139], a[144:147], v[96:103], v10\n"
    "s_waitcnt lgkmcnt(4)\n"
    "v_mfma_f32_16x16x32_bf16 v[16:19], a[80:83], a[0:3], 4.0\n"
    "v_exp_f32 v26, v26\n"
    "v_mfma_f32_16x16x32_bf16 v[16:19], a[84:87], a[4:7], v[16:19]\n"
    "ds_read_b128 a[96:99], v6 offset:16384\n"
    "v_mfma_f32_16x16x32_bf16 v[16:19], a[88:91], a[8:11], v[16:19]\n"
    "v_exp_f32 v27, v27\n"
    "v_mfma_f32_16x16x32_bf16 v[16:19], a[92:95], a[12:15], v[16:19]\n"
    "v_cvt_pk_bf16_f32 v68, v24, v25\n"
    "v_cvt_pk_bf16_f32 v69, v26, v27\n"
    "v_mfma_f32_16x16x32_bf16 v[20:23], a[92:95], a[28:31], 4.0\n"
    "v_exp_f32 v28, v28\n"
    "v_mfma_f32_16x16x32_bf16 v[20:23], a[88:91], a[24:27], v[20:23]\n"
    "ds_read_b128 a[100:103], v6 offset:17408\n"
    "v_mfma_f32_16x16x32_bf16 v[20:23], a[84:87], a[20:23], v[20:23]\n"
    "global_load_lds_dwordx4 v8, s[62:63] offset:0 sc1\n"
    "v_mfma_f32_16x16x32_bf16 v[20:23], a[80:83], a[16:19], v[20:23]\n"
    "v_exp_f32 v29, v29\n"
    "v_mfma_f32_16x16x32_bf16 v[24:27], a[80:83], a[32:35], 4.0\n"
    "v_exp_f32 v30, v30\n"
    "v_mfma_f32_16x16x32_bf16 v[24:27], a[84:87], a[36:39], v[24:27]\n"
    "ds_read_b128 a[104:107], v6 offset:18432\n"
    "v_mfma_f32_16x16x32_bf16 v[24:27], a[88:91], a[40:43], v[24:27]\n"
    "v_exp_f32 v31, v31\n"
    "v_mfma_f32_16x16x32_bf16 v[24:27], a[92:95], a[44:47], v[24:27]\n"
    "v_cvt_pk_bf16_f32 v76, v28, v29\n"
    "v_cvt_pk_bf16_f32 v77, v30, v31\n"
    "v_mfma_f32_16x16x32_bf16 v[28:31], a[92:95], a[60:63], 4.0\n"
    "v_exp_f32 v16, v16\n"
    "v_mfma_f32_16x16x32_bf16 v[28:31], a[88:91], a[56:59], v[28:31]\n"
    "ds_read_b128 a[108:111], v6 offset:19456\n"
    "v_mfma_f32_16x16x32_bf16 v[28:31], a[84:87], a[52:55], v[28:31]\n"
    "v_exp_f32 v17, v17\n"
    "v_mfma_f32_16x16x32_bf16 v[28:31], a[80:83], a[48:51], v[28:31]\n"
    "v_mfma_f32_16x16x32_bf16 v[224:227], v[80:83], a[112:115], v[224:227]\n"
    "v_exp_f32 v18, v18\n"
    "v_mfma_f32_16x16x32_bf16 v[224:227], v[84:87], a[116:119], v[224:227]\n"
    "ds_read_b128 a[80:83], v4 offset:4096\n"
    "v_mfma_f32_16x16x32_bf16 v[228:231], v[92:95], a[116:119], v[228:231]\n"
    "v_exp_f32 v19, v19\n"
    "v_mfma_f32_16x16x32_bf16 v[228:231], v[88:91], a[112:115], v[228:231]\n"
    "v_cvt_pk_bf16_f32 v54, v16, v17\n"
    "v_cvt_pk_bf16_f32 v55, v18, v19\n"
    "v_mfma_f32_16x16x32_bf16 v[232:235], v[96:99], a[112:115], v[232:235]\n"
    "v_exp_f32 v20, v20\n"
    "v_mfma_f32_16x16x32_bf16 v[232:235], v[100:103], a[116:119], v[232:235]\n"
    "ds_read_b128 a[84:87], v4 offset:5120\n"
    "v_mfma_f32_16x16x32_bf16 v[236:239], v[108:111], a[116:119], v[236:239]\n"
    "global_load_lds_dwordx4 v8, s[62:63] offset:1024 sc1\n"
    "v_mfma_f32_16x16x32_bf16 v[236:239], v[104:107], a[112:115], v[236:239]\n"
    "v_exp_f32 v21, v21\n"
    "v_mfma_f32_16x16x32_bf16 v[252:255], v[104:107], a[120:123], v[252:255]\n"
    "v_exp_f32 v22, v22\n"
    "v_mfma_f32_16x16x32_bf16 v[252:255], v[108:111], a[124:127], v[252:255]\n"
    "ds_read_b128 a[88:91], v4 offset:6144\n"
    "v_mfma_f32_16x16x32_bf16 v[248:251], v[100:103], a[124:127], v[248:251]\n"
    "v_exp_f32 v23, v23\n"
    "v_mfma_f32_16x16x32_bf16 v[248:251], v[96:99], a[120:123], v[248:251]\n"
    "v_cvt_pk_bf16_f32 v62, v20, v21\n"
    "v_cvt_pk_bf16_f32 v63, v22, v23\n"
    "v_mfma_f32_16x16x32_bf16 v[244:247], v[88:91], a[120:123], v[244:247]\n"
    "v_exp_f32 v24, v24\n"
    "v_mfma_f32_16x16x32_bf16 v[244:247], v[92:95], a[124:127], v[244:247]\n"
    "ds_read_b128 a[92:95], v4 offset:7168\n"
    "v_mfma_f32_16x16x32_bf16 v[240:243], v[84:87], a[124:127], v[240:243]\n"
    "v_exp_f32 v25, v25\n"
    "v_mfma_f32_16x16x32_bf16 v[240:243], v[80:83], a[120:123], v[240:243]\n"
    "v_smfmac_f32_16x16x64_bf16 a[140:143], a[144:147], v[104:111], v10\n"
    "s_waitcnt lgkmcnt(4)\n"
    "v_mfma_f32_16x16x32_bf16 v[16:19], a[64:67], a[0:3], 4.0\n"
    "v_exp_f32 v26, v26\n"
    "v_mfma_f32_16x16x32_bf16 v[16:19], a[68:71], a[4:7], v[16:19]\n"
    "ds_read_b128 a[112:115], v6 offset:20480\n"
    "v_mfma_f32_16x16x32_bf16 v[16:19], a[72:75], a[8:11], v[16:19]\n"
    "v_exp_f32 v27, v27\n"
    "v_mfma_f32_16x16x32_bf16 v[16:19], a[76:79], a[12:15], v[16:19]\n"
    "v_cvt_pk_bf16_f32 v70, v24, v25\n"
    "v_cvt_pk_bf16_f32 v71, v26, v27\n"
    "v_mfma_f32_16x16x32_bf16 v[20:23], a[76:79], a[28:31], 4.0\n"
    "v_exp_f32 v28, v28\n"
    "v_mfma_f32_16x16x32_bf16 v[20:23], a[72:75], a[24:27], v[20:23]\n"
    "ds_read_b128 a[116:119], v6 offset:21504\n"
    "v_mfma_f32_16x16x32_bf16 v[20:23], a[68:71], a[20:23], v[20:23]\n"
    "global_load_lds_dwordx4 v8, s[62:63] offset:2048 sc1\n"
    "v_mfma_f32_16x16x32_bf16 v[20:23], a[64:67], a[16:19], v[20:23]\n"
    "v_exp_f32 v29, v29\n"
    "v_mfma_f32_16x16x32_bf16 v[24:27], a[64:67], a[32:35], 4.0\n"
    "v_exp_f32 v30, v30\n"
    "v_mfma_f32_16x16x32_bf16 v[24:27], a[68:71], a[36:39], v[24:27]\n"
    "ds_read_b128 a[120:123], v6 offset:22528\n"
    "v_mfma_f32_16x16x32_bf16 v[24:27], a[72:75], a[40:43], v[24:27]\n"
    "v_exp_f32 v31, v31\n"
    "v_mfma_f32_16x16x32_bf16 v[24:27], a[76:79], a[44:47], v[24:27]\n"
    "v_cvt_pk_bf16_f32 v78, v28, v29\n"
    "v_cvt_pk_bf16_f32 v79, v30, v31\n"
    "v_mfma_f32_16x16x32_bf16 v[28:31], a[76:79], a[60:63], 4.0\n"
    "v_exp_f32 v16, v16\n"
    "v_mfma_f32_16x16x32_bf16 v[28:31], a[72:75], a[56:59], v[28:31]\n"
    "ds_read_b128 a[124:127], v6 offset:23552\n"
    "v_mfma_f32_16x16x32_bf16 v[28:31], a[68:71], a[52:55], v[28:31]\n"
    "v_exp_f32 v17, v17\n"
    "v_mfma_f32_16x16x32_bf16 v[28:31], a[64:67], a[48:51], v[28:31]\n"
    "v_mfma_f32_16x16x32_bf16 v[128:131], v[48:51], a[96:99], v[128:131]\n"
    "v_exp_f32 v18, v18\n"
    "v_mfma_f32_16x16x32_bf16 v[128:131], v[52:55], a[100:103], v[128:131]\n"
    "ds_read_b128 a[64:67], v4 offset:8192\n"
    "v_mfma_f32_16x16x32_bf16 v[132:135], v[60:63], a[100:103], v[132:135]\n"
    "v_exp_f32 v19, v19\n"
    "v_mfma_f32_16x16x32_bf16 v[132:135], v[56:59], a[96:99], v[132:135]\n"
    "v_cvt_pk_bf16_f32 v80, v16, v17\n"
    "v_cvt_pk_bf16_f32 v81, v18, v19\n"
    "v_mfma_f32_16x16x32_bf16 v[136:139], v[64:67], a[96:99], v[136:139]\n"
    "v_exp_f32 v20, v20\n"
    "v_mfma_f32_16x16x32_bf16 v[136:139], v[68:71], a[100:103], v[136:139]\n"
    "ds_read_b128 a[68:71], v4 offset:9216\n"
    "v_mfma_f32_16x16x32_bf16 v[140:143], v[76:79], a[100:103], v[140:143]\n"
    "global_load_lds_dwordx4 v8, s[62:63] offset:3072 sc1\n"
    "v_mfma_f32_16x16x32_bf16 v[140:143], v[72:75], a[96:99], v[140:143]\n"
    "v_exp_f32 v21, v21\n"
    "v_mfma_f32_16x16x32_bf16 v[156:159], v[72:75], a[104:107], v[156:159]\n"
    "v_exp_f32 v22, v22\n"
    "v_mfma_f32_16x16x32_bf16 v[156:159], v[76:79], a[108:111], v[156:159]\n"
    "ds_read_b128 a[72:75], v4 offset:10240\n"
    "v_mfma_f32_16x16x32_bf16 v[152:155], v[68:71], a[108:111], v[152:155]\n"
    "v_exp_f32 v23, v23\n"
    "v_mfma_f32_16x16x32_bf16 v[152:155], v[64:67], a[104:107], v[152:155]\n"
    "v_cvt_pk_bf16_f32 v88, v20, v21\n"
    "v_cvt_pk_bf16_f32 v89, v22, v23\n"
    "v_mfma_f32_16x16x32_bf16 v[148:151], v[56:59], a[104:107], v[148:151]\n"
    "s_add_u32 m0, s69, 81920\n"
    "v_exp_f32 v24, v24\n"
    "v_mfma_f32_16x16x32_bf16 v[148:151], v[60:63], a[108:111], v[148:151]\n"
    "ds_read_b128 a[76:79], v4 offset:11264\n"
    "v_mfma_f32_16x16x32_bf16 v[144:147], v[52:55], a[108:111], v[144:147]\n"
    "v_exp_f32 v25, v25\n"
    "v_mfma_f32_16x16x32_bf16 v[144:147], v[48:51], a[104:107], v[144:147]\n"
    "v_smfmac_f32_16x16x64_bf16 a[128:131], a[144:147], v[48:55], v10\n"
    "s_waitcnt lgkmcnt(4)\n"
    "v_mfma_f32_16x16x32_bf16 v[16:19], a[80:83], a[0:3], 4.0\n"
    "v_exp_f32 v26, v26\n"
    "v_mfma_f32_16x16x32_bf16 v[16:19], a[84:87], a[4:7], v[16:19]\n"
    "ds_read_b128 a[96:99], v6 offset:24576\n"
    "v_mfma_f32_16x16x32_bf16 v[16:19], a[88:91], a[8:11], v[16:19]\n"
    "v_exp_f32 v27, v27\n"
    "v_mfma_f32_16x16x32_bf16 v[16:19], a[92:95], a[12:15], v[16:19]\n"
    "v_cvt_pk_bf16_f32 v96, v24, v25\n"
    "v_cvt_pk_bf16_f32 v97, v26, v27\n"
    "v_mfma_f32_16x16x32_bf16 v[20:23], a[92:95], a[28:31], 4.0\n"
    "v_exp_f32 v28, v28\n"
    "v_mfma_f32_16x16x32_bf16 v[20:23], a[88:91], a[24:27], v[20:23]\n"
    "ds_read_b128 a[100:103], v6 offset:25600\n"
    "v_mfma_f32_16x16x32_bf16 v[20:23], a[84:87], a[20:23], v[20:23]\n"
    "global_load_lds_dwordx4 v9, s[62:63] offset:0 sc1\n"
    "v_mfma_f32_16x16x32_bf16 v[20:23], a[80:83], a[16:19], v[20:23]\n"
    "v_exp_f32 v29, v29\n"
    "v_mfma_f32_16x16x32_bf16 v[24:27], a[80:83], a[32:35], 4.0\n"
    "v_exp_f32 v30, v30\n"
    "v_mfma_f32_16x16x32_bf16 v[24:27], a[84:87], a[36:39], v[24:27]\n"
    "ds_read_b128 a[104:107], v6 offset:26624\n"
    "v_mfma_f32_16x16x32_bf16 v[24:27], a[88:91], a[40:43], v[24:27]\n"
    "v_exp_f32 v31, v31\n"
    "v_mfma_f32_16x16x32_bf16 v[24:27], a[92:95], a[44:47], v[24:27]\n"
    "v_cvt_pk_bf16_f32 v104, v28, v29\n"
    "v_cvt_pk_bf16_f32 v105, v30, v31\n"
    "v_mfma_f32_16x16x32_bf16 v[28:31], a[92:95], a[60:63], 4.0\n"
    "v_exp_f32 v16, v16\n"
    "v_mfma_f32_16x16x32_bf16 v[28:31], a[88:91], a[56:59], v[28:31]\n"
    "ds_read_b128 a[108:111], v6 offset:27648\n"
    "v_mfma_f32_16x16x32_bf16 v[28:31], a[84:87], a[52:55], v[28:31]\n"
    "v_exp_f32 v17, v17\n"
    "v_mfma_f32_16x16x32_bf16 v[28:31], a[80:83], a[48:51], v[28:31]\n"
    "v_mfma_f32_16x16x32_bf16 v[160:163], v[48:51], a[112:115], v[160:163]\n"
    "v_exp_f32 v18, v18\n"
    "v_mfma_f32_16x16x32_bf16 v[160:163], v[52:55], a[116:119], v[160:163]\n"
    "ds_read_b128 a[80:83], v4 offset:12288\n"
    "v_mfma_f32_16x16x32_bf16 v[164:167], v[60:63], a[116:119], v[164:167]\n"
    "v_exp_f32 v19, v19\n"
    "v_mfma_f32_16x16x32_bf16 v[164:167], v[56:59], a[112:115], v[164:167]\n"
    "v_cvt_pk_bf16_f32 v82, v16, v17\n"
    "v_cvt_pk_bf16_f32 v83, v18, v19\n"
    "v_mfma_f32_16x16x32_bf16 v[168:171], v[64:67], a[112:115], v[168:171]\n"
    "v_exp_f32 v20, v20\n"
    "v_mfma_f32_16x16x32_bf16 v[168:171], v[68:71], a[116:119], v[168:171]\n"
    "ds_read_b128 a[84:87], v4 offset:13312\n"
    "v_mfma_f32_16x16x32_bf16 v[172:175], v[76:79], a[116:119], v[172:175]\n"
    "global_load_lds_dwordx4 v9, s[62:63] offset:1024 sc1\n"
    "v_mfma_f32_16x16x32_bf16 v[172:175], v[72:75], a[112:115], v[172:175]\n"
    "v_exp_f32 v21, v21\n"
    "v_mfma_f32_16x16x32_bf16 v[188:191], v[72:75], a[120:123], v[188:191]\n"
    "v_exp_f32 v22, v22\n"
    "v_mfma_f32_16x16x32_bf16 v[188:191], v[76:79], a[124:127], v[188:191]\n"
    "ds_read_b128 a[88:91], v4 offset:14336\n"
    "v_mfma_f32_16x16x32_bf16 v[184:187], v[68:71], a[124:127], v[184:187]\n"
    "v_exp_f32 v23, v23\n"
    "v_mfma_f32_16x16x32_bf16 v[184:187], v[64:67], a[120:123], v[184:187]\n"
    "v_cvt_pk_bf16_f32 v90, v20, v21\n"
    "v_cvt_pk_bf16_f32 v91, v22, v23\n"
    "v_mfma_f32_16x16x32_bf16 v[180:183], v[56:59], a[120:123], v[180:183]\n"
    "v_exp_f32 v24, v24\n"
    "v_mfma_f32_16x16x32_bf16 v[180:183], v[60:63], a[124:127], v[180:183]\n"
    "ds_read_b128 a[92:95], v4 offset:15360\n"
    "v_mfma_f32_16x16x32_bf16 v[176:179], v[52:55], a[124:127], v[176:179]\n"
    "v_exp_f32 v25, v25\n"
    "v_mfma_f32_16x16x32_bf16 v[176:179], v[48:51], a[120:123], v[176:179]\n"
    "v_smfmac_f32_16x16x64_bf16 a[132:135], a[144:147], v[56:63], v10\n"
    "s_waitcnt lgkmcnt(4)\n"
    "v_mfma_f32_16x16x32_bf16 v[16:19], a[64:67], a[0:3], 4.0\n"
    "v_exp_f32 v26, v26\n"
    "v_mfma_f32_16x16x32_bf16 v[16:19], a[68:71], a[4:7], v[16:19]\n"
    "ds_read_b128 a[112:115], v6 offset:28672\n"
    "v_mfma_f32_16x16x32_bf16 v[16:19], a[72:75], a[8:11], v[16:19]\n"
    "v_exp_f32 v27, v27\n"
    "v_mfma_f32_16x16x32_bf16 v[16:19], a[76:79], a[12:15], v[16:19]\n"
    "v_cvt_pk_bf16_f32 v98, v24, v25\n"
    "v_cvt_pk_bf16_f32 v99, v26, v27\n"
    "v_mfma_f32_16x16x32_bf16 v[20:23], a[76:79], a[28:31], 4.0\n"
    "v_exp_f32 v28, v28\n"
    "v_mfma_f32_16x16x32_bf16 v[20:23], a[72:75], a[24:27], v[20:23]\n"
    "ds_read_b128 a[116:119], v6 offset:29696\n"
    "v_mfma_f32_16x16x32_bf16 v[20:23], a[68:71], a[20:23], v[20:23]\n"
    "global_load_lds_dwordx4 v9, s[62:63] offset:2048 sc1\n"
    "v_mfma_f32_16x16x32_bf16 v[20:23], a[64:67], a[16:19], v[20:23]\n"
    "v_exp_f32 v29, v29\n"
    "v_mfma_f32_16x16x32_bf16 v[24:27], a[64:67], a[32:35], 4.0\n"
    "v_exp_f32 v30, v30\n"
    "v_mfma_f32_16x16x32_bf16 v[24:27], a[68:71], a[36:39], v[24:27]\n"
    "ds_read_b128 a[120:123], v6 offset:30720\n"
    "v_mfma_f32_16x16x32_bf16 v[24:27], a[72:75], a[40:43], v[24:27]\n"
    "v_exp_f32 v31, v31\n"
    "v_mfma_f32_16x16x32_bf16 v[24:27], a[76:79], a[44:47], v[24:27]\n"
    "v_cvt_pk_bf16_f32 v106, v28, v29\n"
    "v_cvt_pk_bf16_f32 v107, v30, v31\n"
    "v_mfma_f32_16x16x32_bf16 v[28:31], a[76:79], a[60:63], 4.0\n"
    "v_exp_f32 v16, v16\n"
    "v_mfma_f32_16x16x32_bf16 v[28:31], a[72:75], a[56:59], v[28:31]\n"
    "ds_read_b128 a[124:127], v6 offset:31744\n"
    "v_mfma_f32_16x16x32_bf16 v[28:31], a[68:71], a[52:55], v[28:31]\n"
    "s_waitcnt vmcnt(7)\n"
    "v_exp_f32 v17, v17\n"
    "v_mfma_f32_16x16x32_bf16 v[28:31], a[64:67], a[48:51], v[28:31]\n"
    "s_barrier\n"
    "v_mfma_f32_16x16x32_bf16 v[192:195], v[48:51], a[96:99], v[192:195]\n"
    "v_exp_f32 v18, v18\n"
    "v_mfma_f32_16x16x32_bf16 v[192:195], v[52:55], a[100:103], v[192:195]\n"
    "ds_read_b128 a[64:67], v4 offset:32768\n"
    "v_mfma_f32_16x16x32_bf16 v[196:199], v[60:63], a[100:103], v[196:199]\n"
    "v_exp_f32 v19, v19\n"
    "v_mfma_f32_16x16x32_bf16 v[196:199], v[56:59], a[96:99], v[196:199]\n"
    "v_cvt_pk_bf16_f32 v84, v16, v17\n"
    "v_cvt_pk_bf16_f32 v85, v18, v19\n"
    "v_mfma_f32_16x16x32_bf16 v[200:203], v[64:67], a[96:99], v[200:203]\n"
    "v_exp_f32 v20, v20\n"
    "v_mfma_f32_16x16x32_bf16 v[200:203], v[68:71], a[100:103], v[200:203]\n"
    "ds_read_b128 a[68:71], v4 offset:33792\n"
    "v_mfma_f32_16x16x32_bf16 v[204:207], v[76:79], a[100:103], v[204:207]\n"
    "global_load_lds_dwordx4 v9, s[62:63] offset:3072 sc1\n"
    "v_mfma_f32_16x16x32_bf16 v[204:207], v[72:75], a[96:99], v[204:207]\n"
    "v_exp_f32 v21, v21\n"
    "v_mfma_f32_16x16x32_bf16 v[220:223], v[72:75], a[104:107], v[220:223]\n"
    "v_exp_f32 v22, v22\n"
    "v_mfma_f32_16x16x32_bf16 v[220:223], v[76:79], a[108:111], v[220:223]\n"
    "ds_read_b128 a[72:75], v4 offset:34816\n"
    "v_mfma_f32_16x16x32_bf16 v[216:219], v[68:71], a[108:111], v[216:219]\n"
    "s_add_u32 s62, s62, 0x8000\n"
    "v_exp_f32 v23, v23\n"
    "v_mfma_f32_16x16x32_bf16 v[216:219], v[64:67], a[104:107], v[216:219]\n"
    "v_cvt_pk_bf16_f32 v92, v20, v21\n"
    "v_cvt_pk_bf16_f32 v93, v22, v23\n"
    "v_mfma_f32_16x16x32_bf16 v[212:215], v[56:59], a[104:107], v[212:215]\n"
    "s_add_u32 m0, s69, 98304\n"
    "v_exp_f32 v24, v24\n"
    "v_mfma_f32_16x16x32_bf16 v[212:215], v[60:63], a[108:111], v[212:215]\n"
    "ds_read_b128 a[76:79], v4 offset:35840\n"
    "v_accvgpr_read_b32 v2, a155\n"
    "v_mfma_f32_16x16x32_bf16 v[208:211], v[52:55], a[108:111], v[208:211]\n"
    "v_exp_f32 v25, v25\n"
    "v_mfma_f32_16x16x32_bf16 v[208:211], v[48:51], a[104:107], v[208:211]\n"
    "global_load_dwordx2 v[2:3], v2, s[38:39] sc1\n"
    "v_smfmac_f32_16x16x64_bf16 a[136:139], a[144:147], v[64:71], v10\n"
    "s_waitcnt lgkmcnt(4)\n"
    "v_mfma_f32_16x16x32_bf16 v[16:19], a[80:83], a[0:3], 4.0\n"
    "v_exp_f32 v26, v26\n"
    "v_mfma_f32_16x16x32_bf16 v[16:19], a[84:87], a[4:7], v[16:19]\n"
    "ds_read_b128 a[96:99], v6 offset:49152\n"
    "v_mfma_f32_16x16x32_bf16 v[16:19], a[88:91], a[8:11], v[16:19]\n"
    "v_exp_f32 v27, v27\n"
    "v_mfma_f32_16x16x32_bf16 v[16:19], a[92:95], a[12:15], v[16:19]\n"
    "v_cvt_pk_bf16_f32 v100, v24, v25\n"
    "v_cvt_pk_bf16_f32 v101, v26, v27\n"
    "v_mfma_f32_16x16x32_bf16 v[20:23], a[92:95], a[28:31], 4.0\n"
    "v_exp_f32 v28, v28\n"
    "v_mfma_f32_16x16x32_bf16 v[20:23], a[88:91], a[24:27], v[20:23]\n"
    "ds_read_b128 a[100:103], v6 offset:50176\n"
    "v_mfma_f32_16x16x32_bf16 v[20:23], a[84:87], a[20:23], v[20:23]\n"
    "global_load_lds_dwordx4 v8, s[62:63] offset:0 sc1\n"
    "v_mfma_f32_16x16x32_bf16 v[20:23], a[80:83], a[16:19], v[20:23]\n"
    "v_exp_f32 v29, v29\n"
    "v_mfma_f32_16x16x32_bf16 v[24:27], a[80:83], a[32:35], 4.0\n"
    "v_exp_f32 v30, v30\n"
    "v_mfma_f32_16x16x32_bf16 v[24:27], a[84:87], a[36:39], v[24:27]\n"
    "ds_read_b128 a[104:107], v6 offset:51200\n"
    "v_mfma_f32_16x16x32_bf16 v[24:27], a[88:91], a[40:43], v[24:27]\n"
    "v_exp_f32 v31, v31\n"
    "v_mfma_f32_16x16x32_bf16 v[24:27], a[92:95], a[44:47], v[24:27]\n"
    "v_cvt_pk_bf16_f32 v108, v28, v29\n"
    "v_cvt_pk_bf16_f32 v109, v30, v31\n"
    "v_mfma_f32_16x16x32_bf16 v[28:31], a[92:95], a[60:63], 4.0\n"
    "v_exp_f32 v16, v16\n"
    "v_mfma_f32_16x16x32_bf16 v[28:31], a[88:91], a[56:59], v[28:31]\n"
    "ds_read_b128 a[108:111], v6 offset:52224\n"
    "v_mfma_f32_16x16x32_bf16 v[28:31], a[84:87], a[52:55], v[28:31]\n"
    "v_exp_f32 v17, v17\n"
    "v_mfma_f32_16x16x32_bf16 v[28:31], a[80:83], a[48:51], v[28:31]\n"
    "v_mfma_f32_16x16x32_bf16 v[224:227], v[48:51], a[112:115], v[224:227]\n"
    "v_exp_f32 v18, v18\n"
    "v_mfma_f32_16x16x32_bf16 v[224:227], v[52:55], a[116:119], v[224:227]\n"
    "ds_read_b128 a[80:83], v4 offset:36864\n"
    "v_mfma_f32_16x16x32_bf16 v[228:231], v[60:63], a[116:119], v[228:231]\n"
    "v_exp_f32 v19, v19\n"
    "v_mfma_f32_16x16x32_bf16 v[228:231], v[56:59], a[112:115], v[228:231]\n"
    "v_cvt_pk_bf16_f32 v86, v16, v17\n"
    "v_cvt_pk_bf16_f32 v87, v18, v19\n"
    "v_mfma_f32_16x16x32_bf16 v[232:235], v[64:67], a[112:115], v[232:235]\n"
    "v_exp_f32 v20, v20\n"
    "v_mfma_f32_16x16x32_bf16 v[232:235], v[68:71], a[116:119], v[232:235]\n"
    "ds_read_b128 a[84:87], v4 offset:37888\n"
    "v_mfma_f32_16x16x32_bf16 v[236:239], v[76:79], a[116:119], v[236:239]\n"
    "global_load_lds_dwordx4 v8, s[62:63] offset:1024 sc1\n"
    "v_mfma_f32_16x16x32_bf16 v[236:239], v[72:75], a[112:115], v[236:239]\n"
    "v_exp_f32 v21, v21\n"
    "v_mfma_f32_16x16x32_bf16 v[252:255], v[72:75], a[120:123], v[252:255]\n"
    "v_exp_f32 v22, v22\n"
    "v_mfma_f32_16x16x32_bf16 v[252:255], v[76:79], a[124:127], v[252:255]\n"
    "ds_read_b128 a[88:91], v4 offset:38912\n"
    "v_mfma_f32_16x16x32_bf16 v[248:251], v[68:71], a[124:127], v[248:251]\n"
    "v_exp_f32 v23, v23\n"
    "v_mfma_f32_16x16x32_bf16 v[248:251], v[64:67], a[120:123], v[248:251]\n"
    "v_cvt_pk_bf16_f32 v94, v20, v21\n"
    "v_cvt_pk_bf16_f32 v95, v22, v23\n"
    "v_mfma_f32_16x16x32_bf16 v[244:247], v[56:59], a[120:123], v[244:247]\n"
    "v_exp_f32 v24, v24\n"
    "v_mfma_f32_16x16x32_bf16 v[244:247], v[60:63], a[124:127], v[244:247]\n"
    "ds_read_b128 a[92:95], v4 offset:39936\n"
    "v_mfma_f32_16x16x32_bf16 v[240:243], v[52:55], a[124:127], v[240:243]\n"
    "v_exp_f32 v25, v25\n"
    "v_mfma_f32_16x16x32_bf16 v[240:243], v[48:51], a[120:123], v[240:243]\n"
    "v_smfmac_f32_16x16x64_bf16 a[140:143], a[144:147], v[72:79], v10\n"
    "s_waitcnt lgkmcnt(4)\n"
    "v_mfma_f32_16x16x32_bf16 v[16:19], a[64:67], a[0:3], 4.0\n"
    "v_exp_f32 v26, v26\n"
    "v_mfma_f32_16x16x32_bf16 v[16:19], a[68:71], a[4:7], v[16:19]\n"
    "ds_read_b128 a[112:115], v6 offset:53248\n"
    "v_mfma_f32_16x16x32_bf16 v[16:19], a[72:75], a[8:11], v[16:19]\n"
    "v_exp_f32 v27, v27\n"
    "v_mfma_f32_16x16x32_bf16 v[16:19], a[76:79], a[12:15], v[16:19]\n"
    "v_cvt_pk_bf16_f32 v102, v24, v25\n"
    "v_cvt_pk_bf16_f32 v103, v26, v27\n"
    "v_mfma_f32_16x16x32_bf16 v[20:23], a[76:79], a[28:31], 4.0\n"
    "v_exp_f32 v28, v28\n"
    "v_mfma_f32_16x16x32_bf16 v[20:23], a[72:75], a[24:27], v[20:23]\n"
    "ds_read_b128 a[116:119], v6 offset:54272\n"
    "v_mfma_f32_16x16x32_bf16 v[20:23], a[68:71], a[20:23], v[20:23]\n"
    "global_load_lds_dwordx4 v8, s[62:63] offset:2048 sc1\n"
    "v_mfma_f32_16x16x32_bf16 v[20:23], a[64:67], a[16:19], v[20:23]\n"
    "v_exp_f32 v29, v29\n"
    "v_mfma_f32_16x16x32_bf16 v[24:27], a[64:67], a[32:35], 4.0\n"
    "v_exp_f32 v30, v30\n"
    "v_mfma_f32_16x16x32_bf16 v[24:27], a[68:71], a[36:39], v[24:27]\n"
    "ds_read_b128 a[120:123], v6 offset:55296\n"
    "v_mfma_f32_16x16x32_bf16 v[24:27], a[72:75], a[40:43], v[24:27]\n"
    "v_exp_f32 v31, v31\n"
    "v_mfma_f32_16x16x32_bf16 v[24:27], a[76:79], a[44:47], v[24:27]\n"
    "v_cvt_pk_bf16_f32 v110, v28, v29\n"
    "v_cvt_pk_bf16_f32 v111, v30, v31\n"
    "v_mfma_f32_16x16x32_bf16 v[28:31], a[76:79], a[60:63], 4.0\n"
    "v_exp_f32 v16, v16\n"
    "v_mfma_f32_16x16x32_bf16 v[28:31], a[72:75], a[56:59], v[28:31]\n"
    "ds_read_b128 a[124:127], v6 offset:56320\n"
    "v_mfma_f32_16x16x32_bf16 v[28:31], a[68:71], a[52:55], v[28:31]\n"
    "v_exp_f32 v17, v17\n"
    "v_mfma_f32_16x16x32_bf16 v[28:31], a[64:67], a[48:51], v[28:31]\n"
    "v_mfma_f32_16x16x32_bf16 v[128:131], v[80:83], a[96:99], v[128:131]\n"
    "v_exp_f32 v18, v18\n"
    "v_mfma_f32_16x16x32_bf16 v[128:131], v[84:87], a[100:103], v[128:131]\n"
    "ds_read_b128 a[64:67], v4 offset:40960\n"
    "v_mfma_f32_16x16x32_bf16 v[132:135], v[92:95], a[100:103], v[132:135]\n"
    "v_exp_f32 v19, v19\n"
    "v_mfma_f32_16x16x32_bf16 v[132:135], v[88:91], a[96:99], v[132:135]\n"
    "v_cvt_pk_bf16_f32 v48, v16, v17\n"
    "v_cvt_pk_bf16_f32 v49, v18, v19\n"
    "v_mfma_f32_16x16x32_bf16 v[136:139], v[96:99], a[96:99], v[136:139]\n"
    "v_exp_f32 v20, v20\n"
    "v_mfma_f32_16x16x32_bf16 v[136:139], v[100:103], a[100:103], v[136:139]\n"
    "ds_read_b128 a[68:71], v4 offset:41984\n"
    "v_mfma_f32_16x16x32_bf16 v[140:143], v[108:111], a[100:103], v[140:143]\n"
    "global_load_lds_dwordx4 v8, s[62:63] offset:3072 sc1\n"
    "v_mfma_f32_16x16x32_bf16 v[140:143], v[104:107], a[96:99], v[140:143]\n"
    "v_exp_f32 v21, v21\n"
    "v_mfma_f32_16x16x32_bf16 v[156:159], v[104:107], a[104:107], v[156:159]\n"
    "v_exp_f32 v22, v22\n"
    "v_mfma_f32_16x16x32_bf16 v[156:159], v[108:111], a[108:111], v[156:159]\n"
    "ds_read_b128 a[72:75], v4 offset:43008\n"
    "v_mfma_f32_16x16x32_bf16 v[152:155], v[100:103], a[108:111], v[152:155]\n"
    "v_exp_f32 v23, v23\n"
    "v_mfma_f32_16x16x32_bf16 v[152:155], v[96:99], a[104:107], v[152:155]\n"
    "v_cvt_pk_bf16_f32 v56, v20, v21\n"
    "v_cvt_pk_bf16_f32 v57, v22, v23\n"
    "v_mfma_f32_16x16x32_bf16 v[148:151], v[88:91], a[104:107], v[148:151]\n"
    "s_add_u32 m0, s69, 114688\n"
    "v_exp_f32 v24, v24\n"
    "v_mfma_f32_16x16x32_bf16 v[148:151], v[92:95], a[108:111], v[148:151]\n"
    "ds_read_b128 a[76:79], v4 offset:44032\n"
    "v_mfma_f32_16x16x32_bf16 v[144:147], v[84:87], a[108:111], v[144:147]\n"
    "v_exp_f32 v25, v25\n"
    "v_mfma_f32_16x16x32_bf16 v[144:147], v[80:83], a[104:107], v[144:147]\n"
    "v_smfmac_f32_16x16x64_bf16 a[128:131], a[144:147], v[80:87], v10\n"
    "s_waitcnt lgkmcnt(4)\n"
    "v_mfma_f32_16x16x32_bf16 v[16:19], a[80:83], a[0:3], 4.0\n"
    "v_exp_f32 v26, v26\n"
    "v_mfma_f32_16x16x32_bf16 v[16:19], a[84:87], a[4:7], v[16:19]\n"
    "ds_read_b128 a[96:99], v6 offset:57344\n"
    "v_mfma_f32_16x16x32_bf16 v[16:19], a[88:91], a[8:11], v[16:19]\n"
    "v_exp_f32 v27, v27\n"
    "v_mfma_f32_16x16x32_bf16 v[16:19], a[92:95], a[12:15], v[16:19]\n"
    "v_cvt_pk_bf16_f32 v64, v24, v25\n"
    "v_cvt_pk_bf16_f32 v65, v26, v27\n"
    "v_mfma_f32_16x16x32_bf16 v[20:23], a[92:95], a[28:31], 4.0\n"
    "v_exp_f32 v28, v28\n"
    "v_mfma_f32_16x16x32_bf16 v[20:23], a[88:91], a[24:27], v[20:23]\n"
    "ds_read_b128 a[100:103], v6 offset:58368\n"
    "v_mfma_f32_16x16x32_bf16 v[20:23], a[84:87], a[20:23], v[20:23]\n"
    "global_load_lds_dwordx4 v9, s[62:63] offset:0 sc1\n"
    "v_mfma_f32_16x16x32_bf16 v[20:23], a[80:83], a[16:19], v[20:23]\n"
    "v_exp_f32 v29, v29\n"
    "v_mfma_f32_16x16x32_bf16 v[24:27], a[80:83], a[32:35], 4.0\n"
    "v_exp_f32 v30, v30\n"
    "v_mfma_f32_16x16x32_bf16 v[24:27], a[84:87], a[36:39], v[24:27]\n"
    "ds_read_b128 a[104:107], v6 offset:59392\n"
    "v_mfma_f32_16x16x32_bf16 v[24:27], a[88:91], a[40:43], v[24:27]\n"
    "v_exp_f32 v31, v31\n"
    "v_mfma_f32_16x16x32_bf16 v[24:27], a[92:95], a[44:47], v[24:27]\n"
    "v_cvt_pk_bf16_f32 v72, v28, v29\n"
    "v_cvt_pk_bf16_f32 v73, v30, v31\n"
    "v_mfma_f32_16x16x32_bf16 v[28:31], a[92:95], a[60:63], 4.0\n"
    "v_exp_f32 v16, v16\n"
    "v_mfma_f32_16x16x32_bf16 v[28:31], a[88:91], a[56:59], v[28:31]\n"
    "ds_read_b128 a[108:111], v6 offset:60416\n"
    "v_mfma_f32_16x16x32_bf16 v[28:31], a[84:87], a[52:55], v[28:31]\n"
    "v_exp_f32 v17, v17\n"
    "v_mfma_f32_16x16x32_bf16 v[28:31], a[80:83], a[48:51], v[28:31]\n"
    "v_mfma_f32_16x16x32_bf16 v[160:163], v[80:83], a[112:115], v[160:163]\n"
    "v_exp_f32 v18, v18\n"
    "v_mfma_f32_16x16x32_bf16 v[160:163], v[84:87], a[116:119], v[160:163]\n"
    "ds_read_b128 a[80:83], v4 offset:45056\n"
    "v_mfma_f32_16x16x32_bf16 v[164:167], v[92:95], a[116:119], v[164:167]\n"
    "v_exp_f32 v19, v19\n"
    "v_mfma_f32_16x16x32_bf16 v[164:167], v[88:91], a[112:115], v[164:167]\n"
    "v_cvt_pk_bf16_f32 v50, v16, v17\n"
    "v_cvt_pk_bf16_f32 v51, v18, v19\n"
    "v_mfma_f32_16x16x32_bf16 v[168:171], v[96:99], a[112:115], v[168:171]\n"
    "v_exp_f32 v20, v20\n"
    "v_mfma_f32_16x16x32_bf16 v[168:171], v[100:103], a[116:119], v[168:171]\n"
    "ds_read_b128 a[84:87], v4 offset:46080\n"
    "v_mfma_f32_16x16x32_bf16 v[172:175], v[108:111], a[116:119], v[172:175]\n"
    "global_load_lds_dwordx4 v9, s[62:63] offset:1024 sc1\n"
    "v_mfma_f32_16x16x32_bf16 v[172:175], v[104:107], a[112:115], v[172:175]\n"
    "v_exp_f32 v21, v21\n"
    "v_mfma_f32_16x16x32_bf16 v[188:191], v[104:107], a[120:123], v[188:191]\n"
    "v_exp_f32 v22, v22\n"
    "v_mfma_f32_16x16x32_bf16 v[188:191], v[108:111], a[124:127], v[188:191]\n"
    "ds_read_b128 a[88:91], v4 offset:47104\n"
    "v_mfma_f32_16x16x32_bf16 v[184:187], v[100:103], a[124:127], v[184:187]\n"
    "v_exp_f32 v23, v23\n"
    "v_mfma_f32_16x16x32_bf16 v[184:187], v[96:99], a[120:123], v[184:187]\n"
    "v_cvt_pk_bf16_f32 v58, v20, v21\n"
    "v_cvt_pk_bf16_f32 v59, v22, v23\n"
    "v_mfma_f32_16x16x32_bf16 v[180:183], v[88:91], a[120:123], v[180:183]\n"
    "v_exp_f32 v24, v24\n"
    "v_mfma_f32_16x16x32_bf16 v[180:183], v[92:95], a[124:127], v[180:183]\n"
    "ds_read_b128 a[92:95], v4 offset:48128\n"
    "v_mfma_f32_16x16x32_bf16 v[176:179], v[84:87], a[124:127], v[176:179]\n"
    "v_exp_f32 v25, v25\n"
    "v_mfma_f32_16x16x32_bf16 v[176:179], v[80:83], a[120:123], v[176:179]\n"
    "v_smfmac_f32_16x16x64_bf16 a[132:135], a[144:147], v[88:95], v10\n"
    "s_waitcnt lgkmcnt(4)\n"
    "v_mfma_f32_16x16x32_bf16 v[16:19], a[64:67], a[0:3], 4.0\n"
    "s_bitset0_b32 s71, 2\n"
    "v_exp_f32 v26, v26\n"
    "v_mfma_f32_16x16x32_bf16 v[16:19], a[68:71], a[4:7], v[16:19]\n"
    "ds_read_b128 a[112:115], v6 offset:61440\n"
    "v_mfma_f32_16x16x32_bf16 v[16:19], a[72:75], a[8:11], v[16:19]\n"
    "v_exp_f32 v27, v27\n"
    "v_mfma_f32_16x16x32_bf16 v[16:19], a[76:79], a[12:15], v[16:19]\n"
    "v_cvt_pk_bf16_f32 v66, v24, v25\n"
    "v_cvt_pk_bf16_f32 v67, v26, v27\n"
    "v_mfma_f32_16x16x32_bf16 v[20:23], a[76:79], a[28:31], 4.0\n"
    "v_exp_f32 v28, v28\n"
    "v_mfma_f32_16x16x32_bf16 v[20:23], a[72:75], a[24:27], v[20:23]\n"
    "ds_read_b128 a[116:119], v6 offset:62464\n"
    "v_mfma_f32_16x16x32_bf16 v[20:23], a[68:71], a[20:23], v[20:23]\n"
    "global_load_lds_dwordx4 v9, s[62:63] offset:2048 sc1\n"
    "v_mfma_f32_16x16x32_bf16 v[20:23], a[64:67], a[16:19], v[20:23]\n"
    "v_exp_f32 v29, v29\n"
    "v_mfma_f32_16x16x32_bf16 v[24:27], a[64:67], a[32:35], 4.0\n"
    "v_exp_f32 v30, v30\n"
    "v_mfma_f32_16x16x32_bf16 v[24:27], a[68:71], a[36:39], v[24:27]\n"
    "ds_read_b128 a[120:123], v6 offset:63488\n"
    "v_mfma_f32_16x16x32_bf16 v[24:27], a[72:75], a[40:43], v[24:27]\n"
    "v_exp_f32 v31, v31\n"
    "v_mfma_f32_16x16x32_bf16 v[24:27], a[76:79], a[44:47], v[24:27]\n"
    "v_cvt_pk_bf16_f32 v74, v28, v29\n"
    "v_cvt_pk_bf16_f32 v75, v30, v31\n"
    "v_mfma_f32_16x16x32_bf16 v[28:31], a[76:79], a[60:63], 4.0\n"
    "v_exp_f32 v16, v16\n"
    "v_mfma_f32_16x16x32_bf16 v[28:31], a[72:75], a[56:59], v[28:31]\n"
    "ds_read_b128 a[124:127], v6 offset:64512\n"
    "v_mfma_f32_16x16x32_bf16 v[28:31], a[68:71], a[52:55], v[28:31]\n"
    "s_waitcnt vmcnt(7)\n"
    "v_exp_f32 v17, v17\n"
    "v_mfma_f32_16x16x32_bf16 v[28:31], a[64:67], a[48:51], v[28:31]\n"
    "s_barrier\n"
    "v_mfma_f32_16x16x32_bf16 v[192:195], v[80:83], a[96:99], v[192:195]\n"
    "v_exp_f32 v18, v18\n"
    "v_mfma_f32_16x16x32_bf16 v[192:195], v[84:87], a[100:103], v[192:195]\n"
    "ds_read_b128 a[64:67], v5 offset:0\n"
    "v_cmp_ne_u64 vcc, s[72:73], v[2:3]\n"
    "v_mfma_f32_16x16x32_bf16 v[196:199], v[92:95], a[100:103], v[196:199]\n"
    "v_exp_f32 v19, v19\n"
    "v_mfma_f32_16x16x32_bf16 v[196:199], v[88:91], a[96:99], v[196:199]\n"
    "v_cvt_pk_bf16_f32 v52, v16, v17\n"
    "v_cvt_pk_bf16_f32 v53, v18, v19\n"
    "v_mfma_f32_16x16x32_bf16 v[200:203], v[96:99], a[96:99], v[200:203]\n"
    "v_exp_f32 v20, v20\n"
    "v_mfma_f32_16x16x32_bf16 v[200:203], v[100:103], a[100:103], v[200:203]\n"
    "ds_read_b128 a[68:71], v5 offset:1024\n"
    "v_mfma_f32_16x16x32_bf16 v[204:207], v[108:111], a[100:103], v[204:207]\n"
    "global_load_lds_dwordx4 v9, s[62:63] offset:3072 sc1\n"
    "v_mfma_f32_16x16x32_bf16 v[204:207], v[104:107], a[96:99], v[204:207]\n"
    "v_exp_f32 v21, v21\n"
    "v_mfma_f32_16x16x32_bf16 v[220:223], v[104:107], a[104:107], v[220:223]\n"
    "v_exp_f32 v22, v22\n"
    "v_mfma_f32_16x16x32_bf16 v[220:223], v[108:111], a[108:111], v[220:223]\n"
    "ds_read_b128 a[72:75], v5 offset:2048\n"
    "v_mfma_f32_16x16x32_bf16 v[216:219], v[100:103], a[108:111], v[216:219]\n"
    "s_add_u32 s62, s62, 0x8000\n"
    "v_exp_f32 v23, v23\n"
    "v_mfma_f32_16x16x32_bf16 v[216:219], v[96:99], a[104:107], v[216:219]\n"
    "L%=_w4:\n"
    "s_cbranch_execz L%=_w8\n"
    "v_cvt_pk_bf16_f32 v60, v20, v21\n"
    "v_cvt_pk_bf16_f32 v61, v22, v23\n"
    "v_mfma_f32_16x16x32_bf16 v[212:215], v[88:91], a[104:107], v[212:215]\n"
    "s_add_u32 m0, s69, 0\n"
    "v_exp_f32 v24, v24\n"
    "v_mfma_f32_16x16x32_bf16 v[212:215], v[92:95], a[108:111], v[212:215]\n"
    "ds_read_b128 a[76:79], v5 offset:3072\n"
    "v_mfma_f32_16x16x32_bf16 v[208:211], v[84:87], a[108:111], v[208:211]\n"
    "s_cbranch_vccnz L%=_p3_retry\n"
    "L%=_p3_ok:\n"
    "v_exp_f32 v25, v25\n"
    "v_mfma_f32_16x16x32_bf16 v[208:211], v[80:83], a[104:107], v[208:211]\n"
    "v_smfmac_f32_16x16x64_bf16 a[136:139], a[144:147], v[96:103], v10\n"
    "s_waitcnt lgkmcnt(4)\n"
    "v_mfma_f32_16x16x32_bf16 v[16:19], a[80:83], a[0:3], 4.0\n"
    "s_add_u32 s38, s38, 0x100\n"
    "v_exp_f32 v26, v26\n"
    "v_mfma_f32_16x16x32_bf16 v[16:19], a[84:87], a[4:7], v[16:19]\n"
    "ds_read_b128 a[96:99], v7 offset:16384\n"
    "v_mfma_f32_16x16x32_bf16 v[16:19], a[88:91], a[8:11], v[16:19]\n"
    "v_exp_f32 v27, v27\n"
    "v_mfma_f32_16x16x32_bf16 v[16:19], a[92:95], a[12:15], v[16:19]\n"
    "v_cvt_pk_bf16_f32 v68, v24, v25\n"
    "v_cvt_pk_bf16_f32 v69, v26, v27\n"
    "v_mfma_f32_16x16x32_bf16 v[20:23], a[92:95], a[28:31], 4.0\n"
    "v_exp_f32 v28, v28\n"
    "v_mfma_f32_16x16x32_bf16 v[20:23], a[88:91], a[24:27], v[20:23]\n"
    "ds_read_b128 a[100:103], v7 offset:17408\n"
    "v_mfma_f32_16x16x32_bf16 v[20:23], a[84:87], a[20:23], v[20:23]\n"
    "global_load_lds_dwordx4 v8, s[62:63] offset:0 sc1\n"
    "v_mfma_f32_16x16x32_bf16 v[20:23], a[80:83], a[16:19], v[20:23]\n"
    "v_exp_f32 v29, v29\n"
    "v_mfma_f32_16x16x32_bf16 v[24:27], a[80:83], a[32:35], 4.0\n"
    "v_exp_f32 v30, v30\n"
    "v_mfma_f32_16x16x32_bf16 v[24:27], a[84:87], a[36:39], v[24:27]\n"
    "ds_read_b128 a[104:107], v7 offset:18432\n"
    "v_mfma_f32_16x16x32_bf16 v[24:27], a[88:91], a[40:43], v[24:27]\n"
    "v_exp_f32 v31, v31\n"
    "v_mfma_f32_16x16x32_bf16 v[24:27], a[92:95], a[44:47], v[24:27]\n"
    "v_cvt_pk_bf16_f32 v76, v28, v29\n"
    "v_cvt_pk_bf16_f32 v77, v30, v31\n"
    "v_mfma_f32_16x16x32_bf16 v[28:31], a[92:95], a[60:63], 4.0\n"
    "v_exp_f32 v16, v16\n"
    "v_mfma_f32_16x16x32_bf16 v[28:31], a[88:91], a[56:59], v[28:31]\n"
    "ds_read_b128 a[108:111], v7 offset:19456\n"
    "v_mfma_f32_16x16x32_bf16 v[28:31], a[84:87], a[52:55], v[28:31]\n"
    "v_exp_f32 v17, v17\n"
    "v_mfma_f32_16x16x32_bf16 v[28:31], a[80:83], a[48:51], v[28:31]\n"
    "v_mfma_f32_16x16x32_bf16 v[224:227], v[80:83], a[112:115], v[224:227]\n"
    "v_exp_f32 v18, v18\n"
    "v_mfma_f32_16x16x32_bf16 v[224:227], v[84:87], a[116:119], v[224:227]\n"
    "ds_read_b128 a[80:83], v5 offset:4096\n"
    "v_mfma_f32_16x16x32_bf16 v[228:231], v[92:95], a[116:119], v[228:231]\n"
    "v_exp_f32 v19, v19\n"
    "v_mfma_f32_16x16x32_bf16 v[228:231], v[88:91], a[112:115], v[228:231]\n"
    "v_cvt_pk_bf16_f32 v54, v16, v17\n"
    "v_cvt_pk_bf16_f32 v55, v18, v19\n"
    "v_mfma_f32_16x16x32_bf16 v[232:235], v[96:99], a[112:115], v[232:235]\n"
    "v_exp_f32 v20, v20\n"
    "v_mfma_f32_16x16x32_bf16 v[232:235], v[100:103], a[116:119], v[232:235]\n"
    "ds_read_b128 a[84:87], v5 offset:5120\n"
    "v_mfma_f32_16x16x32_bf16 v[236:239], v[108:111], a[116:119], v[236:239]\n"
    "global_load_lds_dwordx4 v8, s[62:63] offset:1024 sc1\n"
    "v_mfma_f32_16x16x32_bf16 v[236:239], v[104:107], a[112:115], v[236:239]\n"
    "v_exp_f32 v21, v21\n"
    "v_mfma_f32_16x16x32_bf16 v[252:255], v[104:107], a[120:123], v[252:255]\n"
    "v_exp_f32 v22, v22\n"
    "v_mfma_f32_16x16x32_bf16 v[252:255], v[108:111], a[124:127], v[252:255]\n"
    "ds_read_b128 a[88:91], v5 offset:6144\n"
    "v_mfma_f32_16x16x32_bf16 v[248:251], v[100:103], a[124:127], v[248:251]\n"
    "v_exp_f32 v23, v23\n"
    "v_mfma_f32_16x16x32_bf16 v[248:251], v[96:99], a[120:123], v[248:251]\n"
    "v_cvt_pk_bf16_f32 v62, v20, v21\n"
    "v_cvt_pk_bf16_f32 v63, v22, v23\n"
    "v_mfma_f32_16x16x32_bf16 v[244:247], v[88:91], a[120:123], v[244:247]\n"
    "v_exp_f32 v24, v24\n"
    "v_mfma_f32_16x16x32_bf16 v[244:247], v[92:95], a[124:127], v[244:247]\n"
    "ds_read_b128 a[92:95], v5 offset:7168\n"
    "v_mfma_f32_16x16x32_bf16 v[240:243], v[84:87], a[124:127], v[240:243]\n"
    "v_exp_f32 v25, v25\n"
    "v_mfma_f32_16x16x32_bf16 v[240:243], v[80:83], a[120:123], v[240:243]\n"
    "v_smfmac_f32_16x16x64_bf16 a[140:143], a[144:147], v[104:111], v10\n"
    "s_waitcnt lgkmcnt(4)\n"
    "v_mfma_f32_16x16x32_bf16 v[16:19], a[64:67], a[0:3], 4.0\n"
    "v_exp_f32 v26, v26\n"
    "v_mfma_f32_16x16x32_bf16 v[16:19], a[68:71], a[4:7], v[16:19]\n"
    "ds_read_b128 a[112:115], v7 offset:20480\n"
    "v_mfma_f32_16x16x32_bf16 v[16:19], a[72:75], a[8:11], v[16:19]\n"
    "v_exp_f32 v27, v27\n"
    "v_mfma_f32_16x16x32_bf16 v[16:19], a[76:79], a[12:15], v[16:19]\n"
    "v_cvt_pk_bf16_f32 v70, v24, v25\n"
    "v_cvt_pk_bf16_f32 v71, v26, v27\n"
    "v_mfma_f32_16x16x32_bf16 v[20:23], a[76:79], a[28:31], 4.0\n"
    "v_exp_f32 v28, v28\n"
    "v_mfma_f32_16x16x32_bf16 v[20:23], a[72:75], a[24:27], v[20:23]\n"
    "ds_read_b128 a[116:119], v7 offset:21504\n"
    "v_mfma_f32_16x16x32_bf16 v[20:23], a[68:71], a[20:23], v[20:23]\n"
    "global_load_lds_dwordx4 v8, s[62:63] offset:2048 sc1\n"
    "v_mfma_f32_16x16x32_bf16 v[20:23], a[64:67], a[16:19], v[20:23]\n"
    "v_exp_f32 v29, v29\n"
    "v_mfma_f32_16x16x32_bf16 v[24:27], a[64:67], a[32:35], 4.0\n"
    "v_exp_f32 v30, v30\n"
    "v_mfma_f32_16x16x32_bf16 v[24:27], a[68:71], a[36:39], v[24:27]\n"
    "ds_read_b128 a[120:123], v7 offset:22528\n"
    "v_mfma_f32_16x16x32_bf16 v[24:27], a[72:75], a[40:43], v[24:27]\n"
    "v_exp_f32 v31, v31\n"
    "v_mfma_f32_16x16x32_bf16 v[24:27], a[76:79], a[44:47], v[24:27]\n"
    "v_cvt_pk_bf16_f32 v78, v28, v29\n"
    "v_cvt_pk_bf16_f32 v79, v30, v31\n"
    "v_mfma_f32_16x16x32_bf16 v[28:31], a[76:79], a[60:63], 4.0\n"
    "v_exp_f32 v16, v16\n"
    "v_mfma_f32_16x16x32_bf16 v[28:31], a[72:75], a[56:59], v[28:31]\n"
    "ds_read_b128 a[124:127], v7 offset:23552\n"
    "v_mfma_f32_16x16x32_bf16 v[28:31], a[68:71], a[52:55], v[28:31]\n"
    "v_exp_f32 v17, v17\n"
    "v_mfma_f32_16x16x32_bf16 v[28:31], a[64:67], a[48:51], v[28:31]\n"
    "v_mfma_f32_16x16x32_bf16 v[128:131], v[48:51], a[96:99], v[128:131]\n"
    "v_exp_f32 v18, v18\n"
    "v_mfma_f32_16x16x32_bf16 v[128:131], v[52:55], a[100:103], v[128:131]\n"
    "ds_read_b128 a[64:67], v5 offset:8192\n"
    "v_mfma_f32_16x16x32_bf16 v[132:135], v[60:63], a[100:103], v[132:135]\n"
    "v_exp_f32 v19, v19\n"
    "v_mfma_f32_16x16x32_bf16 v[132:135], v[56:59], a[96:99], v[132:135]\n"
    "v_cvt_pk_bf16_f32 v80, v16, v17\n"
    "v_cvt_pk_bf16_f32 v81, v18, v19\n"
    "v_mfma_f32_16x16x32_bf16 v[136:139], v[64:67], a[96:99], v[136:139]\n"
    "v_exp_f32 v20, v20\n"
    "v_mfma_f32_16x16x32_bf16 v[136:139], v[68:71], a[100:103], v[136:139]\n"
    "ds_read_b128 a[68:71], v5 offset:9216\n"
    "v_mfma_f32_16x16x32_bf16 v[140:143], v[76:79], a[100:103], v[140:143]\n"
    "global_load_lds_dwordx4 v8, s[62:63] offset:3072 sc1\n"
    "v_mfma_f32_16x16x32_bf16 v[140:143], v[72:75], a[96:99], v[140:143]\n"
    "v_exp_f32 v21, v21\n"
    "v_mfma_f32_16x16x32_bf16 v[156:159], v[72:75], a[104:107], v[156:159]\n"
    "v_exp_f32 v22, v22\n"
    "v_mfma_f32_16x16x32_bf16 v[156:159], v[76:79], a[108:111], v[156:159]\n"
    "ds_read_b128 a[72:75], v5 offset:10240\n"
    "v_mfma_f32_16x16x32_bf16 v[152:155], v[68:71], a[108:111], v[152:155]\n"
    "v_exp_f32 v23, v23\n"
    "v_mfma_f32_16x16x32_bf16 v[152:155], v[64:67], a[104:107], v[152:155]\n"
    "v_cvt_pk_bf16_f32 v88, v20, v21\n"
    "v_cvt_pk_bf16_f32 v89, v22, v23\n"
    "v_mfma_f32_16x16x32_bf16 v[148:151], v[56:59], a[104:107], v[148:151]\n"
    "s_add_u32 m0, s69, 16384\n"
    "v_exp_f32 v24, v24\n"
    "v_mfma_f32_16x16x32_bf16 v[148:151], v[60:63], a[108:111], v[148:151]\n"
    "ds_read_b128 a[76:79], v5 offset:11264\n"
    "v_mfma_f32_16x16x32_bf16 v[144:147], v[52:55], a[108:111], v[144:147]\n"
    "v_exp_f32 v25, v25\n"
    "v_mfma_f32_16x16x32_bf16 v[144:147], v[48:51], a[104:107], v[144:147]\n"
    "v_smfmac_f32_16x16x64_bf16 a[128:131], a[144:147], v[48:55], v10\n"
    "s_waitcnt lgkmcnt(4)\n"
    "v_mfma_f32_16x16x32_bf16 v[16:19], a[80:83], a[0:3], 4.0\n"
    "v_exp_f32 v26, v26\n"
    "v_mfma_f32_16x16x32_bf16 v[16:19], a[84:87], a[4:7], v[16:19]\n"
    "ds_read_b128 a[96:99], v7 offset:24576\n"
    "v_mfma_f32_16x16x32_bf16 v[16:19], a[88:91], a[8:11], v[16:19]\n"
    "v_exp_f32 v27, v27\n"
    "v_mfma_f32_16x16x32_bf16 v[16:19], a[92:95], a[12:15], v[16:19]\n"
    "v_cvt_pk_bf16_f32 v96, v24, v25\n"
    "v_cvt_pk_bf16_f32 v97, v26, v27\n"
    "v_mfma_f32_16x16x32_bf16 v[20:23], a[92:95], a[28:31], 4.0\n"
    "v_exp_f32 v28, v28\n"
    "v_mfma_f32_16x16x32_bf16 v[20:23], a[88:91], a[24:27], v[20:23]\n"
    "ds_read_b128 a[100:103], v7 offset:25600\n"
    "v_mfma_f32_16x16x32_bf16 v[20:23], a[84:87], a[20:23], v[20:23]\n"
    "global_load_lds_dwordx4 v9, s[62:63] offset:0 sc1\n"
    "v_mfma_f32_16x16x32_bf16 v[20:23], a[80:83], a[16:19], v[20:23]\n"
    "v_exp_f32 v29, v29\n"
    "v_mfma_f32_16x16x32_bf16 v[24:27], a[80:83], a[32:35], 4.0\n"
    "v_exp_f32 v30, v30\n"
    "v_mfma_f32_16x16x32_bf16 v[24:27], a[84:87], a[36:39], v[24:27]\n"
    "ds_read_b128 a[104:107], v7 offset:26624\n"
    "v_mfma_f32_16x16x32_bf16 v[24:27], a[88:91], a[40:43], v[24:27]\n"
    "v_exp_f32 v31, v31\n"
    "v_mfma_f32_16x16x32_bf16 v[24:27], a[92:95], a[44:47], v[24:27]\n"
    "v_cvt_pk_bf16_f32 v104, v28, v29\n"
    "v_cvt_pk_bf16_f32 v105, v30, v31\n"
    "v_mfma_f32_16x16x32_bf16 v[28:31], a[92:95], a[60:63], 4.0\n"
    "v_exp_f32 v16, v16\n"
    "v_mfma_f32_16x16x32_bf16 v[28:31], a[88:91], a[56:59], v[28:31]\n"
    "ds_read_b128 a[108:111], v7 offset:27648\n"
    "v_mfma_f32_16x16x32_bf16 v[28:31], a[84:87], a[52:55], v[28:31]\n"
    "v_exp_f32 v17, v17\n"
    "v_mfma_f32_16x16x32_bf16 v[28:31], a[80:83], a[48:51], v[28:31]\n"
    "v_mfma_f32_16x16x32_bf16 v[160:163], v[48:51], a[112:115], v[160:163]\n"
    "v_exp_f32 v18, v18\n"
    "v_mfma_f32_16x16x32_bf16 v[160:163], v[52:55], a[116:119], v[160:163]\n"
    "ds_read_b128 a[80:83], v5 offset:12288\n"
    "v_mfma_f32_16x16x32_bf16 v[164:167], v[60:63], a[116:119], v[164:167]\n"
    "v_exp_f32 v19, v19\n"
    "v_mfma_f32_16x16x32_bf16 v[164:167], v[56:59], a[112:115], v[164:167]\n"
    "v_cvt_pk_bf16_f32 v82, v16, v17\n"
    "v_cvt_pk_bf16_f32 v83, v18, v19\n"
    "v_mfma_f32_16x16x32_bf16 v[168:171], v[64:67], a[112:115], v[168:171]\n"
    "v_exp_f32 v20, v20\n"
    "v_mfma_f32_16x16x32_bf16 v[168:171], v[68:71], a[116:119], v[168:171]\n"
    "ds_read_b128 a[84:87], v5 offset:13312\n"
    "v_mfma_f32_16x16x32_bf16 v[172:175], v[76:79], a[116:119], v[172:175]\n"
    "global_load_lds_dwordx4 v9, s[62:63] offset:1024 sc1\n"
    "v_mfma_f32_16x16x32_bf16 v[172:175], v[72:75], a[112:115], v[172:175]\n"
    "v_exp_f32 v21, v21\n"
    "v_mfma_f32_16x16x32_bf16 v[188:191], v[72:75], a[120:123], v[188:191]\n"
    "v_exp_f32 v22, v22\n"
    "v_mfma_f32_16x16x32_bf16 v[188:191], v[76:79], a[124:127], v[188:191]\n"
    "ds_read_b128 a[88:91], v5 offset:14336\n"
    "v_mfma_f32_16x16x32_bf16 v[184:187], v[68:71], a[124:127], v[184:187]\n"
    "v_exp_f32 v23, v23\n"
    "v_mfma_f32_16x16x32_bf16 v[184:187], v[64:67], a[120:123], v[184:187]\n"
    "v_cvt_pk_bf16_f32 v90, v20, v21\n"
    "v_cvt_pk_bf16_f32 v91, v22, v23\n"
    "v_mfma_f32_16x16x32_bf16 v[180:183], v[56:59], a[120:123], v[180:183]\n"
    "v_exp_f32 v24, v24\n"
    "v_mfma_f32_16x16x32_bf16 v[180:183], v[60:63], a[124:127], v[180:183]\n"
    "ds_read_b128 a[92:95], v5 offset:15360\n"
    "v_mfma_f32_16x16x32_bf16 v[176:179], v[52:55], a[124:127], v[176:179]\n"
    "v_exp_f32 v25, v25\n"
    "v_mfma_f32_16x16x32_bf16 v[176:179], v[48:51], a[120:123], v[176:179]\n"
    "v_smfmac_f32_16x16x64_bf16 a[132:135], a[144:147], v[56:63], v10\n"
    "s_waitcnt lgkmcnt(4)\n"
    "v_mfma_f32_16x16x32_bf16 v[16:19], a[64:67], a[0:3], 4.0\n"
    "v_exp_f32 v26, v26\n"
    "v_mfma_f32_16x16x32_bf16 v[16:19], a[68:71], a[4:7], v[16:19]\n"
    "ds_read_b128 a[112:115], v7 offset:28672\n"
    "v_mfma_f32_16x16x32_bf16 v[16:19], a[72:75], a[8:11], v[16:19]\n"
    "v_exp_f32 v27, v27\n"
    "v_mfma_f32_16x16x32_bf16 v[16:19], a[76:79], a[12:15], v[16:19]\n"
    "v_cvt_pk_bf16_f32 v98, v24, v25\n"
    "v_cvt_pk_bf16_f32 v99, v26, v27\n"
    "v_mfma_f32_16x16x32_bf16 v[20:23], a[76:79], a[28:31], 4.0\n"
    "v_exp_f32 v28, v28\n"
    "v_mfma_f32_16x16x32_bf16 v[20:23], a[72:75], a[24:27], v[20:23]\n"
    "ds_read_b128 a[116:119], v7 offset:29696\n"
    "v_mfma_f32_16x16x32_bf16 v[20:23], a[68:71], a[20:23], v[20:23]\n"
    "global_load_lds_dwordx4 v9, s[62:63] offset:2048 sc1\n"
    "v_mfma_f32_16x16x32_bf16 v[20:23], a[64:67], a[16:19], v[20:23]\n"
    "v_exp_f32 v29, v29\n"
    "v_mfma_f32_16x16x32_bf16 v[24:27], a[64:67], a[32:35], 4.0\n"
    "v_exp_f32 v30, v30\n"
    "v_mfma_f32_16x16x32_bf16 v[24:27], a[68:71], a[36:39], v[24:27]\n"
    "ds_read_b128 a[120:123], v7 offset:30720\n"
    "v_mfma_f32_16x16x32_bf16 v[24:27], a[72:75], a[40:43], v[24:27]\n"
    "v_exp_f32 v31, v31\n"
    "v_mfma_f32_16x16x32_bf16 v[24:27], a[76:79], a[44:47], v[24:27]\n"
    "v_cvt_pk_bf16_f32 v106, v28, v29\n"
    "v_cvt_pk_bf16_f32 v107, v30, v31\n"
    "v_mfma_f32_16x16x32_bf16 v[28:31], a[76:79], a[60:63], 4.0\n"
    "v_exp_f32 v16, v16\n"
    "v_mfma_f32_16x16x32_bf16 v[28:31], a[72:75], a[56:59], v[28:31]\n"
    "ds_read_b128 a[124:127], v7 offset:31744\n"
    "v_mfma_f32_16x16x32_bf16 v[28:31], a[68:71], a[52:55], v[28:31]\n"
    "s_waitcnt vmcnt(7)\n"
    "v_exp_f32 v17, v17\n"
    "v_mfma_f32_16x16x32_bf16 v[28:31], a[64:67], a[48:51], v[28:31]\n"
    "s_barrier\n"
    "v_mfma_f32_16x16x32_bf16 v[192:195], v[48:51], a[96:99], v[192:195]\n"
    "v_exp_f32 v18, v18\n"
    "v_mfma_f32_16x16x32_bf16 v[192:195], v[52:55], a[100:103], v[192:195]\n"
    "ds_read_b128 a[64:67], v5 offset:32768\n"
    "v_mfma_f32_16x16x32_bf16 v[196:199], v[60:63], a[100:103], v[196:199]\n"
    "v_exp_f32 v19, v19\n"
    "v_mfma_f32_16x16x32_bf16 v[196:199], v[56:59], a[96:99], v[196:199]\n"
    "v_cvt_pk_bf16_f32 v84, v16, v17\n"
    "v_cvt_pk_bf16_f32 v85, v18, v19\n"
    "v_mfma_f32_16x16x32_bf16 v[200:203], v[64:67], a[96:99], v[200:203]\n"
    "v_exp_f32 v20, v20\n"
    "v_mfma_f32_16x16x32_bf16 v[200:203], v[68:71], a[100:103], v[200:203]\n"
    "ds_read_b128 a[68:71], v5 offset:33792\n"
    "v_mfma_f32_16x16x32_bf16 v[204:207], v[76:79], a[100:103], v[204:207]\n"
    "global_load_lds_dwordx4 v9, s[62:63] offset:3072 sc1\n"
    "v_mfma_f32_16x16x32_bf16 v[204:207], v[72:75], a[96:99], v[204:207]\n"
    "v_exp_f32 v21, v21\n"
    "v_mfma_f32_16x16x32_bf16 v[220:223], v[72:75], a[104:107], v[220:223]\n"
    "v_exp_f32 v22, v22\n"
    "v_mfma_f32_16x16x32_bf16 v[220:223], v[76:79], a[108:111], v[220:223]\n"
    "ds_read_b128 a[72:75], v5 offset:34816\n"
    "v_mfma_f32_16x16x32_bf16 v[216:219], v[68:71], a[108:111], v[216:219]\n"
    "s_add_u32 s62, s62, 0x8000\n"
    "v_exp_f32 v23, v23\n"
    "v_mfma_f32_16x16x32_bf16 v[216:219], v[64:67], a[104:107], v[216:219]\n"
    "v_cvt_pk_bf16_f32 v92, v20, v21\n"
    "v_cvt_pk_bf16_f32 v93, v22, v23\n"
    "v_mfma_f32_16x16x32_bf16 v[212:215], v[56:59], a[104:107], v[212:215]\n"
    "s_add_u32 m0, s69, 32768\n"
    "v_exp_f32 v24, v24\n"
    "v_mfma_f32_16x16x32_bf16 v[212:215], v[60:63], a[108:111], v[212:215]\n"
    "ds_read_b128 a[76:79], v5 offset:35840\n"
    "v_mfma_f32_16x16x32_bf16 v[208:211], v[52:55], a[108:111], v[208:211]\n"
    "v_exp_f32 v25, v25\n"
    "v_mfma_f32_16x16x32_bf16 v[208:211], v[48:51], a[104:107], v[208:211]\n"
    "v_smfmac_f32_16x16x64_bf16 a[136:139], a[144:147], v[64:71], v10\n"
    "s_waitcnt lgkmcnt(4)\n"
    "v_mfma_f32_16x16x32_bf16 v[16:19], a[80:83], a[0:3], 4.0\n"
    "v_exp_f32 v26, v26\n"
    "v_mfma_f32_16x16x32_bf16 v[16:19], a[84:87], a[4:7], v[16:19]\n"
    "ds_read_b128 a[96:99], v7 offset:49152\n"
    "v_mfma_f32_16x16x32_bf16 v[16:19], a[88:91], a[8:11], v[16:19]\n"
    "v_exp_f32 v27, v27\n"
    "v_mfma_f32_16x16x32_bf16 v[16:19], a[92:95], a[12:15], v[16:19]\n"
    "v_cvt_pk_bf16_f32 v100, v24, v25\n"
    "v_cvt_pk_bf16_f32 v101, v26, v27\n"
    "v_mfma_f32_16x16x32_bf16 v[20:23], a[92:95], a[28:31], 4.0\n"
    "v_exp_f32 v28, v28\n"
    "v_mfma_f32_16x16x32_bf16 v[20:23], a[88:91], a[24:27], v[20:23]\n"
    "ds_read_b128 a[100:103], v7 offset:50176\n"
    "v_mfma_f32_16x16x32_bf16 v[20:23], a[84:87], a[20:23], v[20:23]\n"
    "global_load_lds_dwordx4 v8, s[62:63] offset:0 sc1\n"
    "v_mfma_f32_16x16x32_bf16 v[20:23], a[80:83], a[16:19], v[20:23]\n"
    "v_exp_f32 v29, v29\n"
    "v_mfma_f32_16x16x32_bf16 v[24:27], a[80:83], a[32:35], 4.0\n"
    "v_exp_f32 v30, v30\n"
    "v_mfma_f32_16x16x32_bf16 v[24:27], a[84:87], a[36:39], v[24:27]\n"
    "ds_read_b128 a[104:107], v7 offset:51200\n"
    "v_mfma_f32_16x16x32_bf16 v[24:27], a[88:91], a[40:43], v[24:27]\n"
    "v_exp_f32 v31, v31\n"
    "v_mfma_f32_16x16x32_bf16 v[24:27], a[92:95], a[44:47], v[24:27]\n"
    "v_cvt_pk_bf16_f32 v108, v28, v29\n"
    "v_cvt_pk_bf16_f32 v109, v30, v31\n"
    "v_mfma_f32_16x16x32_bf16 v[28:31], a[92:95], a[60:63], 4.0\n"
    "v_exp_f32 v16, v16\n"
    "v_mfma_f32_16x16x32_bf16 v[28:31], a[88:91], a[56:59], v[28:31]\n"
    "ds_read_b128 a[108:111], v7 offset:52224\n"
    "v_mfma_f32_16x16x32_bf16 v[28:31], a[84:87], a[52:55], v[28:31]\n"
    "v_exp_f32 v17, v17\n"
    "v_mfma_f32_16x16x32_bf16 v[28:31], a[80:83], a[48:51], v[28:31]\n"
    "v_mfma_f32_16x16x32_bf16 v[224:227], v[48:51], a[112:115], v[224:227]\n"
    "v_exp_f32 v18, v18\n"
    "v_mfma_f32_16x16x32_bf16 v[224:227], v[52:55], a[116:119], v[224:227]\n"
    "ds_read_b128 a[80:83], v5 offset:36864\n"
    "v_mfma_f32_16x16x32_bf16 v[228:231], v[60:63], a[116:119], v[228:231]\n"
    "v_exp_f32 v19, v19\n"
    "v_mfma_f32_16x16x32_bf16 v[228:231], v[56:59], a[112:115], v[228:231]\n"
    "v_cvt_pk_bf16_f32 v86, v16, v17\n"
    "v_cvt_pk_bf16_f32 v87, v18, v19\n"
    "v_mfma_f32_16x16x32_bf16 v[232:235], v[64:67], a[112:115], v[232:235]\n"
    "v_exp_f32 v20, v20\n"
    "v_mfma_f32_16x16x32_bf16 v[232:235], v[68:71], a[116:119], v[232:235]\n"
    "ds_read_b128 a[84:87], v5 offset:37888\n"
    "v_mfma_f32_16x16x32_bf16 v[236:239], v[76:79], a[116:119], v[236:239]\n"
    "global_load_lds_dwordx4 v8, s[62:63] offset:1024 sc1\n"
    "v_mfma_f32_16x16x32_bf16 v[236:239], v[72:75], a[112:115], v[236:239]\n"
    "v_exp_f32 v21, v21\n"
    "v_mfma_f32_16x16x32_bf16 v[252:255], v[72:75], a[120:123], v[252:255]\n"
    "v_exp_f32 v22, v22\n"
    "v_mfma_f32_16x16x32_bf16 v[252:255], v[76:79], a[124:127], v[252:255]\n"
    "ds_read_b128 a[88:91], v5 offset:38912\n"
    "v_mfma_f32_16x16x32_bf16 v[248:251], v[68:71], a[124:127], v[248:251]\n"
    "v_exp_f32 v23, v23\n"
    "v_mfma_f32_16x16x32_bf16 v[248:251], v[64:67], a[120:123], v[248:251]\n"
    "v_cvt_pk_bf16_f32 v94, v20, v21\n"
    "v_cvt_pk_bf16_f32 v95, v22, v23\n"
    "v_mfma_f32_16x16x32_bf16 v[244:247], v[56:59], a[120:123], v[244:247]\n"
    "v_exp_f32 v24, v24\n"
    "v_mfma_f32_16x16x32_bf16 v[244:247], v[60:63], a[124:127], v[244:247]\n"
    "ds_read_b128 a[92:95], v5 offset:39936\n"
    "v_mfma_f32_16x16x32_bf16 v[240:243], v[52:55], a[124:127], v[240:243]\n"
    "v_exp_f32 v25, v25\n"
    "v_mfma_f32_16x16x32_bf16 v[240:243], v[48:51], a[120:123], v[240:243]\n"
    "v_smfmac_f32_16x16x64_bf16 a[140:143], a[144:147], v[72:79], v10\n"
    "s_waitcnt lgkmcnt(4)\n"
    "v_mfma_f32_16x16x32_bf16 v[16:19], a[64:67], a[0:3], 4.0\n"
    "v_exp_f32 v26, v26\n"
    "v_mfma_f32_16x16x32_bf16 v[16:19], a[68:71], a[4:7], v[16:19]\n"
    "ds_read_b128 a[112:115], v7 offset:53248\n"
    "v_mfma_f32_16x16x32_bf16 v[16:19], a[72:75], a[8:11], v[16:19]\n"
    "v_exp_f32 v27, v27\n"
    "v_mfma_f32_16x16x32_bf16 v[16:19], a[76:79], a[12:15], v[16:19]\n"
    "v_cvt_pk_bf16_f32 v102, v24, v25\n"
    "v_cvt_pk_bf16_f32 v103, v26, v27\n"
    "v_mfma_f32_16x16x32_bf16 v[20:23], a[76:79], a[28:31], 4.0\n"
    "v_exp_f32 v28, v28\n"
    "v_mfma_f32_16x16x32_bf16 v[20:23], a[72:75], a[24:27], v[20:23]\n"
    "ds_read_b128 a[116:119], v7 offset:54272\n"
    "v_mfma_f32_16x16x32_bf16 v[20:23], a[68:71], a[20:23], v[20:23]\n"
    "global_load_lds_dwordx4 v8, s[62:63] offset:2048 sc1\n"
    "v_mfma_f32_16x16x32_bf16 v[20:23], a[64:67], a[16:19], v[20:23]\n"
    "v_exp_f32 v29, v29\n"
    "v_mfma_f32_16x16x32_bf16 v[24:27], a[64:67], a[32:35], 4.0\n"
    "v_exp_f32 v30, v30\n"
    "v_mfma_f32_16x16x32_bf16 v[24:27], a[68:71], a[36:39], v[24:27]\n"
    "ds_read_b128 a[120:123], v7 offset:55296\n"
    "v_mfma_f32_16x16x32_bf16 v[24:27], a[72:75], a[40:43], v[24:27]\n"
    "v_exp_f32 v31, v31\n"
    "v_mfma_f32_16x16x32_bf16 v[24:27], a[76:79], a[44:47], v[24:27]\n"
    "v_cvt_pk_bf16_f32 v110, v28, v29\n"
    "v_cvt_pk_bf16_f32 v111, v30, v31\n"
    "v_mfma_f32_16x16x32_bf16 v[28:31], a[76:79], a[60:63], 4.0\n"
    "v_exp_f32 v16, v16\n"
    "v_mfma_f32_16x16x32_bf16 v[28:31], a[72:75], a[56:59], v[28:31]\n"
    "ds_read_b128 a[124:127], v7 offset:56320\n"
    "v_mfma_f32_16x16x32_bf16 v[28:31], a[68:71], a[52:55], v[28:31]\n"
    "v_exp_f32 v17, v17\n"
    "v_mfma_f32_16x16x32_bf16 v[28:31], a[64:67], a[48:51], v[28:31]\n"
    "v_mfma_f32_16x16x32_bf16 v[128:131], v[80:83], a[96:99], v[128:131]\n"
    "v_exp_f32 v18, v18\n"
    "v_mfma_f32_16x16x32_bf16 v[128:131], v[84:87], a[100:103], v[128:131]\n"
    "ds_read_b128 a[64:67], v5 offset:40960\n"
    "v_mfma_f32_16x16x32_bf16 v[132:135], v[92:95], a[100:103], v[132:135]\n"
    "v_exp_f32 v19, v19\n"
    "v_mfma_f32_16x16x32_bf16 v[132:135], v[88:91], a[96:99], v[132:135]\n"
    "v_cvt_pk_bf16_f32 v48, v16, v17\n"
    "v_cvt_pk_bf16_f32 v49, v18, v19\n"
    "v_mfma_f32_16x16x32_bf16 v[136:139], v[96:99], a[96:99], v[136:139]\n"
    "v_exp_f32 v20, v20\n"
    "v_mfma_f32_16x16x32_bf16 v[136:139], v[100:103], a[100:103], v[136:139]\n"
    "ds_read_b128 a[68:71], v5 offset:41984\n"
    "v_mfma_f32_16x16x32_bf16 v[140:143], v[108:111], a[100:103], v[140:143]\n"
    "global_load_lds_dwordx4 v8, s[62:63] offset:3072 sc1\n"
    "v_mfma_f32_16x16x32_bf16 v[140:143], v[104:107], a[96:99], v[140:143]\n"
    "v_exp_f32 v21, v21\n"
    "v_mfma_f32_16x16x32_bf16 v[156:159], v[104:107], a[104:107], v[156:159]\n"
    "v_exp_f32 v22, v22\n"
    "v_mfma_f32_16x16x32_bf16 v[156:159], v[108:111], a[108:111], v[156:159]\n"
    "ds_read_b128 a[72:75], v5 offset:43008\n"
    "v_mfma_f32_16x16x32_bf16 v[152:155], v[100:103], a[108:111], v[152:155]\n"
    "v_exp_f32 v23, v23\n"
    "v_mfma_f32_16x16x32_bf16 v[152:155], v[96:99], a[104:107], v[152:155]\n"
    "v_cvt_pk_bf16_f32 v56, v20, v21\n"
    "v_cvt_pk_bf16_f32 v57, v22, v23\n"
    "v_mfma_f32_16x16x32_bf16 v[148:151], v[88:91], a[104:107], v[148:151]\n"
    "s_add_u32 m0, s69, 49152\n"
    "v_exp_f32 v24, v24\n"
    "v_mfma_f32_16x16x32_bf16 v[148:151], v[92:95], a[108:111], v[148:151]\n"
    "ds_read_b128 a[76:79], v5 offset:44032\n"
    "v_mfma_f32_16x16x32_bf16 v[144:147], v[84:87], a[108:111], v[144:147]\n"
    "v_exp_f32 v25, v25\n"
    "v_mfma_f32_16x16x32_bf16 v[144:147], v[80:83], a[104:107], v[144:147]\n"
    "v_smfmac_f32_16x16x64_bf16 a[128:131], a[144:147], v[80:87], v10\n"
    "s_waitcnt lgkmcnt(4)\n"
    "v_mfma_f32_16x16x32_bf16 v[16:19], a[80:83], a[0:3], 4.0\n"
    "v_exp_f32 v26, v26\n"
    "v_mfma_f32_16x16x32_bf16 v[16:19], a[84:87], a[4:7], v[16:19]\n"
    "ds_read_b128 a[96:99], v7 offset:57344\n"
    "v_mfma_f32_16x16x32_bf16 v[16:19], a[88:91], a[8:11], v[16:19]\n"
    "v_exp_f32 v27, v27\n"
    "v_mfma_f32_16x16x32_bf16 v[16:19], a[92:95], a[12:15], v[16:19]\n"
    "v_cvt_pk_bf16_f32 v64, v24, v25\n"
    "v_cvt_pk_bf16_f32 v65, v26, v27\n"
    "v_mfma_f32_16x16x32_bf16 v[20:23], a[92:95], a[28:31], 4.0\n"
    "v_exp_f32 v28, v28\n"
    "v_mfma_f32_16x16x32_bf16 v[20:23], a[88:91], a[24:27], v[20:23]\n"
    "ds_read_b128 a[100:103], v7 offset:58368\n"
    "v_mfma_f32_16x16x32_bf16 v[20:23], a[84:87], a[20:23], v[20:23]\n"
    "global_load_lds_dwordx4 v9, s[62:63] offset:0 sc1\n"
    "v_mfma_f32_16x16x32_bf16 v[20:23], a[80:83], a[16:19], v[20:23]\n"
    "v_exp_f32 v29, v29\n"
    "v_mfma_f32_16x16x32_bf16 v[24:27], a[80:83], a[32:35], 4.0\n"
    "v_exp_f32 v30, v30\n"
    "v_mfma_f32_16x16x32_bf16 v[24:27], a[84:87], a[36:39], v[24:27]\n"
    "ds_read_b128 a[104:107], v7 offset:59392\n"
    "v_mfma_f32_16x16x32_bf16 v[24:27], a[88:91], a[40:43], v[24:27]\n"
    "v_exp_f32 v31, v31\n"
    "v_mfma_f32_16x16x32_bf16 v[24:27], a[92:95], a[44:47], v[24:27]\n"
    "v_cvt_pk_bf16_f32 v72, v28, v29\n"
    "v_cvt_pk_bf16_f32 v73, v30, v31\n"
    "v_mfma_f32_16x16x32_bf16 v[28:31], a[92:95], a[60:63], 4.0\n"
    "v_exp_f32 v16, v16\n"
    "v_mfma_f32_16x16x32_bf16 v[28:31], a[88:91], a[56:59], v[28:31]\n"
    "ds_read_b128 a[108:111], v7 offset:60416\n"
    "v_mfma_f32_16x16x32_bf16 v[28:31], a[84:87], a[52:55], v[28:31]\n"
    "v_exp_f32 v17, v17\n"
    "v_mfma_f32_16x16x32_bf16 v[28:31], a[80:83], a[48:51], v[28:31]\n"
    "v_mfma_f32_16x16x32_bf16 v[160:163], v[80:83], a[112:115], v[160:163]\n"
    "v_exp_f32 v18, v18\n"
    "v_mfma_f32_16x16x32_bf16 v[160:163], v[84:87], a[116:119], v[160:163]\n"
    "ds_read_b128 a[80:83], v5 offset:45056\n"
    "v_mfma_f32_16x16x32_bf16 v[164:167], v[92:95], a[116:119], v[164:167]\n"
    "v_exp_f32 v19, v19\n"
    "v_mfma_f32_16x16x32_bf16 v[164:167], v[88:91], a[112:115], v[164:167]\n"
    "v_cvt_pk_bf16_f32 v50, v16, v17\n"
    "v_cvt_pk_bf16_f32 v51, v18, v19\n"
    "v_mfma_f32_16x16x32_bf16 v[168:171], v[96:99], a[112:115], v[168:171]\n"
    "v_exp_f32 v20, v20\n"
    "v_mfma_f32_16x16x32_bf16 v[168:171], v[100:103], a[116:119], v[168:171]\n"
    "ds_read_b128 a[84:87], v5 offset:46080\n"
    "v_mfma_f32_16x16x32_bf16 v[172:175], v[108:111], a[116:119], v[172:175]\n"
    "global_load_lds_dwordx4 v9, s[62:63] offset:1024 sc1\n"
    "v_mfma_f32_16x16x32_bf16 v[172:175], v[104:107], a[112:115], v[172:175]\n"
    "v_exp_f32 v21, v21\n"
    "v_mfma_f32_16x16x32_bf16 v[188:191], v[104:107], a[120:123], v[188:191]\n"
    "v_exp_f32 v22, v22\n"
    "v_mfma_f32_16x16x32_bf16 v[188:191], v[108:111], a[124:127], v[188:191]\n"
    "ds_read_b128 a[88:91], v5 offset:47104\n"
    "v_mfma_f32_16x16x32_bf16 v[184:187], v[100:103], a[124:127], v[184:187]\n"
    "v_exp_f32 v23, v23\n"
    "v_mfma_f32_16x16x32_bf16 v[184:187], v[96:99], a[120:123], v[184:187]\n"
    "v_cvt_pk_bf16_f32 v58, v20, v21\n"
    "v_cvt_pk_bf16_f32 v59, v22, v23\n"
    "v_mfma_f32_16x16x32_bf16 v[180:183], v[88:91], a[120:123], v[180:183]\n"
    "v_exp_f32 v24, v24\n"
    "v_mfma_f32_16x16x32_bf16 v[180:183], v[92:95], a[124:127], v[180:183]\n"
    "ds_read_b128 a[92:95], v5 offset:48128\n"
    "v_mfma_f32_16x16x32_bf16 v[176:179], v[84:87], a[124:127], v[176:179]\n"
    "v_exp_f32 v25, v25\n"
    "v_mfma_f32_16x16x32_bf16 v[176:179], v[80:83], a[120:123], v[176:179]\n"
    "v_smfmac_f32_16x16x64_bf16 a[132:135], a[144:147], v[88:95], v10\n"
    "s_waitcnt lgkmcnt(4)\n"
    "v_mfma_f32_16x16x32_bf16 v[16:19], a[64:67], a[0:3], 4.0\n"
    "v_exp_f32 v26, v26\n"
    "v_mfma_f32_16x16x32_bf16 v[16:19], a[68:71], a[4:7], v[16:19]\n"
    "ds_read_b128 a[112:115], v7 offset:61440\n"
    "v_mfma_f32_16x16x32_bf16 v[16:19], a[72:75], a[8:11], v[16:19]\n"
    "v_exp_f32 v27, v27\n"
    "v_mfma_f32_16x16x32_bf16 v[16:19], a[76:79], a[12:15], v[16:19]\n"
    "v_cvt_pk_bf16_f32 v66, v24, v25\n"
    "v_cvt_pk_bf16_f32 v67, v26, v27\n"
    "v_mfma_f32_16x16x32_bf16 v[20:23], a[76:79], a[28:31], 4.0\n"
    "v_exp_f32 v28, v28\n"
    "v_mfma_f32_16x16x32_bf16 v[20:23], a[72:75], a[24:27], v[20:23]\n"
    "ds_read_b128 a[116:119], v7 offset:62464\n"
    "v_mfma_f32_16x16x32_bf16 v[20:23], a[68:71], a[20:23], v[20:23]\n"
    "global_load_lds_dwordx4 v9, s[62:63] offset:2048 sc1\n"
    "v_mfma_f32_16x16x32_bf16 v[20:23], a[64:67], a[16:19], v[20:23]\n"
    "v_exp_f32 v29, v29\n"
    "v_mfma_f32_16x16x32_bf16 v[24:27], a[64:67], a[32:35], 4.0\n"
    "v_exp_f32 v30, v30\n"
    "v_mfma_f32_16x16x32_bf16 v[24:27], a[68:71], a[36:39], v[24:27]\n"
    "ds_read_b128 a[120:123], v7 offset:63488\n"
    "v_mfma_f32_16x16x32_bf16 v[24:27], a[72:75], a[40:43], v[24:27]\n"
    "v_exp_f32 v31, v31\n"
    "v_mfma_f32_16x16x32_bf16 v[24:27], a[76:79], a[44:47], v[24:27]\n"
    "v_cvt_pk_bf16_f32 v74, v28, v29\n"
    "v_cvt_pk_bf16_f32 v75, v30, v31\n"
    "v_mfma_f32_16x16x32_bf16 v[28:31], a[76:79], a[60:63], 4.0\n"
    "v_exp_f32 v16, v16\n"
    "v_mfma_f32_16x16x32_bf16 v[28:31], a[72:75], a[56:59], v[28:31]\n"
    "ds_read_b128 a[124:127], v7 offset:64512\n"
    "v_mfma_f32_16x16x32_bf16 v[28:31], a[68:71], a[52:55], v[28:31]\n"
    "s_waitcnt vmcnt(7)\n"
    "v_exp_f32 v17, v17\n"
    "v_mfma_f32_16x16x32_bf16 v[28:31], a[64:67], a[48:51], v[28:31]\n"
    "s_barrier\n"
    "v_mfma_f32_16x16x32_bf16 v[192:195], v[80:83], a[96:99], v[192:195]\n"
    "v_exp_f32 v18, v18\n"
    "v_mfma_f32_16x16x32_bf16 v[192:195], v[84:87], a[100:103], v[192:195]\n"
    "ds_read_b128 a[64:67], v4 offset:0\n"
    "v_mfma_f32_16x16x32_bf16 v[196:199], v[92:95], a[100:103], v[196:199]\n"
    "v_exp_f32 v19, v19\n"
    "v_mfma_f32_16x16x32_bf16 v[196:199], v[88:91], a[96:99], v[196:199]\n"
    "v_cvt_pk_bf16_f32 v52, v16, v17\n"
    "v_cvt_pk_bf16_f32 v53, v18, v19\n"
    "v_mfma_f32_16x16x32_bf16 v[200:203], v[96:99], a[96:99], v[200:203]\n"
    "v_exp_f32 v20, v20\n"
    "v_mfma_f32_16x16x32_bf16 v[200:203], v[100:103], a[100:103], v[200:203]\n"
    "ds_read_b128 a[68:71], v4 offset:1024\n"
    "v_mfma_f32_16x16x32_bf16 v[204:207], v[108:111], a[100:103], v[204:207]\n"
    "global_load_lds_dwordx4 v9, s[62:63] offset:3072 sc1\n"
    "v_mfma_f32_16x16x32_bf16 v[204:207], v[104:107], a[96:99], v[204:207]\n"
    "v_exp_f32 v21, v21\n"
    "v_mfma_f32_16x16x32_bf16 v[220:223], v[104:107], a[104:107], v[220:223]\n"
    "v_exp_f32 v22, v22\n"
    "v_mfma_f32_16x16x32_bf16 v[220:223], v[108:111], a[108:111], v[220:223]\n"
    "ds_read_b128 a[72:75], v4 offset:2048\n"
    "v_mfma_f32_16x16x32_bf16 v[216:219], v[100:103], a[108:111], v[216:219]\n"
    "s_add_u32 s62, s62, 0x8000\n"
    "v_exp_f32 v23, v23\n"
    "v_mfma_f32_16x16x32_bf16 v[216:219], v[96:99], a[104:107], v[216:219]\n"
    "v_cvt_pk_bf16_f32 v60, v20, v21\n"
    "v_cvt_pk_bf16_f32 v61, v22, v23\n"
    "v_mfma_f32_16x16x32_bf16 v[212:215], v[88:91], a[104:107], v[212:215]\n"
    "s_add_u32 m0, s69, 65536\n"
    "v_exp_f32 v24, v24\n"
    "v_mfma_f32_16x16x32_bf16 v[212:215], v[92:95], a[108:111], v[212:215]\n"
    "ds_read_b128 a[76:79], v4 offset:3072\n"
    "v_mfma_f32_16x16x32_bf16 v[208:211], v[84:87], a[108:111], v[208:211]\n"
    "v_exp_f32 v25, v25\n"
    "v_mfma_f32_16x16x32_bf16 v[208:211], v[80:83], a[104:107], v[208:211]\n"
    "v_smfmac_f32_16x16x64_bf16 a[136:139], a[144:147], v[96:103], v10\n"
    "s_waitcnt lgkmcnt(4)\n"
    "v_mfma_f32_16x16x32_bf16 v[16:19], a[80:83], a[0:3], 4.0\n"
    "v_exp_f32 v26, v26\n"
    "v_mfma_f32_16x16x32_bf16 v[16:19], a[84:87], a[4:7], v[16:19]\n"
    "ds_read_b128 a[96:99], v6 offset:16384\n"
    "v_mfma_f32_16x16x32_bf16 v[16:19], a[88:91], a[8:11], v[16:19]\n"
    "v_exp_f32 v27, v27\n"
    "v_mfma_f32_16x16x32_bf16 v[16:19], a[92:95], a[12:15], v[16:19]\n"
    "v_cvt_pk_bf16_f32 v68, v24, v25\n"
    "v_cvt_pk_bf16_f32 v69, v26, v27\n"
    "v_mfma_f32_16x16x32_bf16 v[20:23], a[92:95], a[28:31], 4.0\n"
    "v_exp_f32 v28, v28\n"
    "v_mfma_f32_16x16x32_bf16 v[20:23], a[88:91], a[24:27], v[20:23]\n"
    "ds_read_b128 a[100:103], v6 offset:17408\n"
    "v_mfma_f32_16x16x32_bf16 v[20:23], a[84:87], a[20:23], v[20:23]\n"
    "global_load_lds_dwordx4 v8, s[62:63] offset:0 sc1\n"
    "v_mfma_f32_16x16x32_bf16 v[20:23], a[80:83], a[16:19], v[20:23]\n"
    "L%=_w5:\n"
    "s_cbranch_execz L%=_w9\n"
    "v_exp_f32 v29, v29\n"
    "v_mfma_f32_16x16x32_bf16 v[24:27], a[80:83], a[32:35], 4.0\n"
    "v_exp_f32 v30, v30\n"
    "v_mfma_f32_16x16x32_bf16 v[24:27], a[84:87], a[36:39], v[24:27]\n"
    "ds_read_b128 a[104:107], v6 offset:18432\n"
    "v_mfma_f32_16x16x32_bf16 v[24:27], a[88:91], a[40:43], v[24:27]\n"
    "v_exp_f32 v31, v31\n"
    "v_mfma_f32_16x16x32_bf16 v[24:27], a[92:95], a[44:47], v[24:27]\n"
    "v_cvt_pk_bf16_f32 v76, v28, v29\n"
    "v_cvt_pk_bf16_f32 v77, v30, v31\n"
    "v_mfma_f32_16x16x32_bf16 v[28:31], a[92:95], a[60:63], 4.0\n"
    "v_exp_f32 v16, v16\n"
    "v_mfma_f32_16x16x32_bf16 v[28:31], a[88:91], a[56:59], v[28:31]\n"
    "ds_read_b128 a[108:111], v6 offset:19456\n"
    "v_mfma_f32_16x16x32_bf16 v[28:31], a[84:87], a[52:55], v[28:31]\n"
    "v_exp_f32 v17, v17\n"
    "v_mfma_f32_16x16x32_bf16 v[28:31], a[80:83], a[48:51], v[28:31]\n"
    "v_mfma_f32_16x16x32_bf16 v[224:227], v[80:83], a[112:115], v[224:227]\n"
    "v_exp_f32 v18, v18\n"
    "v_mfma_f32_16x16x32_bf16 v[224:227], v[84:87], a[116:119], v[224:227]\n"
    "ds_read_b128 a[80:83], v4 offset:4096\n"
    "v_mfma_f32_16x16x32_bf16 v[228:231], v[92:95], a[116:119], v[228:231]\n"
    "v_exp_f32 v19, v19\n"
    "v_mfma_f32_16x16x32_bf16 v[228:231], v[88:91], a[112:115], v[228:231]\n"
    "v_cvt_pk_bf16_f32 v54, v16, v17\n"
    "v_cvt_pk_bf16_f32 v55, v18, v19\n"
    "v_mfma_f32_16x16x32_bf16 v[232:235], v[96:99], a[112:115], v[232:235]\n"
    "v_exp_f32 v20, v20\n"
    "v_mfma_f32_16x16x32_bf16 v[232:235], v[100:103], a[116:119], v[232:235]\n"
    "ds_read_b128 a[84:87], v4 offset:5120\n"
    "v_mfma_f32_16x16x32_bf16 v[236:239], v[108:111], a[116:119], v[236:239]\n"
    "global_load_lds_dwordx4 v8, s[62:63] offset:1024 sc1\n"
    "v_mfma_f32_16x16x32_bf16 v[236:239], v[104:107], a[112:115], v[236:239]\n"
    "v_exp_f32 v21, v21\n"
    "v_mfma_f32_16x16x32_bf16 v[252:255], v[104:107], a[120:123], v[252:255]\n"
    "v_exp_f32 v22, v22\n"
    "v_mfma_f32_16x16x32_bf16 v[252:255], v[108:111], a[124:127], v[252:255]\n"
    "ds_read_b128 a[88:91], v4 offset:6144\n"
    "v_mfma_f32_16x16x32_bf16 v[248:251], v[100:103], a[124:127], v[248:251]\n"
    "v_exp_f32 v23, v23\n"
    "v_mfma_f32_16x16x32_bf16 v[248:251], v[96:99], a[120:123], v[248:251]\n"
    "v_cvt_pk_bf16_f32 v62, v20, v21\n"
    "v_cvt_pk_bf16_f32 v63, v22, v23\n"
    "v_mfma_f32_16x16x32_bf16 v[244:247], v[88:91], a[120:123], v[244:247]\n"
    "v_exp_f32 v24, v24\n"
    "v_mfma_f32_16x16x32_bf16 v[244:247], v[92:95], a[124:127], v[244:247]\n"
    "ds_read_b128 a[92:95], v4 offset:7168\n"
    "v_mfma_f32_16x16x32_bf16 v[240:243], v[84:87], a[124:127], v[240:243]\n"
    "v_exp_f32 v25, v25\n"
    "v_mfma_f32_16x16x32_bf16 v[240:243], v[80:83], a[120:123], v[240:243]\n"
    "v_smfmac_f32_16x16x64_bf16 a[140:143], a[144:147], v[104:111], v10\n"
    "s_waitcnt lgkmcnt(4)\n"
    "v_mfma_f32_16x16x32_bf16 v[16:19], a[64:67], a[0:3], 4.0\n"
    "v_exp_f32 v26, v26\n"
    "v_mfma_f32_16x16x32_bf16 v[16:19], a[68:71], a[4:7], v[16:19]\n"
    "ds_read_b128 a[112:115], v6 offset:20480\n"
    "v_mfma_f32_16x16x32_bf16 v[16:19], a[72:75], a[8:11], v[16:19]\n"
    "v_exp_f32 v27, v27\n"
    "v_mfma_f32_16x16x32_bf16 v[16:19], a[76:79], a[12:15], v[16:19]\n"
    "v_cvt_pk_bf16_f32 v70, v24, v25\n"
    "v_cvt_pk_bf16_f32 v71, v26, v27\n"
    "v_mfma_f32_16x16x32_bf16 v[20:23], a[76:79], a[28:31], 4.0\n"
    "v_exp_f32 v28, v28\n"
    "v_mfma_f32_16x16x32_bf16 v[20:23], a[72:75], a[24:27], v[20:23]\n"
    "ds_read_b128 a[116:119], v6 offset:21504\n"
    "v_mfma_f32_16x16x32_bf16 v[20:23], a[68:71], a[20:23], v[20:23]\n"
    "global_load_lds_dwordx4 v8, s[62:63] offset:2048 sc1\n"
    "v_mfma_f32_16x16x32_bf16 v[20:23], a[64:67], a[16:19], v[20:23]\n"
    "v_exp_f32 v29, v29\n"
    "v_mfma_f32_16x16x32_bf16 v[24:27], a[64:67], a[32:35], 4.0\n"
    "v_exp_f32 v30, v30\n"
    "v_mfma_f32_16x16x32_bf16 v[24:27], a[68:71], a[36:39], v[24:27]\n"
    "ds_read_b128 a[120:123], v6 offset:22528\n"
    "v_mfma_f32_16x16x32_bf16 v[24:27], a[72:75], a[40:43], v[24:27]\n"
    "v_exp_f32 v31, v31\n"
    "v_mfma_f32_16x16x32_bf16 v[24:27], a[76:79], a[44:47], v[24:27]\n"
    "v_cvt_pk_bf16_f32 v78, v28, v29\n"
    "v_cvt_pk_bf16_f32 v79, v30, v31\n"
    "v_mfma_f32_16x16x32_bf16 v[28:31], a[76:79], a[60:63], 4.0\n"
    "v_exp_f32 v16, v16\n"
    "v_mfma_f32_16x16x32_bf16 v[28:31], a[72:75], a[56:59], v[28:31]\n"
    "ds_read_b128 a[124:127], v6 offset:23552\n"
    "v_mfma_f32_16x16x32_bf16 v[28:31], a[68:71], a[52:55], v[28:31]\n"
    "v_exp_f32 v17, v17\n"
    "v_mfma_f32_16x16x32_bf16 v[28:31], a[64:67], a[48:51], v[28:31]\n"
    "v_mfma_f32_16x16x32_bf16 v[128:131], v[48:51], a[96:99], v[128:131]\n"
    "v_exp_f32 v18, v18\n"
    "v_mfma_f32_16x16x32_bf16 v[128:131], v[52:55], a[100:103], v[128:131]\n"
    "ds_read_b128 a[64:67], v4 offset:8192\n"
    "v_mfma_f32_16x16x32_bf16 v[132:135], v[60:63], a[100:103], v[132:135]\n"
    "v_exp_f32 v19, v19\n"
    "v_mfma_f32_16x16x32_bf16 v[132:135], v[56:59], a[96:99], v[132:135]\n"
    "v_cvt_pk_bf16_f32 v80, v16, v17\n"
    "v_cvt_pk_bf16_f32 v81, v18, v19\n"
    "v_mfma_f32_16x16x32_bf16 v[136:139], v[64:67], a[96:99], v[136:139]\n"
    "v_exp_f32 v20, v20\n"
    "v_mfma_f32_16x16x32_bf16 v[136:139], v[68:71], a[100:103], v[136:139]\n"
    "ds_read_b128 a[68:71], v4 offset:9216\n"
    "v_mfma_f32_16x16x32_bf16 v[140:143], v[76:79], a[100:103], v[140:143]\n"
    "global_load_lds_dwordx4 v8, s[62:63] offset:3072 sc1\n"
    "v_mfma_f32_16x16x32_bf16 v[140:143], v[72:75], a[96:99], v[140:143]\n"
    "v_exp_f32 v21, v21\n"
    "v_mfma_f32_16x16x32_bf16 v[156:159], v[72:75], a[104:107], v[156:159]\n"
    "v_exp_f32 v22, v22\n"
    "v_mfma_f32_16x16x32_bf16 v[156:159], v[76:79], a[108:111], v[156:159]\n"
    "ds_read_b128 a[72:75], v4 offset:10240\n"
    "v_mfma_f32_16x16x32_bf16 v[152:155], v[68:71], a[108:111], v[152:155]\n"
    "v_exp_f32 v23, v23\n"
    "v_mfma_f32_16x16x32_bf16 v[152:155], v[64:67], a[104:107], v[152:155]\n"
    "v_cvt_pk_bf16_f32 v88, v20, v21\n"
    "v_cvt_pk_bf16_f32 v89, v22, v23\n"
    "v_mfma_f32_16x16x32_bf16 v[148:151], v[56:59], a[104:107], v[148:151]\n"
    "s_add_u32 m0, s69, 81920\n"
    "v_exp_f32 v24, v24\n"
    "v_mfma_f32_16x16x32_bf16 v[148:151], v[60:63], a[108:111], v[148:151]\n"
    "ds_read_b128 a[76:79], v4 offset:11264\n"
    "v_mfma_f32_16x16x32_bf16 v[144:147], v[52:55], a[108:111], v[144:147]\n"
    "v_exp_f32 v25, v25\n"
    "v_mfma_f32_16x16x32_bf16 v[144:147], v[48:51], a[104:107], v[144:147]\n"
    "v_smfmac_f32_16x16x64_bf16 a[128:131], a[144:147], v[48:55], v10\n"
    "s_waitcnt lgkmcnt(4)\n"
    "v_mfma_f32_16x16x32_bf16 v[16:19], a[80:83], a[0:3], 4.0\n"
    "v_exp_f32 v26, v26\n"
    "v_mfma_f32_16x16x32_bf16 v[16:19], a[84:87], a[4:7], v[16:19]\n"
    "ds_read_b128 a[96:99], v6 offset:24576\n"
    "v_mfma_f32_16x16x32_bf16 v[16:19], a[88:91], a[8:11], v[16:19]\n"
    "v_exp_f32 v27, v27\n"
    "v_mfma_f32_16x16x32_bf16 v[16:19], a[92:95], a[12:15], v[16:19]\n"
    "v_cvt_pk_bf16_f32 v96, v24, v25\n"
    "v_cvt_pk_bf16_f32 v97, v26, v27\n"
    "v_mfma_f32_16x16x32_bf16 v[20:23], a[92:95], a[28:31], 4.0\n"
    "v_exp_f32 v28, v28\n"
    "v_mfma_f32_16x16x32_bf16 v[20:23], a[88:91], a[24:27], v[20:23]\n"
    "ds_read_b128 a[100:103], v6 offset:25600\n"
    "v_mfma_f32_16x16x32_bf16 v[20:23], a[84:87], a[20:23], v[20:23]\n"
    "global_load_lds_dwordx4 v9, s[62:63] offset:0 sc1\n"
    "v_mfma_f32_16x16x32_bf16 v[20:23], a[80:83], a[16:19], v[20:23]\n"
    "v_exp_f32 v29, v29\n"
    "v_mfma_f32_16x16x32_bf16 v[24:27], a[80:83], a[32:35], 4.0\n"
    "v_exp_f32 v30, v30\n"
    "v_mfma_f32_16x16x32_bf16 v[24:27], a[84:87], a[36:39], v[24:27]\n"
    "ds_read_b128 a[104:107], v6 offset:26624\n"
    "v_mfma_f32_16x16x32_bf16 v[24:27], a[88:91], a[40:43], v[24:27]\n"
    "v_exp_f32 v31, v31\n"
    "v_mfma_f32_16x16x32_bf16 v[24:27], a[92:95], a[44:47], v[24:27]\n"
    "v_cvt_pk_bf16_f32 v104, v28, v29\n"
    "v_cvt_pk_bf16_f32 v105, v30, v31\n"
    "v_mfma_f32_16x16x32_bf16 v[28:31], a[92:95], a[60:63], 4.0\n"
    "v_exp_f32 v16, v16\n"
    "v_mfma_f32_16x16x32_bf16 v[28:31], a[88:91], a[56:59], v[28:31]\n"
    "ds_read_b128 a[108:111], v6 offset:27648\n"
    "v_mfma_f32_16x16x32_bf16 v[28:31], a[84:87], a[52:55], v[28:31]\n"
    "v_exp_f32 v17, v17\n"
    "v_mfma_f32_16x16x32_bf16 v[28:31], a[80:83], a[48:51], v[28:31]\n"
    "v_mfma_f32_16x16x32_bf16 v[160:163], v[48:51], a[112:115], v[160:163]\n"
    "v_exp_f32 v18, v18\n"
    "v_mfma_f32_16x16x32_bf16 v[160:163], v[52:55], a[116:119], v[160:163]\n"
    "ds_read_b128 a[80:83], v4 offset:12288\n"
    "v_mfma_f32_16x16x32_bf16 v[164:167], v[60:63], a[116:119], v[164:167]\n"
    "v_exp_f32 v19, v19\n"
    "v_mfma_f32_16x16x32_bf16 v[164:167], v[56:59], a[112:115], v[164:167]\n"
    "v_cvt_pk_bf16_f32 v82, v16, v17\n"
    "v_cvt_pk_bf16_f32 v83, v18, v19\n"
    "v_mfma_f32_16x16x32_bf16 v[168:171], v[64:67], a[112:115], v[168:171]\n"
    "v_exp_f32 v20, v20\n"
    "v_mfma_f32_16x16x32_bf16 v[168:171], v[68:71], a[116:119], v[168:171]\n"
    "ds_read_b128 a[84:87], v4 offset:13312\n"
    "v_mfma_f32_16x16x32_bf16 v[172:175], v[76:79], a[116:119], v[172:175]\n"
    "global_load_lds_dwordx4 v9, s[62:63] offset:1024 sc1\n"
    "v_mfma_f32_16x16x32_bf16 v[172:175], v[72:75], a[112:115], v[172:175]\n"
    "v_exp_f32 v21, v21\n"
    "v_mfma_f32_16x16x32_bf16 v[188:191], v[72:75], a[120:123], v[188:191]\n"
    "v_exp_f32 v22, v22\n"
    "v_mfma_f32_16x16x32_bf16 v[188:191], v[76:79], a[124:127], v[188:191]\n"
    "ds_read_b128 a[88:91], v4 offset:14336\n"
    "v_mfma_f32_16x16x32_bf16 v[184:187], v[68:71], a[124:127], v[184:187]\n"
    "v_exp_f32 v23, v23\n"
    "v_mfma_f32_16x16x32_bf16 v[184:187], v[64:67], a[120:123], v[184:187]\n"
    "v_cvt_pk_bf16_f32 v90, v20, v21\n"
    "v_cvt_pk_bf16_f32 v91, v22, v23\n"
    "v_mfma_f32_16x16x32_bf16 v[180:183], v[56:59], a[120:123], v[180:183]\n"
    "v_exp_f32 v24, v24\n"
    "v_mfma_f32_16x16x32_bf16 v[180:183], v[60:63], a[124:127], v[180:183]\n"
    "ds_read_b128 a[92:95], v4 offset:15360\n"
    "v_mfma_f32_16x16x32_bf16 v[176:179], v[52:55], a[124:127], v[176:179]\n"
    "v_exp_f32 v25, v25\n"
    "v_mfma_f32_16x16x32_bf16 v[176:179], v[48:51], a[120:123], v[176:179]\n"
    "v_smfmac_f32_16x16x64_bf16 a[132:135], a[144:147], v[56:63], v10\n"
    "s_waitcnt lgkmcnt(4)\n"
    "v_mfma_f32_16x16x32_bf16 v[16:19], a[64:67], a[0:3], 4.0\n"
    "v_exp_f32 v26, v26\n"
    "v_mfma_f32_16x16x32_bf16 v[16:19], a[68:71], a[4:7], v[16:19]\n"
    "ds_read_b128 a[112:115], v6 offset:28672\n"
    "v_mfma_f32_16x16x32_bf16 v[16:19], a[72:75], a[8:11], v[16:19]\n"
    "v_exp_f32 v27, v27\n"
    "v_mfma_f32_16x16x32_bf16 v[16:19], a[76:79], a[12:15], v[16:19]\n"
    "v_cvt_pk_bf16_f32 v98, v24, v25\n"
    "v_cvt_pk_bf16_f32 v99, v26, v27\n"
    "v_mfma_f32_16x16x32_bf16 v[20:23], a[76:79], a[28:31], 4.0\n"
    "v_exp_f32 v28, v28\n"
    "v_mfma_f32_16x16x32_bf16 v[20:23], a[72:75], a[24:27], v[20:23]\n"
    "ds_read_b128 a[116:119], v6 offset:29696\n"
    "v_mfma_f32_16x16x32_bf16 v[20:23], a[68:71], a[20:23], v[20:23]\n"
    "global_load_lds_dwordx4 v9, s[62:63] offset:2048 sc1\n"
    "v_mfma_f32_16x16x32_bf16 v[20:23], a[64:67], a[16:19], v[20:23]\n"
    "v_exp_f32 v29, v29\n"
    "v_mfma_f32_16x16x32_bf16 v[24:27], a[64:67], a[32:35], 4.0\n"
    "v_exp_f32 v30, v30\n"
    "v_mfma_f32_16x16x32_bf16 v[24:27], a[68:71], a[36:39], v[24:27]\n"
    "ds_read_b128 a[120:123], v6 offset:30720\n"
    "v_mfma_f32_16x16x32_bf16 v[24:27], a[72:75], a[40:43], v[24:27]\n"
    "v_exp_f32 v31, v31\n"
    "v_mfma_f32_16x16x32_bf16 v[24:27], a[76:79], a[44:47], v[24:27]\n"
    "v_cvt_pk_bf16_f32 v106, v28, v29\n"
    "v_cvt_pk_bf16_f32 v107, v30, v31\n"
    "v_mfma_f32_16x16x32_bf16 v[28:31], a[76:79], a[60:63], 4.0\n"
    "v_exp_f32 v16, v16\n"
    "v_mfma_f32_16x16x32_bf16 v[28:31], a[72:75], a[56:59], v[28:31]\n"
    "ds_read_b128 a[124:127], v6 offset:31744\n"
    "v_mfma_f32_16x16x32_bf16 v[28:31], a[68:71], a[52:55], v[28:31]\n"
    "s_waitcnt vmcnt(7)\n"
    "v_exp_f32 v17, v17\n"
    "v_mfma_f32_16x16x32_bf16 v[28:31], a[64:67], a[48:51], v[28:31]\n"
    "s_barrier\n"
    "v_mfma_f32_16x16x32_bf16 v[192:195], v[48:51], a[96:99], v[192:195]\n"
    "v_exp_f32 v18, v18\n"
    "v_mfma_f32_16x16x32_bf16 v[192:195], v[52:55], a[100:103], v[192:195]\n"
    "ds_read_b128 a[64:67], v4 offset:32768\n"
    "v_mfma_f32_16x16x32_bf16 v[196:199], v[60:63], a[100:103], v[196:199]\n"
    "v_exp_f32 v19, v19\n"
    "v_mfma_f32_16x16x32_bf16 v[196:199], v[56:59], a[96:99], v[196:199]\n"
    "v_cvt_pk_bf16_f32 v84, v16, v17\n"
    "v_cvt_pk_bf16_f32 v85, v18, v19\n"
    "v_mfma_f32_16x16x32_bf16 v[200:203], v[64:67], a[96:99], v[200:203]\n"
    "v_exp_f32 v20, v20\n"
    "v_mfma_f32_16x16x32_bf16 v[200:203], v[68:71], a[100:103], v[200:203]\n"
    "ds_read_b128 a[68:71], v4 offset:33792\n"
    "v_mfma_f32_16x16x32_bf16 v[204:207], v[76:79], a[100:103], v[204:207]\n"
    "global_load_lds_dwordx4 v9, s[62:63] offset:3072 sc1\n"
    "v_mfma_f32_16x16x32_bf16 v[204:207], v[72:75], a[96:99], v[204:207]\n"
    "v_exp_f32 v21, v21\n"
    "v_mfma_f32_16x16x32_bf16 v[220:223], v[72:75], a[104:107], v[220:223]\n"
    "v_exp_f32 v22, v22\n"
    "v_mfma_f32_16x16x32_bf16 v[220:223], v[76:79], a[108:111], v[220:223]\n"
    "ds_read_b128 a[72:75], v4 offset:34816\n"
    "v_mfma_f32_16x16x32_bf16 v[216:219], v[68:71], a[108:111], v[216:219]\n"
    "s_add_u32 s62, s62, 0x8000\n"
    "v_exp_f32 v23, v23\n"
    "v_mfma_f32_16x16x32_bf16 v[216:219], v[64:67], a[104:107], v[216:219]\n"
    "v_cvt_pk_bf16_f32 v92, v20, v21\n"
    "v_cvt_pk_bf16_f32 v93, v22, v23\n"
    "v_mfma_f32_16x16x32_bf16 v[212:215], v[56:59], a[104:107], v[212:215]\n"
    "s_add_u32 m0, s69, 98304\n"
    "v_exp_f32 v24, v24\n"
    "v_mfma_f32_16x16x32_bf16 v[212:215], v[60:63], a[108:111], v[212:215]\n"
    "ds_read_b128 a[76:79], v4 offset:35840\n"
    "v_mfma_f32_16x16x32_bf16 v[208:211], v[52:55], a[108:111], v[208:211]\n"
    "v_exp_f32 v25, v25\n"
    "v_mfma_f32_16x16x32_bf16 v[208:211], v[48:51], a[104:107], v[208:211]\n"
    "v_smfmac_f32_16x16x64_bf16 a[136:139], a[144:147], v[64:71], v10\n"
    "s_waitcnt lgkmcnt(4)\n"
    "v_mfma_f32_16x16x32_bf16 v[16:19], a[80:83], a[0:3], 4.0\n"
    "v_exp_f32 v26, v26\n"
    "v_mfma_f32_16x16x32_bf16 v[16:19], a[84:87], a[4:7], v[16:19]\n"
    "ds_read_b128 a[96:99], v6 offset:49152\n"
    "v_mfma_f32_16x16x32_bf16 v[16:19], a[88:91], a[8:11], v[16:19]\n"
    "v_exp_f32 v27, v27\n"
    "v_mfma_f32_16x16x32_bf16 v[16:19], a[92:95], a[12:15], v[16:19]\n"
    "v_cvt_pk_bf16_f32 v100, v24, v25\n"
    "v_cvt_pk_bf16_f32 v101, v26, v27\n"
    "v_mfma_f32_16x16x32_bf16 v[20:23], a[92:95], a[28:31], 4.0\n"
    "v_exp_f32 v28, v28\n"
    "v_mfma_f32_16x16x32_bf16 v[20:23], a[88:91], a[24:27], v[20:23]\n"
    "ds_read_b128 a[100:103], v6 offset:50176\n"
    "v_mfma_f32_16x16x32_bf16 v[20:23], a[84:87], a[20:23], v[20:23]\n"
    "global_load_lds_dwordx4 v8, s[62:63] offset:0 sc1\n"
    "v_mfma_f32_16x16x32_bf16 v[20:23], a[80:83], a[16:19], v[20:23]\n"
    "v_exp_f32 v29, v29\n"
    "v_mfma_f32_16x16x32_bf16 v[24:27], a[80:83], a[32:35], 4.0\n"
    "v_exp_f32 v30, v30\n"
    "v_mfma_f32_16x16x32_bf16 v[24:27], a[84:87], a[36:39], v[24:27]\n"
    "ds_read_b128 a[104:107], v6 offset:51200\n"
    "v_mfma_f32_16x16x32_bf16 v[24:27], a[88:91], a[40:43], v[24:27]\n"
    "v_exp_f32 v31, v31\n"
    "v_mfma_f32_16x16x32_bf16 v[24:27], a[92:95], a[44:47], v[24:27]\n"
    "v_cvt_pk_bf16_f32 v108, v28, v29\n"
    "v_cvt_pk_bf16_f32 v109, v30, v31\n"
    "v_mfma_f32_16x16x32_bf16 v[28:31], a[92:95], a[60:63], 4.0\n"
    "v_exp_f32 v16, v16\n"
    "v_mfma_f32_16x16x32_bf16 v[28:31], a[88:91], a[56:59], v[28:31]\n"
    "ds_read_b128 a[108:111], v6 offset:52224\n"
    "v_mfma_f32_16x16x32_bf16 v[28:31], a[84:87], a[52:55], v[28:31]\n"
    "v_exp_f32 v17, v17\n"
    "v_mfma_f32_16x16x32_bf16 v[28:31], a[80:83], a[48:51], v[28:31]\n"
    "v_mfma_f32_16x16x32_bf16 v[224:227], v[48:51], a[112:115], v[224:227]\n"
    "v_exp_f32 v18, v18\n"
    "v_mfma_f32_16x16x32_bf16 v[224:227], v[52:55], a[116:119], v[224:227]\n"
    "ds_read_b128 a[80:83], v4 offset:36864\n"
    "v_mfma_f32_16x16x32_bf16 v[228:231], v[60:63], a[116:119], v[228:231]\n"
    "v_exp_f32 v19, v19\n"
    "v_mfma_f32_16x16x32_bf16 v[228:231], v[56:59], a[112:115], v[228:231]\n"
    "v_cvt_pk_bf16_f32 v86, v16, v17\n"
    "v_cvt_pk_bf16_f32 v87, v18, v19\n"
    "v_mfma_f32_16x16x32_bf16 v[232:235], v[64:67], a[112:115], v[232:235]\n"
    "v_exp_f32 v20, v20\n"
    "v_mfma_f32_16x16x32_bf16 v[232:235], v[68:71], a[116:119], v[232:235]\n"
    "ds_read_b128 a[84:87], v4 offset:37888\n"
    "v_mfma_f32_16x16x32_bf16 v[236:239], v[76:79], a[116:119], v[236:239]\n"
    "global_load_lds_dwordx4 v8, s[62:63] offset:1024 sc1\n"
    "v_mfma_f32_16x16x32_bf16 v[236:239], v[72:75], a[112:115], v[236:239]\n"
    "v_exp_f32 v21, v21\n"
    "v_mfma_f32_16x16x32_bf16 v[252:255], v[72:75], a[120:123], v[252:255]\n"
    "v_exp_f32 v22, v22\n"
    "v_mfma_f32_16x16x32_bf16 v[252:255], v[76:79], a[124:127], v[252:255]\n"
    "ds_read_b128 a[88:91], v4 offset:38912\n"
    "v_mfma_f32_16x16x32_bf16 v[248:251], v[68:71], a[124:127], v[248:251]\n"
    "v_exp_f32 v23, v23\n"
    "v_mfma_f32_16x16x32_bf16 v[248:251], v[64:67], a[120:123], v[248:251]\n"
    "v_cvt_pk_bf16_f32 v94, v20, v21\n"
    "v_cvt_pk_bf16_f32 v95, v22, v23\n"
    "v_mfma_f32_16x16x32_bf16 v[244:247], v[56:59], a[120:123], v[244:247]\n"
    "v_exp_f32 v24, v24\n"
    "v_mfma_f32_16x16x32_bf16 v[244:247], v[60:63], a[124:127], v[244:247]\n"
    "ds_read_b128 a[92:95], v4 offset:39936\n"
    "v_mfma_f32_16x16x32_bf16 v[240:243], v[52:55], a[124:127], v[240:243]\n"
    "v_exp_f32 v25, v25\n"
    "v_mfma_f32_16x16x32_bf16 v[240:243], v[48:51], a[120:123], v[240:243]\n"
    "v_smfmac_f32_16x16x64_bf16 a[140:143], a[144:147], v[72:79], v10\n"
    "s_waitcnt lgkmcnt(4)\n"
    "s_sub_u32 s70, s70, 1\n"
    "s_cbranch_scc1 L%=_tail\n"
    "s_cmp_ge_u32 %3, 2\n"
    "s_cbranch_scc1 L%=_uk\n"
    "L%=_uv:\n"
    "v_mfma_f32_16x16x32_bf16 v[16:19], a[64:67], a[0:3], 4.0\n"
    "L%=_w6:\n"
    "s_cbranch_execz L%=_w10\n"
    "v_exp_f32 v26, v26\n"
    "v_mfma_f32_16x16x32_bf16 v[16:19], a[68:71], a[4:7], v[16:19]\n"
    "ds_read_b128 a[112:115], v6 offset:53248\n"
    "v_cndmask_b32 v33, v36, v37, s[34:35]\n"
    "v_mfma_f32_16x16x32_bf16 v[16:19], a[72:75], a[8:11], v[16:19]\n"
    "v_exp_f32 v27, v27\n"
    "v_mfma_f32_16x16x32_bf16 v[16:19], a[76:79], a[12:15], v[16:19]\n"
    "v_cvt_pk_bf16_f32 v102, v24, v25\n"
    "v_cvt_pk_bf16_f32 v103, v26, v27\n"
    "v_mfma_f32_16x16x32_bf16 v[20:23], a[76:79], a[28:31], 4.0\n"
    "v_exp_f32 v28, v28\n"
    "v_mfma_f32_16x16x32_bf16 v[20:23], a[72:75], a[24:27], v[20:23]\n"
    "ds_read_b128 a[116:119], v6 offset:54272\n"
    "v_mfma_f32_16x16x32_bf16 v[20:23], a[68:71], a[20:23], v[20:23]\n"
    "global_load_lds_dwordx4 v8, s[62:63] offset:2048 sc1\n"
    "v_cndmask_b32 v35, v38, v39, s[34:35]\n"
    "v_mfma_f32_16x16x32_bf16 v[20:23], a[64:67], a[16:19], v[20:23]\n"
    "v_exp_f32 v29, v29\n"
    "v_mfma_f32_16x16x32_bf16 v[24:27], a[64:67], a[32:35], 4.0\n"
    "v_exp_f32 v30, v30\n"
    "v_mfma_f32_16x16x32_bf16 v[24:27], a[68:71], a[36:39], v[24:27]\n"
    "ds_read_b128 a[120:123], v6 offset:55296\n"
    "v_cndmask_b32 v41, v44, v45, s[34:35]\n"
    "v_mfma_f32_16x16x32_bf16 v[24:27], a[72:75], a[40:43], v[24:27]\n"
    "v_exp_f32 v31, v31\n"
    "v_mfma_f32_16x16x32_bf16 v[24:27], a[76:79], a[44:47], v[24:27]\n"
    "v_cvt_pk_bf16_f32 v110, v28, v29\n"
    "v_cvt_pk_bf16_f32 v111, v30, v31\n"
    "v_mfma_f32_16x16x32_bf16 v[28:31], a[76:79], a[60:63], 4.0\n"
    "v_exp_f32 v16, v16\n"
    "v_mfma_f32_16x16x32_bf16 v[28:31], a[72:75], a[56:59], v[28:31]\n"
    "ds_read_b128 a[124:127], v6 offset:56320\n"
    "v_cndmask_b32 v43, v46, v47, s[34:35]\n"
    "v_mfma_f32_16x16x32_bf16 v[28:31], a[68:71], a[52:55], v[28:31]\n"
    "v_exp_f32 v17, v17\n"
    "v_mfma_f32_16x16x32_bf16 v[28:31], a[64:67], a[48:51], v[28:31]\n"
    "v_lshlrev_b32 v32, 16, v33\n"
    "v_lshlrev_b32 v34, 16, v35\n"
    "v_mfma_f32_16x16x32_bf16 v[128:131], v[80:83], a[96:99], v[128:131]\n"
    "v_exp_f32 v18, v18\n"
    "v_mfma_f32_16x16x32_bf16 v[128:131], v[84:87], a[100:103], v[128:131]\n"
    "ds_read_b128 a[64:67], v4 offset:40960\n"
    "v_lshlrev_b32 v40, 16, v41\n"
    "v_mfma_f32_16x16x32_bf16 v[132:135], v[92:95], a[100:103], v[132:135]\n"
    "v_exp_f32 v19, v19\n"
    "v_mfma_f32_16x16x32_bf16 v[132:135], v[88:91], a[96:99], v[132:135]\n"
    "v_cvt_pk_bf16_f32 v48, v16, v17\n"
    "v_cvt_pk_bf16_f32 v49, v18, v19\n"
    "v_mfma_f32_16x16x32_bf16 v[136:139], v[96:99], a[96:99], v[136:139]\n"
    "v_exp_f32 v20, v20\n"
    "v_mfma_f32_16x16x32_bf16 v[136:139], v[100:103], a[100:103], v[136:139]\n"
    "ds_read_b128 a[68:71], v4 offset:41984\n"
    "v_mfma_f32_16x16x32_bf16 v[140:143], v[108:111], a[100:103], v[140:143]\n"
    "global_load_lds_dwordx4 v8, s[62:63] offset:3072 sc1\n"
    "v_lshlrev_b32 v42, 16, v43\n"
    "v_mfma_f32_16x16x32_bf16 v[140:143], v[104:107], a[96:99], v[140:143]\n"
    "v_exp_f32 v21, v21\n"
    "v_mfma_f32_16x16x32_bf16 v[156:159], v[104:107], a[104:107], v[156:159]\n"
    "v_exp_f32 v22, v22\n"
    "v_mfma_f32_16x16x32_bf16 v[156:159], v[108:111], a[108:111], v[156:159]\n"
    "ds_read_b128 a[72:75], v4 offset:43008\n"
    "v_and_b32 v33, 0xffff0000, v33\n"
    "v_mfma_f32_16x16x32_bf16 v[152:155], v[100:103], a[108:111], v[152:155]\n"
    "v_exp_f32 v23, v23\n"
    "v_mfma_f32_16x16x32_bf16 v[152:155], v[96:99], a[104:107], v[152:155]\n"
    "v_cvt_pk_bf16_f32 v56, v20, v21\n"
    "v_cvt_pk_bf16_f32 v57, v22, v23\n"
    "v_mfma_f32_16x16x32_bf16 v[148:151], v[88:91], a[104:107], v[148:151]\n"
    "s_add_u32 m0, s69, 114688\n"
    "v_exp_f32 v24, v24\n"
    "v_mfma_f32_16x16x32_bf16 v[148:151], v[92:95], a[108:111], v[148:151]\n"
    "ds_read_b128 a[76:79], v4 offset:44032\n"
    "v_and_b32 v35, 0xffff0000, v35\n"
    "v_mfma_f32_16x16x32_bf16 v[144:147], v[84:87], a[108:111], v[144:147]\n"
    "v_exp_f32 v25, v25\n"
    "v_mfma_f32_16x16x32_bf16 v[144:147], v[80:83], a[104:107], v[144:147]\n"
    "v_and_b32 v41, 0xffff0000, v41\n"
    "v_and_b32 v43, 0xffff0000, v43\n"
    "v_smfmac_f32_16x16x64_bf16 a[128:131], a[144:147], v[80:87], v10\n"
    "s_waitcnt lgkmcnt(4)\n"
    "v_mul_f32 v40, 0x3d000000, v40\n"
    "v_mul_f32 v41, 0x3d000000, v41\n"
    "v_mfma_f32_16x16x32_bf16 v[16:19], a[80:83], a[0:3], 4.0\n"
    "v_exp_f32 v26, v26\n"
    "v_mfma_f32_16x16x32_bf16 v[16:19], a[84:87], a[4:7], v[16:19]\n"
    "ds_read_b128 a[96:99], v6 offset:57344\n"
    "v_mul_f32 v42, 0x3d000000, v42\n"
    "v_mfma_f32_16x16x32_bf16 v[16:19], a[88:91], a[8:11], v[16:19]\n"
    "v_exp_f32 v27, v27\n"
    "v_mfma_f32_16x16x32_bf16 v[16:19], a[92:95], a[12:15], v[16:19]\n"
    "v_cvt_pk_bf16_f32 v64, v24, v25\n"
    "v_cvt_pk_bf16_f32 v65, v26, v27\n"
    "v_mfma_f32_16x16x32_bf16 v[20:23], a[92:95], a[28:31], 4.0\n"
    "v_exp_f32 v28, v28\n"
    "v_mfma_f32_16x16x32_bf16 v[20:23], a[88:91], a[24:27], v[20:23]\n"
    "ds_read_b128 a[100:103], v6 offset:58368\n"
    "v_mfma_f32_16x16x32_bf16 v[20:23], a[84:87], a[20:23], v[20:23]\n"
    "global_load_lds_dwordx4 v9, s[62:63] offset:0 sc1\n"
    "v_mul_f32 v43, 0x3d000000, v43\n"
    "v_mfma_f32_16x16x32_bf16 v[20:23], a[80:83], a[16:19], v[20:23]\n"
    "v_exp_f32 v29, v29\n"
    "v_mfma_f32_16x16x32_bf16 v[24:27], a[80:83], a[32:35], 4.0\n"
    "v_exp_f32 v30, v30\n"
    "v_mfma_f32_16x16x32_bf16 v[24:27], a[84:87], a[36:39], v[24:27]\n"
    "ds_read_b128 a[104:107], v6 offset:59392\n"
    "v_mul_f32 v32, v32, v40\n"
    "v_mfma_f32_16x16x32_bf16 v[24:27], a[88:91], a[40:43], v[24:27]\n"
    "v_exp_f32 v31, v31\n"
    "v_mfma_f32_16x16x32_bf16 v[24:27], a[92:95], a[44:47], v[24:27]\n"
    "v_cvt_pk_bf16_f32 v72, v28, v29\n"
    "v_cvt_pk_bf16_f32 v73, v30, v31\n"
    "v_mfma_f32_16x16x32_bf16 v[28:31], a[92:95], a[60:63], 4.0\n"
    "v_exp_f32 v16, v16\n"
    "v_mfma_f32_16x16x32_bf16 v[28:31], a[88:91], a[56:59], v[28:31]\n"
    "ds_read_b128 a[108:111], v6 offset:60416\n"
    "v_mul_f32 v33, v33, v41\n"
    "v_mfma_f32_16x16x32_bf16 v[28:31], a[84:87], a[52:55], v[28:31]\n"
    "v_exp_f32 v17, v17\n"
    "v_mfma_f32_16x16x32_bf16 v[28:31], a[80:83], a[48:51], v[28:31]\n"
    "v_mul_f32 v34, v34, v42\n"
    "v_mul_f32 v35, v35, v43\n"
    "v_mfma_f32_16x16x32_bf16 v[160:163], v[80:83], a[112:115], v[160:163]\n"
    "v_exp_f32 v18, v18\n"
    "v_mfma_f32_16x16x32_bf16 v[160:163], v[84:87], a[116:119], v[160:163]\n"
    "ds_read_b128 a[80:83], v4 offset:45056\n"
    "v_cvt_f32_i32 v112, v112\n"
    "v_mfma_f32_16x16x32_bf16 v[164:167], v[92:95], a[116:119], v[164:167]\n"
    "v_exp_f32 v19, v19\n"
    "v_mfma_f32_16x16x32_bf16 v[164:167], v[88:91], a[112:115], v[164:167]\n"
    "v_cvt_pk_bf16_f32 v50, v16, v17\n"
    "v_cvt_pk_bf16_f32 v51, v18, v19\n"
    "v_mfma_f32_16x16x32_bf16 v[168:171], v[96:99], a[112:115], v[168:171]\n"
    "v_exp_f32 v20, v20\n"
    "v_mfma_f32_16x16x32_bf16 v[168:171], v[100:103], a[116:119], v[168:171]\n"
    "ds_read_b128 a[84:87], v4 offset:46080\n"
    "v_mfma_f32_16x16x32_bf16 v[172:175], v[108:111], a[116:119], v[172:175]\n"
    "global_load_lds_dwordx4 v9, s[62:63] offset:1024 sc1\n"
    "v_cvt_f32_i32 v113, v113\n"
    "v_mfma_f32_16x16x32_bf16 v[172:175], v[104:107], a[112:115], v[172:175]\n"
    "v_exp_f32 v21, v21\n"
    "v_mfma_f32_16x16x32_bf16 v[188:191], v[104:107], a[120:123], v[188:191]\n"
    "v_exp_f32 v22, v22\n"
    "v_mfma_f32_16x16x32_bf16 v[188:191], v[108:111], a[124:127], v[188:191]\n"
    "ds_read_b128 a[88:91], v4 offset:47104\n"
    "v_cvt_f32_i32 v114, v114\n"
    "v_mfma_f32_16x16x32_bf16 v[184:187], v[100:103], a[124:127], v[184:187]\n"
    "v_exp_f32 v23, v23\n"
    "v_mfma_f32_16x16x32_bf16 v[184:187], v[96:99], a[120:123], v[184:187]\n"
    "v_cvt_pk_bf16_f32 v58, v20, v21\n"
    "v_cvt_pk_bf16_f32 v59, v22, v23\n"
    "v_mfma_f32_16x16x32_bf16 v[180:183], v[88:91], a[120:123], v[180:183]\n"
    "v_exp_f32 v24, v24\n"
    "v_mfma_f32_16x16x32_bf16 v[180:183], v[92:95], a[124:127], v[180:183]\n"
    "ds_read_b128 a[92:95], v4 offset:48128\n"
    "v_cvt_f32_i32 v115, v115\n"
    "v_mfma_f32_16x16x32_bf16 v[176:179], v[84:87], a[124:127], v[176:179]\n"
    "v_exp_f32 v25, v25\n"
    "v_mfma_f32_16x16x32_bf16 v[176:179], v[80:83], a[120:123], v[176:179]\n"
    "v_cvt_f32_i32 v116, v116\n"
    "v_cvt_f32_i32 v117, v117\n"
    "v_smfmac_f32_16x16x64_bf16 a[132:135], a[144:147], v[88:95], v10\n"
    "s_waitcnt lgkmcnt(4)\n"
    "v_cvt_f32_i32 v118, v118\n"
    "v_cvt_f32_i32 v119, v119\n"
    "v_mfma_f32_16x16x32_bf16 v[16:19], a[64:67], a[0:3], 4.0\n"
    "v_exp_f32 v26, v26\n"
    "v_mfma_f32_16x16x32_bf16 v[16:19], a[68:71], a[4:7], v[16:19]\n"
    "ds_read_b128 a[112:115], v6 offset:61440\n"
    "v_fma_f32 v112, v112, v40, -v32\n"
    "v_mfma_f32_16x16x32_bf16 v[16:19], a[72:75], a[8:11], v[16:19]\n"
    "v_exp_f32 v27, v27\n"
    "v_mfma_f32_16x16x32_bf16 v[16:19], a[76:79], a[12:15], v[16:19]\n"
    "v_cvt_pk_bf16_f32 v66, v24, v25\n"
    "v_cvt_pk_bf16_f32 v67, v26, v27\n"
    "v_mfma_f32_16x16x32_bf16 v[20:23], a[76:79], a[28:31], 4.0\n"
    "v_exp_f32 v28, v28\n"
    "v_mfma_f32_16x16x32_bf16 v[20:23], a[72:75], a[24:27], v[20:23]\n"
    "ds_read_b128 a[116:119], v6 offset:62464\n"
    "v_mfma_f32_16x16x32_bf16 v[20:23], a[68:71], a[20:23], v[20:23]\n"
    "global_load_lds_dwordx4 v9, s[62:63] offset:2048 sc1\n"
    "v_fma_f32 v113, v113, v40, -v32\n"
    "v_mfma_f32_16x16x32_bf16 v[20:23], a[64:67], a[16:19], v[20:23]\n"
    "v_exp_f32 v29, v29\n"
    "v_mfma_f32_16x16x32_bf16 v[24:27], a[64:67], a[32:35], 4.0\n"
    "v_exp_f32 v30, v30\n"
    "v_mfma_f32_16x16x32_bf16 v[24:27], a[68:71], a[36:39], v[24:27]\n"
    "ds_read_b128 a[120:123], v6 offset:63488\n"
    "v_fma_f32 v114, v114, v40, -v32\n"
    "v_mfma_f32_16x16x32_bf16 v[24:27], a[72:75], a[40:43], v[24:27]\n"
    "v_exp_f32 v31, v31\n"
    "v_mfma_f32_16x16x32_bf16 v[24:27], a[76:79], a[44:47], v[24:27]\n"
    "v_cvt_pk_bf16_f32 v74, v28, v29\n"
    "v_cvt_pk_bf16_f32 v75, v30, v31\n"
    "v_mfma_f32_16x16x32_bf16 v[28:31], a[76:79], a[60:63], 4.0\n"
    "v_exp_f32 v16, v16\n"
    "v_mfma_f32_16x16x32_bf16 v[28:31], a[72:75], a[56:59], v[28:31]\n"
    "ds_read_b128 a[124:127], v6 offset:64512\n"
    "v_fma_f32 v115, v115, v40, -v32\n"
    "v_mfma_f32_16x16x32_bf16 v[28:31], a[68:71], a[52:55], v[28:31]\n"
    "s_waitcnt vmcnt(7)\n"
    "v_exp_f32 v17, v17\n"
    "v_mfma_f32_16x16x32_bf16 v[28:31], a[64:67], a[48:51], v[28:31]\n"
    "s_barrier\n"
    "v_fma_f32 v116, v116, v41, -v33\n"
    "v_fma_f32 v117, v117, v41, -v33\n"
    "v_mfma_f32_16x16x32_bf16 v[192:195], v[80:83], a[96:99], v[192:195]\n"
    "v_exp_f32 v18, v18\n"
    "v_mfma_f32_16x16x32_bf16 v[192:195], v[84:87], a[100:103], v[192:195]\n"
    "ds_read_b128 a[64:67], v5 offset:0\n"
    "v_fma_f32 v118, v118, v41, -v33\n"
    "v_mfma_f32_16x16x32_bf16 v[196:199], v[92:95], a[100:103], v[196:199]\n"
    "v_exp_f32 v19, v19\n"
    "v_mfma_f32_16x16x32_bf16 v[196:199], v[88:91], a[96:99], v[196:199]\n"
    "v_cvt_pk_bf16_f32 v52, v16, v17\n"
    "v_cvt_pk_bf16_f32 v53, v18, v19\n"
    "v_mfma_f32_16x16x32_bf16 v[200:203], v[96:99], a[96:99], v[200:203]\n"
    "v_exp_f32 v20, v20\n"
    "v_mfma_f32_16x16x32_bf16 v[200:203], v[100:103], a[100:103], v[200:203]\n"
    "ds_read_b128 a[68:71], v5 offset:1024\n"
    "v_mfma_f32_16x16x32_bf16 v[204:207], v[108:111], a[100:103], v[204:207]\n"
    "global_load_lds_dwordx4 v9, s[62:63] offset:3072 sc1\n"
    "v_fma_f32 v119, v119, v41, -v33\n"
    "v_mfma_f32_16x16x32_bf16 v[204:207], v[104:107], a[96:99], v[204:207]\n"
    "v_exp_f32 v21, v21\n"
    "v_mfma_f32_16x16x32_bf16 v[220:223], v[104:107], a[104:107], v[220:223]\n"
    "v_exp_f32 v22, v22\n"
    "v_mfma_f32_16x16x32_bf16 v[220:223], v[108:111], a[108:111], v[220:223]\n"
    "ds_read_b128 a[72:75], v5 offset:2048\n"
    "v_cvt_f32_i32 v120, v120\n"
    "v_mfma_f32_16x16x32_bf16 v[216:219], v[100:103], a[108:111], v[216:219]\n"
    "s_add_u32 s62, s62, 0x8000\n"
    "v_exp_f32 v23, v23\n"
    "v_mfma_f32_16x16x32_bf16 v[216:219], v[96:99], a[104:107], v[216:219]\n"
    "v_cvt_pk_bf16_f32 v60, v20, v21\n"
    "v_cvt_pk_bf16_f32 v61, v22, v23\n"
    "v_mfma_f32_16x16x32_bf16 v[212:215], v[88:91], a[104:107], v[212:215]\n"
    "s_add_u32 m0, s69, 0\n"
    "v_exp_f32 v24, v24\n"
    "v_mfma_f32_16x16x32_bf16 v[212:215], v[92:95], a[108:111], v[212:215]\n"
    "ds_read_b128 a[76:79], v5 offset:3072\n"
    "v_cvt_f32_i32 v121, v121\n"
    "v_mfma_f32_16x16x32_bf16 v[208:211], v[84:87], a[108:111], v[208:211]\n"
    "v_exp_f32 v25, v25\n"
    "v_mfma_f32_16x16x32_bf16 v[208:211], v[80:83], a[104:107], v[208:211]\n"
    "v_cvt_f32_i32 v122, v122\n"
    "v_cvt_f32_i32 v123, v123\n"
    "v_smfmac_f32_16x16x64_bf16 a[136:139], a[144:147], v[96:103], v10\n"
    "s_waitcnt lgkmcnt(4)\n"
    "v_cvt_f32_i32 v124, v124\n"
    "v_cvt_f32_i32 v125, v125\n"
    "v_mfma_f32_16x16x32_bf16 v[16:19], a[80:83], a[0:3], 4.0\n"
    "v_exp_f32 v26, v26\n"
    "v_mfma_f32_16x16x32_bf16 v[16:19], a[84:87], a[4:7], v[16:19]\n"
    "ds_read_b128 a[96:99], v7 offset:16384\n"
    "v_cvt_f32_i32 v126, v126\n"
    "v_mfma_f32_16x16x32_bf16 v[16:19], a[88:91], a[8:11], v[16:19]\n"
    "v_exp_f32 v27, v27\n"
    "v_mfma_f32_16x16x32_bf16 v[16:19], a[92:95], a[12:15], v[16:19]\n"
    "v_cvt_pk_bf16_f32 v68, v24, v25\n"
    "v_cvt_pk_bf16_f32 v69, v26, v27\n"
    "v_mfma_f32_16x16x32_bf16 v[20:23], a[92:95], a[28:31], 4.0\n"
    "L%=_w7:\n"
    "s_cbranch_execz L%=_w11\n"
    "v_exp_f32 v28, v28\n"
    "v_mfma_f32_16x16x32_bf16 v[20:23], a[88:91], a[24:27], v[20:23]\n"
    "ds_read_b128 a[100:103], v7 offset:17408\n"
    "v_mfma_f32_16x16x32_bf16 v[20:23], a[84:87], a[20:23], v[20:23]\n"
    "global_load_lds_dwordx4 v8, s[62:63] offset:0 sc1\n"
    "v_cvt_f32_i32 v127, v127\n"
    "v_mfma_f32_16x16x32_bf16 v[20:23], a[80:83], a[16:19], v[20:23]\n"
    "v_exp_f32 v29, v29\n"
    "v_mfma_f32_16x16x32_bf16 v[24:27], a[80:83], a[32:35], 4.0\n"
    "v_exp_f32 v30, v30\n"
    "v_mfma_f32_16x16x32_bf16 v[24:27], a[84:87], a[36:39], v[24:27]\n"
    "ds_read_b128 a[104:107], v7 offset:18432\n"
    "v_fma_f32 v120, v120, v42, -v34\n"
    "v_mfma_f32_16x16x32_bf16 v[24:27], a[88:91], a[40:43], v[24:27]\n"
    "v_exp_f32 v31, v31\n"
    "v_mfma_f32_16x16x32_bf16 v[24:27], a[92:95], a[44:47], v[24:27]\n"
    "v_cvt_pk_bf16_f32 v76, v28, v29\n"
    "v_cvt_pk_bf16_f32 v77, v30, v31\n"
    "v_mfma_f32_16x16x32_bf16 v[28:31], a[92:95], a[60:63], 4.0\n"
    "v_exp_f32 v16, v16\n"
    "v_mfma_f32_16x16x32_bf16 v[28:31], a[88:91], a[56:59], v[28:31]\n"
    "ds_read_b128 a[108:111], v7 offset:19456\n"
    "v_fma_f32 v121, v121, v42, -v34\n"
    "v_mfma_f32_16x16x32_bf16 v[28:31], a[84:87], a[52:55], v[28:31]\n"
    "v_exp_f32 v17, v17\n"
    "v_mfma_f32_16x16x32_bf16 v[28:31], a[80:83], a[48:51], v[28:31]\n"
    "v_fma_f32 v122, v122, v42, -v34\n"
    "v_fma_f32 v123, v123, v42, -v34\n"
    "v_mfma_f32_16x16x32_bf16 v[224:227], v[80:83], a[112:115], v[224:227]\n"
    "v_exp_f32 v18, v18\n"
    "v_mfma_f32_16x16x32_bf16 v[224:227], v[84:87], a[116:119], v[224:227]\n"
    "ds_read_b128 a[80:83], v5 offset:4096\n"
    "v_fma_f32 v124, v124, v43, -v35\n"
    "v_mfma_f32_16x16x32_bf16 v[228:231], v[92:95], a[116:119], v[228:231]\n"
    "v_exp_f32 v19, v19\n"
    "v_mfma_f32_16x16x32_bf16 v[228:231], v[88:91], a[112:115], v[228:231]\n"
    "v_cvt_pk_bf16_f32 v54, v16, v17\n"
    "v_cvt_pk_bf16_f32 v55, v18, v19\n"
    "v_mfma_f32_16x16x32_bf16 v[232:235], v[96:99], a[112:115], v[232:235]\n"
    "v_exp_f32 v20, v20\n"
    "v_mfma_f32_16x16x32_bf16 v[232:235], v[100:103], a[116:119], v[232:235]\n"
    "ds_read_b128 a[84:87], v5 offset:5120\n"
    "v_mfma_f32_16x16x32_bf16 v[236:239], v[108:111], a[116:119], v[236:239]\n"
    "global_load_lds_dwordx4 v8, s[62:63] offset:1024 sc1\n"
    "v_fma_f32 v125, v125, v43, -v35\n"
    "v_mfma_f32_16x16x32_bf16 v[236:239], v[104:107], a[112:115], v[236:239]\n"
    "v_exp_f32 v21, v21\n"
    "v_mfma_f32_16x16x32_bf16 v[252:255], v[104:107], a[120:123], v[252:255]\n"
    "v_exp_f32 v22, v22\n"
    "v_mfma_f32_16x16x32_bf16 v[252:255], v[108:111], a[124:127], v[252:255]\n"
    "ds_read_b128 a[88:91], v5 offset:6144\n"
    "v_fma_f32 v126, v126, v43, -v35\n"
    "v_mfma_f32_16x16x32_bf16 v[248:251], v[100:103], a[124:127], v[248:251]\n"
    "v_exp_f32 v23, v23\n"
    "v_mfma_f32_16x16x32_bf16 v[248:251], v[96:99], a[120:123], v[248:251]\n"
    "v_cvt_pk_bf16_f32 v62, v20, v21\n"
    "v_cvt_pk_bf16_f32 v63, v22, v23\n"
    "v_mfma_f32_16x16x32_bf16 v[244:247], v[88:91], a[120:123], v[244:247]\n"
    "v_exp_f32 v24, v24\n"
    "v_mfma_f32_16x16x32_bf16 v[244:247], v[92:95], a[124:127], v[244:247]\n"
    "ds_read_b128 a[92:95], v5 offset:7168\n"
    "v_fma_f32 v127, v127, v43, -v35\n"
    "v_mfma_f32_16x16x32_bf16 v[240:243], v[84:87], a[124:127], v[240:243]\n"
    "v_exp_f32 v25, v25\n"
    "v_mfma_f32_16x16x32_bf16 v[240:243], v[80:83], a[120:123], v[240:243]\n"
    "v_cvt_pk_bf16_f32 v36, v112, v116\n"
    "v_cvt_pk_bf16_f32 v37, v114, v118\n"
    "v_smfmac_f32_16x16x64_bf16 a[140:143], a[144:147], v[104:111], v10\n"
    "s_waitcnt lgkmcnt(4)\n"
    "v_cvt_pk_bf16_f32 v38, v120, v124\n"
    "v_cvt_pk_bf16_f32 v39, v122, v126\n"
    "v_mfma_f32_16x16x32_bf16 v[16:19], a[64:67], a[0:3], 4.0\n"
    "v_exp_f32 v26, v26\n"
    "v_mfma_f32_16x16x32_bf16 v[16:19], a[68:71], a[4:7], v[16:19]\n"
    "ds_read_b128 a[112:115], v7 offset:20480\n"
    "v_cvt_pk_bf16_f32 v44, v113, v117\n"
    "v_mfma_f32_16x16x32_bf16 v[16:19], a[72:75], a[8:11], v[16:19]\n"
    "v_exp_f32 v27, v27\n"
    "v_mfma_f32_16x16x32_bf16 v[16:19], a[76:79], a[12:15], v[16:19]\n"
    "v_cvt_pk_bf16_f32 v70, v24, v25\n"
    "v_cvt_pk_bf16_f32 v71, v26, v27\n"
    "v_mfma_f32_16x16x32_bf16 v[20:23], a[76:79], a[28:31], 4.0\n"
    "v_exp_f32 v28, v28\n"
    "v_mfma_f32_16x16x32_bf16 v[20:23], a[72:75], a[24:27], v[20:23]\n"
    "ds_read_b128 a[116:119], v7 offset:21504\n"
    "v_mfma_f32_16x16x32_bf16 v[20:23], a[68:71], a[20:23], v[20:23]\n"
    "global_load_lds_dwordx4 v8, s[62:63] offset:2048 sc1\n"
    "v_cvt_pk_bf16_f32 v45, v115, v119\n"
    "v_mfma_f32_16x16x32_bf16 v[20:23], a[64:67], a[16:19], v[20:23]\n"
    "v_exp_f32 v29, v29\n"
    "v_mfma_f32_16x16x32_bf16 v[24:27], a[64:67], a[32:35], 4.0\n"
    "v_exp_f32 v30, v30\n"
    "v_mfma_f32_16x16x32_bf16 v[24:27], a[68:71], a[36:39], v[24:27]\n"
    "ds_read_b128 a[120:123], v7 offset:22528\n"
    "v_cvt_pk_bf16_f32 v46, v121, v125\n"
    "v_mfma_f32_16x16x32_bf16 v[24:27], a[72:75], a[40:43], v[24:27]\n"
    "v_exp_f32 v31, v31\n"
    "v_mfma_f32_16x16x32_bf16 v[24:27], a[76:79], a[44:47], v[24:27]\n"
    "v_cvt_pk_bf16_f32 v78, v28, v29\n"
    "v_cvt_pk_bf16_f32 v79, v30, v31\n"
    "v_mfma_f32_16x16x32_bf16 v[28:31], a[76:79], a[60:63], 4.0\n"
    "v_exp_f32 v16, v16\n"
    "v_mfma_f32_16x16x32_bf16 v[28:31], a[72:75], a[56:59], v[28:31]\n"
    "ds_read_b128 a[124:127], v7 offset:23552\n"
    "v_cvt_pk_bf16_f32 v47, v123, v127\n"
    "v_mfma_f32_16x16x32_bf16 v[28:31], a[68:71], a[52:55], v[28:31]\n"
    "v_exp_f32 v17, v17\n"
    "v_mfma_f32_16x16x32_bf16 v[28:31], a[64:67], a[48:51], v[28:31]\n"
    "v_permlane32_swap_b32 v36, v37\n"
    "v_permlane32_swap_b32 v38, v39\n"
    "v_mfma_f32_16x16x32_bf16 v[128:131], v[48:51], a[96:99], v[128:131]\n"
    "v_exp_f32 v18, v18\n"
    "v_mfma_f32_16x16x32_bf16 v[128:131], v[52:55], a[100:103], v[128:131]\n"
    "ds_read_b128 a[64:67], v5 offset:8192\n"
    "v_permlane32_swap_b32 v44, v45\n"
    "v_mfma_f32_16x16x32_bf16 v[132:135], v[60:63], a[100:103], v[132:135]\n"
    "v_exp_f32 v19, v19\n"
    "v_mfma_f32_16x16x32_bf16 v[132:135], v[56:59], a[96:99], v[132:135]\n"
    "v_cvt_pk_bf16_f32 v80, v16, v17\n"
    "v_cvt_pk_bf16_f32 v81, v18, v19\n"
    "v_mfma_f32_16x16x32_bf16 v[136:139], v[64:67], a[96:99], v[136:139]\n"
    "v_exp_f32 v20, v20\n"
    "v_mfma_f32_16x16x32_bf16 v[136:139], v[68:71], a[100:103], v[136:139]\n"
    "ds_read_b128 a[68:71], v5 offset:9216\n"
    "v_mfma_f32_16x16x32_bf16 v[140:143], v[76:79], a[100:103], v[140:143]\n"
    "global_load_lds_dwordx4 v8, s[62:63] offset:3072 sc1\n"
    "v_permlane32_swap_b32 v46, v47\n"
    "v_mfma_f32_16x16x32_bf16 v[140:143], v[72:75], a[96:99], v[140:143]\n"
    "v_exp_f32 v21, v21\n"
    "v_mfma_f32_16x16x32_bf16 v[156:159], v[72:75], a[104:107], v[156:159]\n"
    "v_exp_f32 v22, v22\n"
    "v_mfma_f32_16x16x32_bf16 v[156:159], v[76:79], a[108:111], v[156:159]\n"
    "ds_read_b128 a[72:75], v5 offset:10240\n"
    "v_mfma_f32_16x16x32_bf16 v[152:155], v[68:71], a[108:111], v[152:155]\n"
    "v_exp_f32 v23, v23\n"
    "v_mfma_f32_16x16x32_bf16 v[152:155], v[64:67], a[104:107], v[152:155]\n"
    "v_cvt_pk_bf16_f32 v88, v20, v21\n"
    "v_cvt_pk_bf16_f32 v89, v22, v23\n"
    "v_mfma_f32_16x16x32_bf16 v[148:151], v[56:59], a[104:107], v[148:151]\n"
    "s_add_u32 m0, s69, 16384\n"
    "v_exp_f32 v24, v24\n"
    "v_mfma_f32_16x16x32_bf16 v[148:151], v[60:63], a[108:111], v[148:151]\n"
    "ds_read_b128 a[76:79], v5 offset:11264\n"
    "v_mfma_f32_16x16x32_bf16 v[144:147], v[52:55], a[108:111], v[144:147]\n"
    "v_exp_f32 v25, v25\n"
    "v_mfma_f32_16x16x32_bf16 v[144:147], v[48:51], a[104:107], v[144:147]\n"
    "global_store_dwordx4 v14, v[36:39], s[64:65] offset:0 sc1\n"
    "v_smfmac_f32_16x16x64_bf16 a[128:131], a[144:147], v[48:55], v10\n"
    "s_waitcnt lgkmcnt(4)\n"
    "global_store_dwordx4 v14, v[44:47], s[64:65] offset:2048 sc1\n"
    "v_mfma_f32_16x16x32_bf16 v[16:19], a[80:83], a[0:3], 4.0\n"
    "v_exp_f32 v26, v26\n"
    "v_mfma_f32_16x16x32_bf16 v[16:19], a[84:87], a[4:7], v[16:19]\n"
    "ds_read_b128 a[96:99], v7 offset:24576\n"
    "v_mfma_f32_16x16x32_bf16 v[16:19], a[88:91], a[8:11], v[16:19]\n"
    "v_exp_f32 v27, v27\n"
    "v_mfma_f32_16x16x32_bf16 v[16:19], a[92:95], a[12:15], v[16:19]\n"
    "v_cvt_pk_bf16_f32 v96, v24, v25\n"
    "v_cvt_pk_bf16_f32 v97, v26, v27\n"
    "v_mfma_f32_16x16x32_bf16 v[20:23], a[92:95], a[28:31], 4.0\n"
    "v_exp_f32 v28, v28\n"
    "v_mfma_f32_16x16x32_bf16 v[20:23], a[88:91], a[24:27], v[20:23]\n"
    "ds_read_b128 a[100:103], v7 offset:25600\n"
    "v_mfma_f32_16x16x32_bf16 v[20:23], a[84:87], a[20:23], v[20:23]\n"
    "global_load_lds_dwordx4 v9, s[62:63] offset:0 sc1\n"
    "v_mfma_f32_16x16x32_bf16 v[20:23], a[80:83], a[16:19], v[20:23]\n"
    "v_exp_f32 v29, v29\n"
    "v_mfma_f32_16x16x32_bf16 v[24:27], a[80:83], a[32:35], 4.0\n"
    "v_exp_f32 v30, v30\n"
    "v_mfma_f32_16x16x32_bf16 v[24:27], a[84:87], a[36:39], v[24:27]\n"
    "ds_read_b128 a[104:107], v7 offset:26624\n"
    "v_mfma_f32_16x16x32_bf16 v[24:27], a[88:91], a[40:43], v[24:27]\n"
    "v_exp_f32 v31, v31\n"
    "v_mfma_f32_16x16x32_bf16 v[24:27], a[92:95], a[44:47], v[24:27]\n"
    "v_cvt_pk_bf16_f32 v104, v28, v29\n"
    "v_cvt_pk_bf16_f32 v105, v30, v31\n"
    "v_mfma_f32_16x16x32_bf16 v[28:31], a[92:95], a[60:63], 4.0\n"
    "v_exp_f32 v16, v16\n"
    "v_mfma_f32_16x16x32_bf16 v[28:31], a[88:91], a[56:59], v[28:31]\n"
    "ds_read_b128 a[108:111], v7 offset:27648\n"
    "v_mfma_f32_16x16x32_bf16 v[28:31], a[84:87], a[52:55], v[28:31]\n"
    "v_exp_f32 v17, v17\n"
    "v_mfma_f32_16x16x32_bf16 v[28:31], a[80:83], a[48:51], v[28:31]\n"
    "v_mfma_f32_16x16x32_bf16 v[160:163], v[48:51], a[112:115], v[160:163]\n"
    "v_exp_f32 v18, v18\n"
    "v_mfma_f32_16x16x32_bf16 v[160:163], v[52:55], a[116:119], v[160:163]\n"
    "ds_read_b128 a[80:83], v5 offset:12288\n"
    "v_mfma_f32_16x16x32_bf16 v[164:167], v[60:63], a[116:119], v[164:167]\n"
    "v_exp_f32 v19, v19\n"
    "v_mfma_f32_16x16x32_bf16 v[164:167], v[56:59], a[112:115], v[164:167]\n"
    "v_cvt_pk_bf16_f32 v82, v16, v17\n"
    "v_cvt_pk_bf16_f32 v83, v18, v19\n"
    "v_mfma_f32_16x16x32_bf16 v[168:171], v[64:67], a[112:115], v[168:171]\n"
    "v_exp_f32 v20, v20\n"
    "v_mfma_f32_16x16x32_bf16 v[168:171], v[68:71], a[116:119], v[168:171]\n"
    "ds_read_b128 a[84:87], v5 offset:13312\n"
    "v_mfma_f32_16x16x32_bf16 v[172:175], v[76:79], a[116:119], v[172:175]\n"
    "global_load_lds_dwordx4 v9, s[62:63] offset:1024 sc1\n"
    "v_mfma_f32_16x16x32_bf16 v[172:175], v[72:75], a[112:115], v[172:175]\n"
    "v_exp_f32 v21, v21\n"
    "v_mfma_f32_16x16x32_bf16 v[188:191], v[72:75], a[120:123], v[188:191]\n"
    "v_exp_f32 v22, v22\n"
    "v_mfma_f32_16x16x32_bf16 v[188:191], v[76:79], a[124:127], v[188:191]\n"
    "ds_read_b128 a[88:91], v5 offset:14336\n"
    "v_mfma_f32_16x16x32_bf16 v[184:187], v[68:71], a[124:127], v[184:187]\n"
    "v_exp_f32 v23, v23\n"
    "v_mfma_f32_16x16x32_bf16 v[184:187], v[64:67], a[120:123], v[184:187]\n"
    "v_cvt_pk_bf16_f32 v90, v20, v21\n"
    "v_cvt_pk_bf16_f32 v91, v22, v23\n"
    "v_mfma_f32_16x16x32_bf16 v[180:183], v[56:59], a[120:123], v[180:183]\n"
    "v_exp_f32 v24, v24\n"
    "v_mfma_f32_16x16x32_bf16 v[180:183], v[60:63], a[124:127], v[180:183]\n"
    "ds_read_b128 a[92:95], v5 offset:15360\n"
    "v_mfma_f32_16x16x32_bf16 v[176:179], v[52:55], a[124:127], v[176:179]\n"
    "v_exp_f32 v25, v25\n"
    "v_mfma_f32_16x16x32_bf16 v[176:179], v[48:51], a[120:123], v[176:179]\n"
    "v_smfmac_f32_16x16x64_bf16 a[132:135], a[144:147], v[56:63], v10\n"
    "s_waitcnt lgkmcnt(4)\n"
    "s_branch L%=_body\n"
    "L%=_p1_retry:\n"
    "s_sub_u32 s67, s67, 1\n"
    "s_cbranch_scc1 L%=_p1_to\n"
    "v_accvgpr_read_b32 v2, a155\n"
    "global_load_dwordx2 v[2:3], v2, s[38:39] sc1\n"
    "s_waitcnt vmcnt(0)\n"
    "v_cmp_ne_u64 vcc, s[72:73], v[2:3]\n"
    "s_and_b32 s78, vcc_lo, 0xf\n"
    "s_cbranch_scc1 L%=_p1_retry\n"
    "s_branch L%=_p1_ok\n"
    "L%=_p1_to:\n"
    "s_mov_b32 s67, 256\n"
    "s_or_b32 s71, s71, 6\n"
    "s_branch L%=_p1_ok\n"
    "L%=_p2_retry:\n"
    "s_sub_u32 s67, s67, 1\n"
    "s_cbranch_scc1 L%=_p2_to\n"
    "v_accvgpr_read_b32 v2, a155\n"
    "global_load_dwordx2 v[2:3], v2, s[38:39] sc1\n"
    "s_waitcnt vmcnt(0)\n"
    "v_cmp_ne_u64 vcc, s[72:73], v[2:3]\n"
    "s_and_b32 s78, vcc_lo, 0xffffffff\n"
    "s_cbranch_scc1 L%=_p2_retry\n"
    "s_branch L%=_p2_ok\n"
    "L%=_p2_to:\n"
    "s_mov_b32 s67, 256\n"
    "s_or_b32 s71, s71, 6\n"
    "s_branch L%=_p2_ok\n"
    "L%=_p3_retry:\n"
    "s_sub_u32 s67, s67, 1\n"
    "s_cbranch_scc1 L%=_p3_to\n"
    "v_accvgpr_read_b32 v2, a155\n"
    "global_load_dwordx2 v[2:3], v2, s[38:39] sc1\n"
    "s_waitcnt vmcnt(0)\n"
    "v_cmp_ne_u64 vcc, s[72:73], v[2:3]\n"
    "s_and_b32 s78, vcc_lo, 0xffffffff\n"
    "s_cbranch_scc1 L%=_p3_retry\n"
    "s_branch L%=_p3_ok\n"
    "L%=_p3_to:\n"
    "s_mov_b32 s67, 256\n"
    "s_or_b32 s71, s71, 6\n"
    "s_branch L%=_p3_ok\n"
    "L%=_tail:\n"
    "v_mfma_f32_16x16x32_bf16 v[16:19], a[64:67], a[0:3], 4.0\n"
    "L%=_w8:\n"
    "s_cbranch_execz L%=_wret\n"
    "v_exp_f32 v26, v26\n"
    "v_mfma_f32_16x16x32_bf16 v[16:19], a[68:71], a[4:7], v[16:19]\n"
    "ds_read_b128 a[112:115], v6 offset:53248\n"
    "v_mfma_f32_16x16x32_bf16 v[16:19], a[72:75], a[8:11], v[16:19]\n"
    "v_exp_f32 v27, v27\n"
    "v_mfma_f32_16x16x32_bf16 v[16:19], a[76:79], a[12:15], v[16:19]\n"
    "v_cvt_pk_bf16_f32 v102, v24, v25\n"
    "v_cvt_pk_bf16_f32 v103, v26, v27\n"
    "v_mfma_f32_16x16x32_bf16 v[20:23], a[76:79], a[28:31], 4.0\n"
    "v_exp_f32 v28, v28\n"
    "v_mfma_f32_16x16x32_bf16 v[20:23], a[72:75], a[24:27], v[20:23]\n"
    "ds_read_b128 a[116:119], v6 offset:54272\n"
    "v_mfma_f32_16x16x32_bf16 v[20:23], a[68:71], a[20:23], v[20:23]\n"
    "global_load_lds_dwordx4 v8, s[62:63] offset:2048 sc1\n"
    "v_mfma_f32_16x16x32_bf16 v[20:23], a[64:67], a[16:19], v[20:23]\n"
    "v_exp_f32 v29, v29\n"
    "v_mfma_f32_16x16x32_bf16 v[24:27], a[64:67], a[32:35], 4.0\n"
    "v_exp_f32 v30, v30\n"
    "v_mfma_f32_16x16x32_bf16 v[24:27], a[68:71], a[36:39], v[24:27]\n"
    "ds_read_b128 a[120:123], v6 offset:55296\n"
    "v_mfma_f32_16x16x32_bf16 v[24:27], a[72:75], a[40:43], v[24:27]\n"
    "v_exp_f32 v31, v31\n"
    "v_mfma_f32_16x16x32_bf16 v[24:27], a[76:79], a[44:47], v[24:27]\n"
    "v_cvt_pk_bf16_f32 v110, v28, v29\n"
    "v_cvt_pk_bf16_f32 v111, v30, v31\n"
    "v_mfma_f32_16x16x32_bf16 v[28:31], a[76:79], a[60:63], 4.0\n"
    "v_exp_f32 v16, v16\n"
    "v_mfma_f32_16x16x32_bf16 v[28:31], a[72:75], a[56:59], v[28:31]\n"
    "ds_read_b128 a[124:127], v6 offset:56320\n"
    "v_mfma_f32_16x16x32_bf16 v[28:31], a[68:71], a[52:55], v[28:31]\n"
    "v_exp_f32 v17, v17\n"
    "v_mfma_f32_16x16x32_bf16 v[28:31], a[64:67], a[48:51], v[28:31]\n"
    "v_mfma_f32_16x16x32_bf16 v[128:131], v[80:83], a[96:99], v[128:131]\n"
    "v_exp_f32 v18, v18\n"
    "v_mfma_f32_16x16x32_bf16 v[128:131], v[84:87], a[100:103], v[128:131]\n"
    "ds_read_b128 a[64:67], v4 offset:40960\n"
    "v_mfma_f32_16x16x32_bf16 v[132:135], v[92:95], a[100:103], v[132:135]\n"
    "v_exp_f32 v19, v19\n"
    "v_mfma_f32_16x16x32_bf16 v[132:135], v[88:91], a[96:99], v[132:135]\n"
    "v_cvt_pk_bf16_f32 v48, v16, v17\n"
    "v_cvt_pk_bf16_f32 v49, v18, v19\n"
    "v_mfma_f32_16x16x32_bf16 v[136:139], v[96:99], a[96:99], v[136:139]\n"
    "v_exp_f32 v20, v20\n"
    "v_mfma_f32_16x16x32_bf16 v[136:139], v[100:103], a[100:103], v[136:139]\n"
    "ds_read_b128 a[68:71], v4 offset:41984\n"
    "v_mfma_f32_16x16x32_bf16 v[140:143], v[108:111], a[100:103], v[140:143]\n"
    "global_load_lds_dwordx4 v8, s[62:63] offset:3072 sc1\n"
    "v_mfma_f32_16x16x32_bf16 v[140:143], v[104:107], a[96:99], v[140:143]\n"
    "v_exp_f32 v21, v21\n"
    "v_mfma_f32_16x16x32_bf16 v[156:159], v[104:107], a[104:107], v[156:159]\n"
    "v_exp_f32 v22, v22\n"
    "v_mfma_f32_16x16x32_bf16 v[156:159], v[108:111], a[108:111], v[156:159]\n"
    "ds_read_b128 a[72:75], v4 offset:43008\n"
    "v_mfma_f32_16x16x32_bf16 v[152:155], v[100:103], a[108:111], v[152:155]\n"
    "v_exp_f32 v23, v23\n"
    "v_mfma_f32_16x16x32_bf16 v[152:155], v[96:99], a[104:107], v[152:155]\n"
    "v_cvt_pk_bf16_f32 v56, v20, v21\n"
    "v_cvt_pk_bf16_f32 v57, v22, v23\n"
    "v_mfma_f32_16x16x32_bf16 v[148:151], v[88:91], a[104:107], v[148:151]\n"
    "s_add_u32 m0, s69, 114688\n"
    "v_exp_f32 v24, v24\n"
    "v_mfma_f32_16x16x32_bf16 v[148:151], v[92:95], a[108:111], v[148:151]\n"
    "ds_read_b128 a[76:79], v4 offset:44032\n"
    "v_mfma_f32_16x16x32_bf16 v[144:147], v[84:87], a[108:111], v[144:147]\n"
    "v_exp_f32 v25, v25\n"
    "v_mfma_f32_16x16x32_bf16 v[144:147], v[80:83], a[104:107], v[144:147]\n"
    "v_smfmac_f32_16x16x64_bf16 a[128:131], a[144:147], v[80:87], v10\n"
    "s_waitcnt lgkmcnt(4)\n"
    "v_mfma_f32_16x16x32_bf16 v[16:19], a[80:83], a[0:3], 4.0\n"
    "s_add_u32 s75, s74, 0\n"
    "v_exp_f32 v26, v26\n"
    "v_mfma_f32_16x16x32_bf16 v[16:19], a[84:87], a[4:7], v[16:19]\n"
    "ds_read_b128 a[96:99], v6 offset:57344\n"
    "v_mfma_f32_16x16x32_bf16 v[16:19], a[88:91], a[8:11], v[16:19]\n"
    "s_and_b32 s75, s75, 3\n"
    "v_exp_f32 v27, v27\n"
    "v_mfma_f32_16x16x32_bf16 v[16:19], a[92:95], a[12:15], v[16:19]\n"
    "v_cvt_pk_bf16_f32 v64, v24, v25\n"
    "v_cvt_pk_bf16_f32 v65, v26, v27\n"
    "v_mfma_f32_16x16x32_bf16 v[20:23], a[92:95], a[28:31], 4.0\n"
    "s_lshl_b32 s75, s75, 12\n"
    "v_exp_f32 v28, v28\n"
    "v_mfma_f32_16x16x32_bf16 v[20:23], a[88:91], a[24:27], v[20:23]\n"
    "ds_read_b128 a[100:103], v6 offset:58368\n"
    "v_mfma_f32_16x16x32_bf16 v[20:23], a[84:87], a[20:23], v[20:23]\n"
    "global_load_lds_dwordx4 v9, s[62:63] offset:0 sc1\n"
    "v_mfma_f32_16x16x32_bf16 v[20:23], a[80:83], a[16:19], v[20:23]\n"
    "v_exp_f32 v29, v29\n"
    "v_mfma_f32_16x16x32_bf16 v[24:27], a[80:83], a[32:35], 4.0\n"
    "s_add_u32 s42, s54, s75\n"
    "v_exp_f32 v30, v30\n"
    "v_mfma_f32_16x16x32_bf16 v[24:27], a[84:87], a[36:39], v[24:27]\n"
    "ds_read_b128 a[104:107], v6 offset:59392\n"
    "v_mfma_f32_16x16x32_bf16 v[24:27], a[88:91], a[40:43], v[24:27]\n"
    "s_addc_u32 s43, s55, 0\n"
    "v_exp_f32 v31, v31\n"
    "v_mfma_f32_16x16x32_bf16 v[24:27], a[92:95], a[44:47], v[24:27]\n"
    "v_cvt_pk_bf16_f32 v72, v28, v29\n"
    "v_cvt_pk_bf16_f32 v73, v30, v31\n"
    "v_mfma_f32_16x16x32_bf16 v[28:31], a[92:95], a[60:63], 4.0\n"
    "v_exp_f32 v16, v16\n"
    "v_mfma_f32_16x16x32_bf16 v[28:31], a[88:91], a[56:59], v[28:31]\n"
    "ds_read_b128 a[108:111], v6 offset:60416\n"
    "v_mfma_f32_16x16x32_bf16 v[28:31], a[84:87], a[52:55], v[28:31]\n"
    "v_exp_f32 v17, v17\n"
    "v_mfma_f32_16x16x32_bf16 v[28:31], a[80:83], a[48:51], v[28:31]\n"
    "v_mfma_f32_16x16x32_bf16 v[160:163], v[80:83], a[112:115], v[160:163]\n"
    "v_exp_f32 v18, v18\n"
    "v_mfma_f32_16x16x32_bf16 v[160:163], v[84:87], a[116:119], v[160:163]\n"
    "ds_read_b128 a[80:83], v4 offset:45056\n"
    "v_mfma_f32_16x16x32_bf16 v[164:167], v[92:95], a[116:119], v[164:167]\n"
    "v_exp_f32 v19, v19\n"
    "v_mfma_f32_16x16x32_bf16 v[164:167], v[88:91], a[112:115], v[164:167]\n"
    "v_cvt_pk_bf16_f32 v50, v16, v17\n"
    "v_cvt_pk_bf16_f32 v51, v18, v19\n"
    "v_mfma_f32_16x16x32_bf16 v[168:171], v[96:99], a[112:115], v[168:171]\n"
    "v_exp_f32 v20, v20\n"
    "v_mfma_f32_16x16x32_bf16 v[168:171], v[100:103], a[116:119], v[168:171]\n"
    "ds_read_b128 a[84:87], v4 offset:46080\n"
    "v_mfma_f32_16x16x32_bf16 v[172:175], v[108:111], a[116:119], v[172:175]\n"
    "global_load_lds_dwordx4 v9, s[62:63] offset:1024 sc1\n"
    "v_mfma_f32_16x16x32_bf16 v[172:175], v[104:107], a[112:115], v[172:175]\n"
    "v_exp_f32 v21, v21\n"
    "v_mfma_f32_16x16x32_bf16 v[188:191], v[104:107], a[120:123], v[188:191]\n"
    "v_exp_f32 v22, v22\n"
    "v_mfma_f32_16x16x32_bf16 v[188:191], v[108:111], a[124:127], v[188:191]\n"
    "ds_read_b128 a[88:91], v4 offset:47104\n"
    "v_mfma_f32_16x16x32_bf16 v[184:187], v[100:103], a[124:127], v[184:187]\n"
    "v_exp_f32 v23, v23\n"
    "v_mfma_f32_16x16x32_bf16 v[184:187], v[96:99], a[120:123], v[184:187]\n"
    "v_cvt_pk_bf16_f32 v58, v20, v21\n"
    "v_cvt_pk_bf16_f32 v59, v22, v23\n"
    "v_mfma_f32_16x16x32_bf16 v[180:183], v[88:91], a[120:123], v[180:183]\n"
    "v_exp_f32 v24, v24\n"
    "v_mfma_f32_16x16x32_bf16 v[180:183], v[92:95], a[124:127], v[180:183]\n"
    "ds_read_b128 a[92:95], v4 offset:48128\n"
    "v_mfma_f32_16x16x32_bf16 v[176:179], v[84:87], a[124:127], v[176:179]\n"
    "v_exp_f32 v25, v25\n"
    "v_mfma_f32_16x16x32_bf16 v[176:179], v[80:83], a[120:123], v[176:179]\n"
    "v_smfmac_f32_16x16x64_bf16 a[132:135], a[144:147], v[88:95], v10\n"
    "s_waitcnt lgkmcnt(4)\n"
    "v_mfma_f32_16x16x32_bf16 v[16:19], a[64:67], a[0:3], 4.0\n"
    "v_exp_f32 v26, v26\n"
    "v_mfma_f32_16x16x32_bf16 v[16:19], a[68:71], a[4:7], v[16:19]\n"
    "ds_read_b128 a[112:115], v6 offset:61440\n"
    "v_mfma_f32_16x16x32_bf16 v[16:19], a[72:75], a[8:11], v[16:19]\n"
    "v_exp_f32 v27, v27\n"
    "v_mfma_f32_16x16x32_bf16 v[16:19], a[76:79], a[12:15], v[16:19]\n"
    "v_cvt_pk_bf16_f32 v66, v24, v25\n"
    "v_cvt_pk_bf16_f32 v67, v26, v27\n"
    "v_mfma_f32_16x16x32_bf16 v[20:23], a[76:79], a[28:31], 4.0\n"
    "v_exp_f32 v28, v28\n"
    "v_mfma_f32_16x16x32_bf16 v[20:23], a[72:75], a[24:27], v[20:23]\n"
    "ds_read_b128 a[116:119], v6 offset:62464\n"
    "v_mfma_f32_16x16x32_bf16 v[20:23], a[68:71], a[20:23], v[20:23]\n"
    "global_load_lds_dwordx4 v9, s[62:63] offset:2048 sc1\n"
    "v_mfma_f32_16x16x32_bf16 v[20:23], a[64:67], a[16:19], v[20:23]\n"
    "v_exp_f32 v29, v29\n"
    "v_mfma_f32_16x16x32_bf16 v[24:27], a[64:67], a[32:35], 4.0\n"
    "v_exp_f32 v30, v30\n"
    "v_mfma_f32_16x16x32_bf16 v[24:27], a[68:71], a[36:39], v[24:27]\n"
    "ds_read_b128 a[120:123], v6 offset:63488\n"
    "v_mfma_f32_16x16x32_bf16 v[24:27], a[72:75], a[40:43], v[24:27]\n"
    "v_exp_f32 v31, v31\n"
    "v_mfma_f32_16x16x32_bf16 v[24:27], a[76:79], a[44:47], v[24:27]\n"
    "v_cvt_pk_bf16_f32 v74, v28, v29\n"
    "v_cvt_pk_bf16_f32 v75, v30, v31\n"
    "v_mfma_f32_16x16x32_bf16 v[28:31], a[76:79], a[60:63], 4.0\n"
    "v_exp_f32 v16, v16\n"
    "v_mfma_f32_16x16x32_bf16 v[28:31], a[72:75], a[56:59], v[28:31]\n"
    "ds_read_b128 a[124:127], v6 offset:64512\n"
    "v_mfma_f32_16x16x32_bf16 v[28:31], a[68:71], a[52:55], v[28:31]\n"
    "s_waitcnt vmcnt(7)\n"
    "v_exp_f32 v17, v17\n"
    "v_mfma_f32_16x16x32_bf16 v[28:31], a[64:67], a[48:51], v[28:31]\n"
    "s_barrier\n"
    "v_mfma_f32_16x16x32_bf16 v[192:195], v[80:83], a[96:99], v[192:195]\n"
    "v_exp_f32 v18, v18\n"
    "v_mfma_f32_16x16x32_bf16 v[192:195], v[84:87], a[100:103], v[192:195]\n"
    "ds_read_b128 a[64:67], v5 offset:0\n"
    "v_mfma_f32_16x16x32_bf16 v[196:199], v[92:95], a[100:103], v[196:199]\n"
    "v_exp_f32 v19, v19\n"
    "v_mfma_f32_16x16x32_bf16 v[196:199], v[88:91], a[96:99], v[196:199]\n"
    "v_cvt_pk_bf16_f32 v52, v16, v17\n"
    "v_cvt_pk_bf16_f32 v53, v18, v19\n"
    "v_mfma_f32_16x16x32_bf16 v[200:203], v[96:99], a[96:99], v[200:203]\n"
    "v_exp_f32 v20, v20\n"
    "v_mfma_f32_16x16x32_bf16 v[200:203], v[100:103], a[100:103], v[200:203]\n"
    "ds_read_b128 a[68:71], v5 offset:1024\n"
    "v_mfma_f32_16x16x32_bf16 v[204:207], v[108:111], a[100:103], v[204:207]\n"
    "global_load_lds_dwordx4 v9, s[62:63] offset:3072 sc1\n"
    "v_mfma_f32_16x16x32_bf16 v[204:207], v[104:107], a[96:99], v[204:207]\n"
    "v_exp_f32 v21, v21\n"
    "v_mfma_f32_16x16x32_bf16 v[220:223], v[104:107], a[104:107], v[220:223]\n"
    "v_exp_f32 v22, v22\n"
    "v_mfma_f32_16x16x32_bf16 v[220:223], v[108:111], a[108:111], v[220:223]\n"
    "ds_read_b128 a[72:75], v5 offset:2048\n"
    "v_mfma_f32_16x16x32_bf16 v[216:219], v[100:103], a[108:111], v[216:219]\n"
    "s_add_u32 s62, s62, 0x8000\n"
    "v_exp_f32 v23, v23\n"
    "v_mfma_f32_16x16x32_bf16 v[216:219], v[96:99], a[104:107], v[216:219]\n"
    "v_cvt_pk_bf16_f32 v60, v20, v21\n"
    "v_cvt_pk_bf16_f32 v61, v22, v23\n"
    "v_mfma_f32_16x16x32_bf16 v[212:215], v[88:91], a[104:107], v[212:215]\n"
    "s_add_u32 m0, s69, 0\n"
    "v_exp_f32 v24, v24\n"
    "v_mfma_f32_16x16x32_bf16 v[212:215], v[92:95], a[108:111], v[212:215]\n"
    "ds_read_b128 a[76:79], v5 offset:3072\n"
    "v_mfma_f32_16x16x32_bf16 v[208:211], v[84:87], a[108:111], v[208:211]\n"
    "v_exp_f32 v25, v25\n"
    "v_mfma_f32_16x16x32_bf16 v[208:211], v[80:83], a[104:107], v[208:211]\n"
    "v_smfmac_f32_16x16x64_bf16 a[136:139], a[144:147], v[96:103], v10\n"
    "s_waitcnt lgkmcnt(4)\n"
    "v_mfma_f32_16x16x32_bf16 v[16:19], a[80:83], a[0:3], 4.0\n"
    "s_lshl_b32 s61, %3, 10\n"
    "v_exp_f32 v26, v26\n"
    "v_mfma_f32_16x16x32_bf16 v[16:19], a[84:87], a[4:7], v[16:19]\n"
    "ds_read_b128 a[96:99], v7 offset:16384\n"
    "v_mfma_f32_16x16x32_bf16 v[16:19], a[88:91], a[8:11], v[16:19]\n"
    "v_exp_f32 v27, v27\n"
    "v_mfma_f32_16x16x32_bf16 v[16:19], a[92:95], a[12:15], v[16:19]\n"
    "v_cvt_pk_bf16_f32 v68, v24, v25\n"
    "v_cvt_pk_bf16_f32 v69, v26, v27\n"
    "v_mfma_f32_16x16x32_bf16 v[20:23], a[92:95], a[28:31], 4.0\n"
    "v_exp_f32 v28, v28\n"
    "v_mfma_f32_16x16x32_bf16 v[20:23], a[88:91], a[24:27], v[20:23]\n"
    "ds_read_b128 a[100:103], v7 offset:17408\n"
    "v_mfma_f32_16x16x32_bf16 v[20:23], a[84:87], a[20:23], v[20:23]\n"
    "global_load_lds_dwordx4 v8, s[62:63] offset:0 sc1\n"
    "v_mfma_f32_16x16x32_bf16 v[20:23], a[80:83], a[16:19], v[20:23]\n"
    "v_exp_f32 v29, v29\n"
    "v_mfma_f32_16x16x32_bf16 v[24:27], a[80:83], a[32:35], 4.0\n"
    "v_exp_f32 v30, v30\n"
    "v_mfma_f32_16x16x32_bf16 v[24:27], a[84:87], a[36:39], v[24:27]\n"
    "ds_read_b128 a[104:107], v7 offset:18432\n"
    "v_mfma_f32_16x16x32_bf16 v[24:27], a[88:91], a[40:43], v[24:27]\n"
    "v_exp_f32 v31, v31\n"
    "v_mfma_f32_16x16x32_bf16 v[24:27], a[92:95], a[44:47], v[24:27]\n"
    "v_cvt_pk_bf16_f32 v76, v28, v29\n"
    "v_cvt_pk_bf16_f32 v77, v30, v31\n"
    "v_mfma_f32_16x16x32_bf16 v[28:31], a[92:95], a[60:63], 4.0\n"
    "v_exp_f32 v16, v16\n"
    "v_mfma_f32_16x16x32_bf16 v[28:31], a[88:91], a[56:59], v[28:31]\n"
    "ds_read_b128 a[108:111], v7 offset:19456\n"
    "v_mfma_f32_16x16x32_bf16 v[28:31], a[84:87], a[52:55], v[28:31]\n"
    "v_exp_f32 v17, v17\n"
    "v_mfma_f32_16x16x32_bf16 v[28:31], a[80:83], a[48:51], v[28:31]\n"
    "v_mfma_f32_16x16x32_bf16 v[224:227], v[80:83], a[112:115], v[224:227]\n"
    "v_exp_f32 v18, v18\n"
    "v_mfma_f32_16x16x32_bf16 v[224:227], v[84:87], a[116:119], v[224:227]\n"
    "ds_read_b128 a[80:83], v5 offset:4096\n"
    "v_mfma_f32_16x16x32_bf16 v[228:231], v[92:95], a[116:119], v[228:231]\n"
    "v_exp_f32 v19, v19\n"
    "v_mfma_f32_16x16x32_bf16 v[228:231], v[88:91], a[112:115], v[228:231]\n"
    "v_cvt_pk_bf16_f32 v54, v16, v17\n"
    "v_cvt_pk_bf16_f32 v55, v18, v19\n"
    "v_mfma_f32_16x16x32_bf16 v[232:235], v[96:99], a[112:115], v[232:235]\n"
    "v_exp_f32 v20, v20\n"
    "v_mfma_f32_16x16x32_bf16 v[232:235], v[100:103], a[116:119], v[232:235]\n"
    "ds_read_b128 a[84:87], v5 offset:5120\n"
    "v_mfma_f32_16x16x32_bf16 v[236:239], v[108:111], a[116:119], v[236:239]\n"
    "global_load_lds_dwordx4 v8, s[62:63] offset:1024 sc1\n"
    "v_mfma_f32_16x16x32_bf16 v[236:239], v[104:107], a[112:115], v[236:239]\n"
    "v_exp_f32 v21, v21\n"
    "v_mfma_f32_16x16x32_bf16 v[252:255], v[104:107], a[120:123], v[252:255]\n"
    "v_exp_f32 v22, v22\n"
    "v_mfma_f32_16x16x32_bf16 v[252:255], v[108:111], a[124:127], v[252:255]\n"
    "ds_read_b128 a[88:91], v5 offset:6144\n"
    "v_mfma_f32_16x16x32_bf16 v[248:251], v[100:103], a[124:127], v[248:251]\n"
    "v_exp_f32 v23, v23\n"
    "v_mfma_f32_16x16x32_bf16 v[248:251], v[96:99], a[120:123], v[248:251]\n"
    "v_cvt_pk_bf16_f32 v62, v20, v21\n"
    "v_cvt_pk_bf16_f32 v63, v22, v23\n"
    "v_mfma_f32_16x16x32_bf16 v[244:247], v[88:91], a[120:123], v[244:247]\n"
    "v_exp_f32 v24, v24\n"
    "v_mfma_f32_16x16x32_bf16 v[244:247], v[92:95], a[124:127], v[244:247]\n"
    "ds_read_b128 a[92:95], v5 offset:7168\n"
    "v_mfma_f32_16x16x32_bf16 v[240:243], v[84:87], a[124:127], v[240:243]\n"
    "v_exp_f32 v25, v25\n"
    "v_mfma_f32_16x16x32_bf16 v[240:243], v[80:83], a[120:123], v[240:243]\n"
    "v_smfmac_f32_16x16x64_bf16 a[140:143], a[144:147], v[104:111], v10\n"
    "s_waitcnt lgkmcnt(4)\n"
    "v_add_u32 v2, s61, v6\n"
    "v_mfma_f32_16x16x32_bf16 v[16:19], a[64:67], a[0:3], 4.0\n"
    "v_exp_f32 v26, v26\n"
    "v_mfma_f32_16x16x32_bf16 v[16:19], a[68:71], a[4:7], v[16:19]\n"
    "ds_read_b128 a[112:115], v7 offset:20480\n"
    "v_mfma_f32_16x16x32_bf16 v[16:19], a[72:75], a[8:11], v[16:19]\n"
    "v_exp_f32 v27, v27\n"
    "v_mfma_f32_16x16x32_bf16 v[16:19], a[76:79], a[12:15], v[16:19]\n"
    "v_cvt_pk_bf16_f32 v70, v24, v25\n"
    "v_cvt_pk_bf16_f32 v71, v26, v27\n"
    "v_mfma_f32_16x16x32_bf16 v[20:23], a[76:79], a[28:31], 4.0\n"
    "v_exp_f32 v28, v28\n"
    "v_mfma_f32_16x16x32_bf16 v[20:23], a[72:75], a[24:27], v[20:23]\n"
    "ds_read_b128 a[116:119], v7 offset:21504\n"
    "v_mfma_f32_16x16x32_bf16 v[20:23], a[68:71], a[20:23], v[20:23]\n"
    "global_load_lds_dwordx4 v8, s[62:63] offset:2048 sc1\n"
    "v_mfma_f32_16x16x32_bf16 v[20:23], a[64:67], a[16:19], v[20:23]\n"
    "v_exp_f32 v29, v29\n"
    "v_mfma_f32_16x16x32_bf16 v[24:27], a[64:67], a[32:35], 4.0\n"
    "v_exp_f32 v30, v30\n"
    "v_mfma_f32_16x16x32_bf16 v[24:27], a[68:71], a[36:39], v[24:27]\n"
    "ds_read_b128 a[120:123], v7 offset:22528\n"
    "v_mfma_f32_16x16x32_bf16 v[24:27], a[72:75], a[40:43], v[24:27]\n"
    "v_exp_f32 v31, v31\n"
    "v_mfma_f32_16x16x32_bf16 v[24:27], a[76:79], a[44:47], v[24:27]\n"
    "v_cvt_pk_bf16_f32 v78, v28, v29\n"
    "v_cvt_pk_bf16_f32 v79, v30, v31\n"
    "v_mfma_f32_16x16x32_bf16 v[28:31], a[76:79], a[60:63], 4.0\n"
    "v_exp_f32 v16, v16\n"
    "v_mfma_f32_16x16x32_bf16 v[28:31], a[72:75], a[56:59], v[28:31]\n"
    "ds_read_b128 a[124:127], v7 offset:23552\n"
    "v_mfma_f32_16x16x32_bf16 v[28:31], a[68:71], a[52:55], v[28:31]\n"
    "v_exp_f32 v17, v17\n"
    "v_mfma_f32_16x16x32_bf16 v[28:31], a[64:67], a[48:51], v[28:31]\n"
    "v_mfma_f32_16x16x32_bf16 v[128:131], v[48:51], a[96:99], v[128:131]\n"
    "v_exp_f32 v18, v18\n"
    "v_mfma_f32_16x16x32_bf16 v[128:131], v[52:55], a[100:103], v[128:131]\n"
    "ds_read_b128 a[64:67], v5 offset:8192\n"
    "v_mfma_f32_16x16x32_bf16 v[132:135], v[60:63], a[100:103], v[132:135]\n"
    "v_exp_f32 v19, v19\n"
    "v_mfma_f32_16x16x32_bf16 v[132:135], v[56:59], a[96:99], v[132:135]\n"
    "v_cvt_pk_bf16_f32 v80, v16, v17\n"
    "v_cvt_pk_bf16_f32 v81, v18, v19\n"
    "v_mfma_f32_16x16x32_bf16 v[136:139], v[64:67], a[96:99], v[136:139]\n"
    "v_exp_f32 v20, v20\n"
    "v_mfma_f32_16x16x32_bf16 v[136:139], v[68:71], a[100:103], v[136:139]\n"
    "ds_read_b128 a[68:71], v5 offset:9216\n"
    "v_mfma_f32_16x16x32_bf16 v[140:143], v[76:79], a[100:103], v[140:143]\n"
    "global_load_lds_dwordx4 v8, s[62:63] offset:3072 sc1\n"
    "v_mfma_f32_16x16x32_bf16 v[140:143], v[72:75], a[96:99], v[140:143]\n"
    "v_exp_f32 v21, v21\n"
    "v_mfma_f32_16x16x32_bf16 v[156:159], v[72:75], a[104:107], v[156:159]\n"
    "v_exp_f32 v22, v22\n"
    "v_mfma_f32_16x16x32_bf16 v[156:159], v[76:79], a[108:111], v[156:159]\n"
    "ds_read_b128 a[72:75], v5 offset:10240\n"
    "v_mfma_f32_16x16x32_bf16 v[152:155], v[68:71], a[108:111], v[152:155]\n"
    "v_exp_f32 v23, v23\n"
    "v_mfma_f32_16x16x32_bf16 v[152:155], v[64:67], a[104:107], v[152:155]\n"
    "v_cvt_pk_bf16_f32 v88, v20, v21\n"
    "v_cvt_pk_bf16_f32 v89, v22, v23\n"
    "v_mfma_f32_16x16x32_bf16 v[148:151], v[56:59], a[104:107], v[148:151]\n"
    "L%=_w9:\n"
    "s_cbranch_execz L%=_wret\n"
    "s_add_u32 m0, s69, 16384\n"
    "v_exp_f32 v24, v24\n"
    "v_mfma_f32_16x16x32_bf16 v[148:151], v[60:63], a[108:111], v[148:151]\n"
    "ds_read_b128 a[76:79], v5 offset:11264\n"
    "v_mfma_f32_16x16x32_bf16 v[144:147], v[52:55], a[108:111], v[144:147]\n"
    "v_exp_f32 v25, v25\n"
    "v_mfma_f32_16x16x32_bf16 v[144:147], v[48:51], a[104:107], v[144:147]\n"
    "v_smfmac_f32_16x16x64_bf16 a[128:131], a[144:147], v[48:55], v10\n"
    "s_waitcnt lgkmcnt(4)\n"
    "v_mfma_f32_16x16x32_bf16 v[16:19], a[80:83], a[0:3], 4.0\n"
    "s_add_u32 s75, s74, 1\n"
    "v_exp_f32 v26, v26\n"
    "v_mfma_f32_16x16x32_bf16 v[16:19], a[84:87], a[4:7], v[16:19]\n"
    "ds_read_b128 a[96:99], v7 offset:24576\n"
    "v_mfma_f32_16x16x32_bf16 v[16:19], a[88:91], a[8:11], v[16:19]\n"
    "s_and_b32 s75, s75, 3\n"
    "v_exp_f32 v27, v27\n"
    "v_mfma_f32_16x16x32_bf16 v[16:19], a[92:95], a[12:15], v[16:19]\n"
    "v_cvt_pk_bf16_f32 v96, v24, v25\n"
    "v_cvt_pk_bf16_f32 v97, v26, v27\n"
    "v_mfma_f32_16x16x32_bf16 v[20:23], a[92:95], a[28:31], 4.0\n"
    "s_lshl_b32 s75, s75, 12\n"
    "v_exp_f32 v28, v28\n"
    "v_mfma_f32_16x16x32_bf16 v[20:23], a[88:91], a[24:27], v[20:23]\n"
    "ds_read_b128 a[100:103], v7 offset:25600\n"
    "v_mfma_f32_16x16x32_bf16 v[20:23], a[84:87], a[20:23], v[20:23]\n"
    "global_load_lds_dwordx4 v9, s[62:63] offset:0 sc1\n"
    "v_mfma_f32_16x16x32_bf16 v[20:23], a[80:83], a[16:19], v[20:23]\n"
    "v_exp_f32 v29, v29\n"
    "v_mfma_f32_16x16x32_bf16 v[24:27], a[80:83], a[32:35], 4.0\n"
    "s_add_u32 s44, s54, s75\n"
    "v_exp_f32 v30, v30\n"
    "v_mfma_f32_16x16x32_bf16 v[24:27], a[84:87], a[36:39], v[24:27]\n"
    "ds_read_b128 a[104:107], v7 offset:26624\n"
    "v_mfma_f32_16x16x32_bf16 v[24:27], a[88:91], a[40:43], v[24:27]\n"
    "s_addc_u32 s45, s55, 0\n"
    "v_exp_f32 v31, v31\n"
    "v_mfma_f32_16x16x32_bf16 v[24:27], a[92:95], a[44:47], v[24:27]\n"
    "v_cvt_pk_bf16_f32 v104, v28, v29\n"
    "v_cvt_pk_bf16_f32 v105, v30, v31\n"
    "v_mfma_f32_16x16x32_bf16 v[28:31], a[92:95], a[60:63], 4.0\n"
    "v_exp_f32 v16, v16\n"
    "v_mfma_f32_16x16x32_bf16 v[28:31], a[88:91], a[56:59], v[28:31]\n"
    "ds_read_b128 a[108:111], v7 offset:27648\n"
    "v_mfma_f32_16x16x32_bf16 v[28:31], a[84:87], a[52:55], v[28:31]\n"
    "v_exp_f32 v17, v17\n"
    "v_mfma_f32_16x16x32_bf16 v[28:31], a[80:83], a[48:51], v[28:31]\n"
    "v_mfma_f32_16x16x32_bf16 v[160:163], v[48:51], a[112:115], v[160:163]\n"
    "v_exp_f32 v18, v18\n"
    "v_mfma_f32_16x16x32_bf16 v[160:163], v[52:55], a[116:119], v[160:163]\n"
    "ds_read_b128 a[80:83], v5 offset:12288\n"
    "v_mfma_f32_16x16x32_bf16 v[164:167], v[60:63], a[116:119], v[164:167]\n"
    "v_exp_f32 v19, v19\n"
    "v_mfma_f32_16x16x32_bf16 v[164:167], v[56:59], a[112:115], v[164:167]\n"
    "v_cvt_pk_bf16_f32 v82, v16, v17\n"
    "v_cvt_pk_bf16_f32 v83, v18, v19\n"
    "v_mfma_f32_16x16x32_bf16 v[168:171], v[64:67], a[112:115], v[168:171]\n"
    "v_exp_f32 v20, v20\n"
    "v_mfma_f32_16x16x32_bf16 v[168:171], v[68:71], a[116:119], v[168:171]\n"
    "ds_read_b128 a[84:87], v5 offset:13312\n"
    "v_mfma_f32_16x16x32_bf16 v[172:175], v[76:79], a[116:119], v[172:175]\n"
    "global_load_lds_dwordx4 v9, s[62:63] offset:1024 sc1\n"
    "v_mfma_f32_16x16x32_bf16 v[172:175], v[72:75], a[112:115], v[172:175]\n"
    "v_exp_f32 v21, v21\n"
    "v_mfma_f32_16x16x32_bf16 v[188:191], v[72:75], a[120:123], v[188:191]\n"
    "v_exp_f32 v22, v22\n"
    "v_mfma_f32_16x16x32_bf16 v[188:191], v[76:79], a[124:127], v[188:191]\n"
    "ds_read_b128 a[88:91], v5 offset:14336\n"
    "v_mfma_f32_16x16x32_bf16 v[184:187], v[68:71], a[124:127], v[184:187]\n"
    "v_exp_f32 v23, v23\n"
    "v_mfma_f32_16x16x32_bf16 v[184:187], v[64:67], a[120:123], v[184:187]\n"
    "v_cvt_pk_bf16_f32 v90, v20, v21\n"
    "v_cvt_pk_bf16_f32 v91, v22, v23\n"
    "v_mfma_f32_16x16x32_bf16 v[180:183], v[56:59], a[120:123], v[180:183]\n"
    "v_exp_f32 v24, v24\n"
    "v_mfma_f32_16x16x32_bf16 v[180:183], v[60:63], a[124:127], v[180:183]\n"
    "ds_read_b128 a[92:95], v5 offset:15360\n"
    "v_mfma_f32_16x16x32_bf16 v[176:179], v[52:55], a[124:127], v[176:179]\n"
    "v_exp_f32 v25, v25\n"
    "v_mfma_f32_16x16x32_bf16 v[176:179], v[48:51], a[120:123], v[176:179]\n"
    "v_smfmac_f32_16x16x64_bf16 a[132:135], a[144:147], v[56:63], v10\n"
    "s_waitcnt lgkmcnt(4)\n"
    "v_mfma_f32_16x16x32_bf16 v[16:19], a[64:67], a[0:3], 4.0\n"
    "v_exp_f32 v26, v26\n"
    "v_mfma_f32_16x16x32_bf16 v[16:19], a[68:71], a[4:7], v[16:19]\n"
    "ds_read_b128 a[112:115], v7 offset:28672\n"
    "v_mfma_f32_16x16x32_bf16 v[16:19], a[72:75], a[8:11], v[16:19]\n"
    "v_exp_f32 v27, v27\n"
    "v_mfma_f32_16x16x32_bf16 v[16:19], a[76:79], a[12:15], v[16:19]\n"
    "v_cvt_pk_bf16_f32 v98, v24, v25\n"
    "v_cvt_pk_bf16_f32 v99, v26, v27\n"
    "v_mfma_f32_16x16x32_bf16 v[20:23], a[76:79], a[28:31], 4.0\n"
    "v_exp_f32 v28, v28\n"
    "v_mfma_f32_16x16x32_bf16 v[20:23], a[72:75], a[24:27], v[20:23]\n"
    "ds_read_b128 a[116:119], v7 offset:29696\n"
    "v_mfma_f32_16x16x32_bf16 v[20:23], a[68:71], a[20:23], v[20:23]\n"
    "global_load_lds_dwordx4 v9, s[62:63] offset:2048 sc1\n"
    "v_mfma_f32_16x16x32_bf16 v[20:23], a[64:67], a[16:19], v[20:23]\n"
    "v_exp_f32 v29, v29\n"
    "v_mfma_f32_16x16x32_bf16 v[24:27], a[64:67], a[32:35], 4.0\n"
    "v_exp_f32 v30, v30\n"
    "v_mfma_f32_16x16x32_bf16 v[24:27], a[68:71], a[36:39], v[24:27]\n"
    "ds_read_b128 a[120:123], v7 offset:30720\n"
    "v_mfma_f32_16x16x32_bf16 v[24:27], a[72:75], a[40:43], v[24:27]\n"
    "v_exp_f32 v31, v31\n"
    "v_mfma_f32_16x16x32_bf16 v[24:27], a[76:79], a[44:47], v[24:27]\n"
    "v_cvt_pk_bf16_f32 v106, v28, v29\n"
    "v_cvt_pk_bf16_f32 v107, v30, v31\n"
    "v_mfma_f32_16x16x32_bf16 v[28:31], a[76:79], a[60:63], 4.0\n"
    "v_exp_f32 v16, v16\n"
    "v_mfma_f32_16x16x32_bf16 v[28:31], a[72:75], a[56:59], v[28:31]\n"
    "ds_read_b128 a[124:127], v7 offset:31744\n"
    "v_mfma_f32_16x16x32_bf16 v[28:31], a[68:71], a[52:55], v[28:31]\n"
    "s_waitcnt vmcnt(7)\n"
    "v_exp_f32 v17, v17\n"
    "v_mfma_f32_16x16x32_bf16 v[28:31], a[64:67], a[48:51], v[28:31]\n"
    "s_barrier\n"
    "v_mfma_f32_16x16x32_bf16 v[192:195], v[48:51], a[96:99], v[192:195]\n"
    "v_exp_f32 v18, v18\n"
    "v_mfma_f32_16x16x32_bf16 v[192:195], v[52:55], a[100:103], v[192:195]\n"
    "ds_read_b128 a[64:67], v5 offset:32768\n"
    "v_mfma_f32_16x16x32_bf16 v[196:199], v[60:63], a[100:103], v[196:199]\n"
    "v_exp_f32 v19, v19\n"
    "v_mfma_f32_16x16x32_bf16 v[196:199], v[56:59], a[96:99], v[196:199]\n"
    "v_cvt_pk_bf16_f32 v84, v16, v17\n"
    "v_cvt_pk_bf16_f32 v85, v18, v19\n"
    "v_mfma_f32_16x16x32_bf16 v[200:203], v[64:67], a[96:99], v[200:203]\n"
    "v_exp_f32 v20, v20\n"
    "v_mfma_f32_16x16x32_bf16 v[200:203], v[68:71], a[100:103], v[200:203]\n"
    "ds_read_b128 a[68:71], v5 offset:33792\n"
    "v_mfma_f32_16x16x32_bf16 v[204:207], v[76:79], a[100:103], v[204:207]\n"
    "global_load_lds_dwordx4 v9, s[62:63] offset:3072 sc1\n"
    "v_mfma_f32_16x16x32_bf16 v[204:207], v[72:75], a[96:99], v[204:207]\n"
    "v_exp_f32 v21, v21\n"
    "v_mfma_f32_16x16x32_bf16 v[220:223], v[72:75], a[104:107], v[220:223]\n"
    "v_exp_f32 v22, v22\n"
    "v_mfma_f32_16x16x32_bf16 v[220:223], v[76:79], a[108:111], v[220:223]\n"
    "ds_read_b128 a[72:75], v5 offset:34816\n"
    "v_mfma_f32_16x16x32_bf16 v[216:219], v[68:71], a[108:111], v[216:219]\n"
    "s_add_u32 s62, s62, 0x8000\n"
    "v_exp_f32 v23, v23\n"
    "v_mfma_f32_16x16x32_bf16 v[216:219], v[64:67], a[104:107], v[216:219]\n"
    "v_cvt_pk_bf16_f32 v92, v20, v21\n"
    "v_cvt_pk_bf16_f32 v93, v22, v23\n"
    "v_mfma_f32_16x16x32_bf16 v[212:215], v[56:59], a[104:107], v[212:215]\n"
    "s_add_u32 m0, s69, 32768\n"
    "v_exp_f32 v24, v24\n"
    "v_mfma_f32_16x16x32_bf16 v[212:215], v[60:63], a[108:111], v[212:215]\n"
    "ds_read_b128 a[76:79], v5 offset:35840\n"
    "v_mfma_f32_16x16x32_bf16 v[208:211], v[52:55], a[108:111], v[208:211]\n"
    "v_exp_f32 v25, v25\n"
    "v_mfma_f32_16x16x32_bf16 v[208:211], v[48:51], a[104:107], v[208:211]\n"
    "v_smfmac_f32_16x16x64_bf16 a[136:139], a[144:147], v[64:71], v10\n"
    "s_waitcnt lgkmcnt(4)\n"
    "v_mfma_f32_16x16x32_bf16 v[16:19], a[80:83], a[0:3], 4.0\n"
    "v_exp_f32 v26, v26\n"
    "v_mfma_f32_16x16x32_bf16 v[16:19], a[84:87], a[4:7], v[16:19]\n"
    "ds_read_b128 a[96:99], v7 offset:49152\n"
    "v_mfma_f32_16x16x32_bf16 v[16:19], a[88:91], a[8:11], v[16:19]\n"
    "v_exp_f32 v27, v27\n"
    "v_mfma_f32_16x16x32_bf16 v[16:19], a[92:95], a[12:15], v[16:19]\n"
    "v_cvt_pk_bf16_f32 v100, v24, v25\n"
    "v_cvt_pk_bf16_f32 v101, v26, v27\n"
    "v_mfma_f32_16x16x32_bf16 v[20:23], a[92:95], a[28:31], 4.0\n"
    "v_exp_f32 v28, v28\n"
    "v_mfma_f32_16x16x32_bf16 v[20:23], a[88:91], a[24:27], v[20:23]\n"
    "ds_read_b128 a[100:103], v7 offset:50176\n"
    "v_mfma_f32_16x16x32_bf16 v[20:23], a[84:87], a[20:23], v[20:23]\n"
    "global_load_lds_dwordx4 v8, s[62:63] offset:0 sc1\n"
    "v_mfma_f32_16x16x32_bf16 v[20:23], a[80:83], a[16:19], v[20:23]\n"
    "v_exp_f32 v29, v29\n"
    "v_mfma_f32_16x16x32_bf16 v[24:27], a[80:83], a[32:35], 4.0\n"
    "v_exp_f32 v30, v30\n"
    "v_mfma_f32_16x16x32_bf16 v[24:27], a[84:87], a[36:39], v[24:27]\n"
    "ds_read_b128 a[104:107], v7 offset:51200\n"
    "v_mfma_f32_16x16x32_bf16 v[24:27], a[88:91], a[40:43], v[24:27]\n"
    "v_exp_f32 v31, v31\n"
    "v_mfma_f32_16x16x32_bf16 v[24:27], a[92:95], a[44:47], v[24:27]\n"
    "v_cvt_pk_bf16_f32 v108, v28, v29\n"
    "v_cvt_pk_bf16_f32 v109, v30, v31\n"
    "v_mfma_f32_16x16x32_bf16 v[28:31], a[92:95], a[60:63], 4.0\n"
    "v_exp_f32 v16, v16\n"
    "v_mfma_f32_16x16x32_bf16 v[28:31], a[88:91], a[56:59], v[28:31]\n"
    "ds_read_b128 a[108:111], v7 offset:52224\n"
    "v_mfma_f32_16x16x32_bf16 v[28:31], a[84:87], a[52:55], v[28:31]\n"
    "v_exp_f32 v17, v17\n"
    "v_mfma_f32_16x16x32_bf16 v[28:31], a[80:83], a[48:51], v[28:31]\n"
    "v_mfma_f32_16x16x32_bf16 v[224:227], v[48:51], a[112:115], v[224:227]\n"
    "v_exp_f32 v18, v18\n"
    "v_mfma_f32_16x16x32_bf16 v[224:227], v[52:55], a[116:119], v[224:227]\n"
    "ds_read_b128 a[80:83], v5 offset:36864\n"
    "v_mfma_f32_16x16x32_bf16 v[228:231], v[60:63], a[116:119], v[228:231]\n"
    "v_exp_f32 v19, v19\n"
    "v_mfma_f32_16x16x32_bf16 v[228:231], v[56:59], a[112:115], v[228:231]\n"
    "v_cvt_pk_bf16_f32 v86, v16, v17\n"
    "v_cvt_pk_bf16_f32 v87, v18, v19\n"
    "v_mfma_f32_16x16x32_bf16 v[232:235], v[64:67], a[112:115], v[232:235]\n"
    "v_exp_f32 v20, v20\n"
    "v_mfma_f32_16x16x32_bf16 v[232:235], v[68:71], a[116:119], v[232:235]\n"
    "ds_read_b128 a[84:87], v5 offset:37888\n"
    "v_mfma_f32_16x16x32_bf16 v[236:239], v[76:79], a[116:119], v[236:239]\n"
    "global_load_lds_dwordx4 v8, s[62:63] offset:1024 sc1\n"
    "v_mfma_f32_16x16x32_bf16 v[236:239], v[72:75], a[112:115], v[236:239]\n"
    "v_exp_f32 v21, v21\n"
    "v_mfma_f32_16x16x32_bf16 v[252:255], v[72:75], a[120:123], v[252:255]\n"
    "v_exp_f32 v22, v22\n"
    "v_mfma_f32_16x16x32_bf16 v[252:255], v[76:79], a[124:127], v[252:255]\n"
    "ds_read_b128 a[88:91], v5 offset:38912\n"
    "v_mfma_f32_16x16x32_bf16 v[248:251], v[68:71], a[124:127], v[248:251]\n"
    "v_exp_f32 v23, v23\n"
    "v_mfma_f32_16x16x32_bf16 v[248:251], v[64:67], a[120:123], v[248:251]\n"
    "v_cvt_pk_bf16_f32 v94, v20, v21\n"
    "v_cvt_pk_bf16_f32 v95, v22, v23\n"
    "v_mfma_f32_16x16x32_bf16 v[244:247], v[56:59], a[120:123], v[244:247]\n"
    "v_exp_f32 v24, v24\n"
    "v_mfma_f32_16x16x32_bf16 v[244:247], v[60:63], a[124:127], v[244:247]\n"
    "ds_read_b128 a[92:95], v5 offset:39936\n"
    "v_mfma_f32_16x16x32_bf16 v[240:243], v[52:55], a[124:127], v[240:243]\n"
    "v_exp_f32 v25, v25\n"
    "v_mfma_f32_16x16x32_bf16 v[240:243], v[48:51], a[120:123], v[240:243]\n"
    "v_smfmac_f32_16x16x64_bf16 a[140:143], a[144:147], v[72:79], v10\n"
    "s_waitcnt lgkmcnt(4)\n"
    "global_load_dwordx4 v[112:115], v2, s[50:51]\n"
    "v_mfma_f32_16x16x32_bf16 v[16:19], a[64:67], a[0:3], 4.0\n"
    "v_exp_f32 v26, v26\n"
    "v_mfma_f32_16x16x32_bf16 v[16:19], a[68:71], a[4:7], v[16:19]\n"
    "ds_read_b128 a[112:115], v7 offset:53248\n"
    "v_mfma_f32_16x16x32_bf16 v[16:19], a[72:75], a[8:11], v[16:19]\n"
    "v_exp_f32 v27, v27\n"
    "v_mfma_f32_16x16x32_bf16 v[16:19], a[76:79], a[12:15], v[16:19]\n"
    "v_cvt_pk_bf16_f32 v102, v24, v25\n"
    "v_cvt_pk_bf16_f32 v103, v26, v27\n"
    "v_mfma_f32_16x16x32_bf16 v[20:23], a[76:79], a[28:31], 4.0\n"
    "v_exp_f32 v28, v28\n"
    "v_mfma_f32_16x16x32_bf16 v[20:23], a[72:75], a[24:27], v[20:23]\n"
    "ds_read_b128 a[116:119], v7 offset:54272\n"
    "v_mfma_f32_16x16x32_bf16 v[20:23], a[68:71], a[20:23], v[20:23]\n"
    "global_load_lds_dwordx4 v8, s[62:63] offset:2048 sc1\n"
    "v_mfma_f32_16x16x32_bf16 v[20:23], a[64:67], a[16:19], v[20:23]\n"
    "v_exp_f32 v29, v29\n"
    "v_mfma_f32_16x16x32_bf16 v[24:27], a[64:67], a[32:35], 4.0\n"
    "v_exp_f32 v30, v30\n"
    "v_mfma_f32_16x16x32_bf16 v[24:27], a[68:71], a[36:39], v[24:27]\n"
    "ds_read_b128 a[120:123], v7 offset:55296\n"
    "v_mfma_f32_16x16x32_bf16 v[24:27], a[72:75], a[40:43], v[24:27]\n"
    "v_exp_f32 v31, v31\n"
    "v_mfma_f32_16x16x32_bf16 v[24:27], a[76:79], a[44:47], v[24:27]\n"
    "v_cvt_pk_bf16_f32 v110, v28, v29\n"
    "v_cvt_pk_bf16_f32 v111, v30, v31\n"
    "v_mfma_f32_16x16x32_bf16 v[28:31], a[76:79], a[60:63], 4.0\n"
    "v_exp_f32 v16, v16\n"
    "v_mfma_f32_16x16x32_bf16 v[28:31], a[72:75], a[56:59], v[28:31]\n"
    "ds_read_b128 a[124:127], v7 offset:56320\n"
    "v_mfma_f32_16x16x32_bf16 v[28:31], a[68:71], a[52:55], v[28:31]\n"
    "v_exp_f32 v17, v17\n"
    "v_mfma_f32_16x16x32_bf16 v[28:31], a[64:67], a[48:51], v[28:31]\n"
    "v_mfma_f32_16x16x32_bf16 v[128:131], v[80:83], a[96:99], v[128:131]\n"
    "v_exp_f32 v18, v18\n"
    "v_mfma_f32_16x16x32_bf16 v[128:131], v[84:87], a[100:103], v[128:131]\n"
    "ds_read_b128 a[64:67], v5 offset:40960\n"
    "v_mfma_f32_16x16x32_bf16 v[132:135], v[92:95], a[100:103], v[132:135]\n"
    "v_exp_f32 v19, v19\n"
    "v_mfma_f32_16x16x32_bf16 v[132:135], v[88:91], a[96:99], v[132:135]\n"
    "v_cvt_pk_bf16_f32 v48, v16, v17\n"
    "v_cvt_pk_bf16_f32 v49, v18, v19\n"
    "v_mfma_f32_16x16x32_bf16 v[136:139], v[96:99], a[96:99], v[136:139]\n"
    "v_exp_f32 v20, v20\n"
    "v_mfma_f32_16x16x32_bf16 v[136:139], v[100:103], a[100:103], v[136:139]\n"
    "ds_read_b128 a[68:71], v5 offset:41984\n"
    "v_mfma_f32_16x16x32_bf16 v[140:143], v[108:111], a[100:103], v[140:143]\n"
    "global_load_lds_dwordx4 v8, s[62:63] offset:3072 sc1\n"
    "v_mfma_f32_16x16x32_bf16 v[140:143], v[104:107], a[96:99], v[140:143]\n"
    "v_exp_f32 v21, v21\n"
    "v_mfma_f32_16x16x32_bf16 v[156:159], v[104:107], a[104:107], v[156:159]\n"
    "v_exp_f32 v22, v22\n"
    "v_mfma_f32_16x16x32_bf16 v[156:159], v[108:111], a[108:111], v[156:159]\n"
    "ds_read_b128 a[72:75], v5 offset:43008\n"
    "v_mfma_f32_16x16x32_bf16 v[152:155], v[100:103], a[108:111], v[152:155]\n"
    "v_exp_f32 v23, v23\n"
    "v_mfma_f32_16x16x32_bf16 v[152:155], v[96:99], a[104:107], v[152:155]\n"
    "v_cvt_pk_bf16_f32 v56, v20, v21\n"
    "v_cvt_pk_bf16_f32 v57, v22, v23\n"
    "v_mfma_f32_16x16x32_bf16 v[148:151], v[88:91], a[104:107], v[148:151]\n"
    "s_add_u32 m0, s69, 49152\n"
    "v_exp_f32 v24, v24\n"
    "v_mfma_f32_16x16x32_bf16 v[148:151], v[92:95], a[108:111], v[148:151]\n"
    "ds_read_b128 a[76:79], v5 offset:44032\n"
    "v_mfma_f32_16x16x32_bf16 v[144:147], v[84:87], a[108:111], v[144:147]\n"
    "v_exp_f32 v25, v25\n"
    "v_mfma_f32_16x16x32_bf16 v[144:147], v[80:83], a[104:107], v[144:147]\n"
    "v_smfmac_f32_16x16x64_bf16 a[128:131], a[144:147], v[80:87], v10\n"
    "s_waitcnt lgkmcnt(4)\n"
    "v_mfma_f32_16x16x32_bf16 v[16:19], a[80:83], a[0:3], 4.0\n"
    "s_add_u32 s75, s74, 2\n"
    "v_exp_f32 v26, v26\n"
    "v_mfma_f32_16x16x32_bf16 v[16:19], a[84:87], a[4:7], v[16:19]\n"
    "ds_read_b128 a[96:99], v7 offset:57344\n"
    "v_mfma_f32_16x16x32_bf16 v[16:19], a[88:91], a[8:11], v[16:19]\n"
    "s_and_b32 s75, s75, 3\n"
    "v_exp_f32 v27, v27\n"
    "v_mfma_f32_16x16x32_bf16 v[16:19], a[92:95], a[12:15], v[16:19]\n"
    "v_cvt_pk_bf16_f32 v64, v24, v25\n"
    "v_cvt_pk_bf16_f32 v65, v26, v27\n"
    "v_mfma_f32_16x16x32_bf16 v[20:23], a[92:95], a[28:31], 4.0\n"
    "s_lshl_b32 s75, s75, 12\n"
    "v_exp_f32 v28, v28\n"
    "v_mfma_f32_16x16x32_bf16 v[20:23], a[88:91], a[24:27], v[20:23]\n"
    "ds_read_b128 a[100:103], v7 offset:58368\n"
    "v_mfma_f32_16x16x32_bf16 v[20:23], a[84:87], a[20:23], v[20:23]\n"
    "global_load_lds_dwordx4 v9, s[62:63] offset:0 sc1\n"
    "v_mfma_f32_16x16x32_bf16 v[20:23], a[80:83], a[16:19], v[20:23]\n"
    "v_exp_f32 v29, v29\n"
    "v_mfma_f32_16x16x32_bf16 v[24:27], a[80:83], a[32:35], 4.0\n"
    "s_add_u32 s46, s54, s75\n"
    "v_exp_f32 v30, v30\n"
    "v_mfma_f32_16x16x32_bf16 v[24:27], a[84:87], a[36:39], v[24:27]\n"
    "ds_read_b128 a[104:107], v7 offset:59392\n"
    "v_mfma_f32_16x16x32_bf16 v[24:27], a[88:91], a[40:43], v[24:27]\n"
    "s_addc_u32 s47, s55, 0\n"
    "v_exp_f32 v31, v31\n"
    "v_mfma_f32_16x16x32_bf16 v[24:27], a[92:95], a[44:47], v[24:27]\n"
    "v_cvt_pk_bf16_f32 v72, v28, v29\n"
    "v_cvt_pk_bf16_f32 v73, v30, v31\n"
    "v_mfma_f32_16x16x32_bf16 v[28:31], a[92:95], a[60:63], 4.0\n"
    "v_exp_f32 v16, v16\n"
    "v_mfma_f32_16x16x32_bf16 v[28:31], a[88:91], a[56:59], v[28:31]\n"
    "ds_read_b128 a[108:111], v7 offset:60416\n"
    "v_mfma_f32_16x16x32_bf16 v[28:31], a[84:87], a[52:55], v[28:31]\n"
    "v_exp_f32 v17, v17\n"
    "v_mfma_f32_16x16x32_bf16 v[28:31], a[80:83], a[48:51], v[28:31]\n"
    "v_mfma_f32_16x16x32_bf16 v[160:163], v[80:83], a[112:115], v[160:163]\n"
    "v_exp_f32 v18, v18\n"
    "v_mfma_f32_16x16x32_bf16 v[160:163], v[84:87], a[116:119], v[160:163]\n"
    "ds_read_b128 a[80:83], v5 offset:45056\n"
    "v_mfma_f32_16x16x32_bf16 v[164:167], v[92:95], a[116:119], v[164:167]\n"
    "v_exp_f32 v19, v19\n"
    "v_mfma_f32_16x16x32_bf16 v[164:167], v[88:91], a[112:115], v[164:167]\n"
    "v_cvt_pk_bf16_f32 v50, v16, v17\n"
    "v_cvt_pk_bf16_f32 v51, v18, v19\n"
    "v_mfma_f32_16x16x32_bf16 v[168:171], v[96:99], a[112:115], v[168:171]\n"
    "v_exp_f32 v20, v20\n"
    "v_mfma_f32_16x16x32_bf16 v[168:171], v[100:103], a[116:119], v[168:171]\n"
    "ds_read_b128 a[84:87], v5 offset:46080\n"
    "v_mfma_f32_16x16x32_bf16 v[172:175], v[108:111], a[116:119], v[172:175]\n"
    "global_load_lds_dwordx4 v9, s[62:63] offset:1024 sc1\n"
    "v_mfma_f32_16x16x32_bf16 v[172:175], v[104:107], a[112:115], v[172:175]\n"
    "v_exp_f32 v21, v21\n"
    "v_mfma_f32_16x16x32_bf16 v[188:191], v[104:107], a[120:123], v[188:191]\n"
    "v_exp_f32 v22, v22\n"
    "v_mfma_f32_16x16x32_bf16 v[188:191], v[108:111], a[124:127], v[188:191]\n"
    "ds_read_b128 a[88:91], v5 offset:47104\n"
    "v_mfma_f32_16x16x32_bf16 v[184:187], v[100:103], a[124:127], v[184:187]\n"
    "v_exp_f32 v23, v23\n"
    "v_mfma_f32_16x16x32_bf16 v[184:187], v[96:99], a[120:123], v[184:187]\n"
    "v_cvt_pk_bf16_f32 v58, v20, v21\n"
    "v_cvt_pk_bf16_f32 v59, v22, v23\n"
    "v_mfma_f32_16x16x32_bf16 v[180:183], v[88:91], a[120:123], v[180:183]\n"
    "v_exp_f32 v24, v24\n"
    "v_mfma_f32_16x16x32_bf16 v[180:183], v[92:95], a[124:127], v[180:183]\n"
    "ds_read_b128 a[92:95], v5 offset:48128\n"
    "v_mfma_f32_16x16x32_bf16 v[176:179], v[84:87], a[124:127], v[176:179]\n"
    "v_exp_f32 v25, v25\n"
    "v_mfma_f32_16x16x32_bf16 v[176:179], v[80:83], a[120:123], v[176:179]\n"
    "v_smfmac_f32_16x16x64_bf16 a[132:135], a[144:147], v[88:95], v10\n"
    "s_waitcnt lgkmcnt(4)\n"
    "v_mfma_f32_16x16x32_bf16 v[16:19], a[64:67], a[0:3], 4.0\n"
    "v_exp_f32 v26, v26\n"
    "v_mfma_f32_16x16x32_bf16 v[16:19], a[68:71], a[4:7], v[16:19]\n"
    "ds_read_b128 a[112:115], v7 offset:61440\n"
    "v_mfma_f32_16x16x32_bf16 v[16:19], a[72:75], a[8:11], v[16:19]\n"
    "v_exp_f32 v27, v27\n"
    "v_mfma_f32_16x16x32_bf16 v[16:19], a[76:79], a[12:15], v[16:19]\n"
    "v_cvt_pk_bf16_f32 v66, v24, v25\n"
    "v_cvt_pk_bf16_f32 v67, v26, v27\n"
    "v_mfma_f32_16x16x32_bf16 v[20:23], a[76:79], a[28:31], 4.0\n"
    "v_exp_f32 v28, v28\n"
    "v_mfma_f32_16x16x32_bf16 v[20:23], a[72:75], a[24:27], v[20:23]\n"
    "ds_read_b128 a[116:119], v7 offset:62464\n"
    "v_mfma_f32_16x16x32_bf16 v[20:23], a[68:71], a[20:23], v[20:23]\n"
    "global_load_lds_dwordx4 v9, s[62:63] offset:2048 sc1\n"
    "v_mfma_f32_16x16x32_bf16 v[20:23], a[64:67], a[16:19], v[20:23]\n"
    "v_exp_f32 v29, v29\n"
    "v_mfma_f32_16x16x32_bf16 v[24:27], a[64:67], a[32:35], 4.0\n"
    "v_exp_f32 v30, v30\n"
    "v_mfma_f32_16x16x32_bf16 v[24:27], a[68:71], a[36:39], v[24:27]\n"
    "ds_read_b128 a[120:123], v7 offset:63488\n"
    "v_mfma_f32_16x16x32_bf16 v[24:27], a[72:75], a[40:43], v[24:27]\n"
    "v_exp_f32 v31, v31\n"
    "v_mfma_f32_16x16x32_bf16 v[24:27], a[76:79], a[44:47], v[24:27]\n"
    "v_cvt_pk_bf16_f32 v74, v28, v29\n"
    "v_cvt_pk_bf16_f32 v75, v30, v31\n"
    "v_mfma_f32_16x16x32_bf16 v[28:31], a[76:79], a[60:63], 4.0\n"
    "v_exp_f32 v16, v16\n"
    "v_mfma_f32_16x16x32_bf16 v[28:31], a[72:75], a[56:59], v[28:31]\n"
    "ds_read_b128 a[124:127], v7 offset:64512\n"
    "v_mfma_f32_16x16x32_bf16 v[28:31], a[68:71], a[52:55], v[28:31]\n"
    "s_waitcnt vmcnt(8)\n"
    "v_exp_f32 v17, v17\n"
    "v_mfma_f32_16x16x32_bf16 v[28:31], a[64:67], a[48:51], v[28:31]\n"
    "s_barrier\n"
    "v_mfma_f32_16x16x32_bf16 v[192:195], v[80:83], a[96:99], v[192:195]\n"
    "v_exp_f32 v18, v18\n"
    "v_mfma_f32_16x16x32_bf16 v[192:195], v[84:87], a[100:103], v[192:195]\n"
    "ds_read_b128 a[64:67], v4 offset:0\n"
    "v_mfma_f32_16x16x32_bf16 v[196:199], v[92:95], a[100:103], v[196:199]\n"
    "v_exp_f32 v19, v19\n"
    "v_mfma_f32_16x16x32_bf16 v[196:199], v[88:91], a[96:99], v[196:199]\n"
    "v_cvt_pk_bf16_f32 v52, v16, v17\n"
    "v_cvt_pk_bf16_f32 v53, v18, v19\n"
    "v_mfma_f32_16x16x32_bf16 v[200:203], v[96:99], a[96:99], v[200:203]\n"
    "v_exp_f32 v20, v20\n"
    "v_mfma_f32_16x16x32_bf16 v[200:203], v[100:103], a[100:103], v[200:203]\n"
    "ds_read_b128 a[68:71], v4 offset:1024\n"
    "v_mfma_f32_16x16x32_bf16 v[204:207], v[108:111], a[100:103], v[204:207]\n"
    "global_load_lds_dwordx4 v9, s[62:63] offset:3072 sc1\n"
    "v_mfma_f32_16x16x32_bf16 v[204:207], v[104:107], a[96:99], v[204:207]\n"
    "v_exp_f32 v21, v21\n"
    "v_mfma_f32_16x16x32_bf16 v[220:223], v[104:107], a[104:107], v[220:223]\n"
    "v_exp_f32 v22, v22\n"
    "v_mfma_f32_16x16x32_bf16 v[220:223], v[108:111], a[108:111], v[220:223]\n"
    "ds_read_b128 a[72:75], v4 offset:2048\n"
    "v_mfma_f32_16x16x32_bf16 v[216:219], v[100:103], a[108:111], v[216:219]\n"
    "s_add_u32 s62, s62, 0x8000\n"
    "v_exp_f32 v23, v23\n"
    "v_mfma_f32_16x16x32_bf16 v[216:219], v[96:99], a[104:107], v[216:219]\n"
    "v_cvt_pk_bf16_f32 v60, v20, v21\n"
    "v_cvt_pk_bf16_f32 v61, v22, v23\n"
    "v_mfma_f32_16x16x32_bf16 v[212:215], v[88:91], a[104:107], v[212:215]\n"
    "s_add_u32 m0, s69, 65536\n"
    "v_exp_f32 v24, v24\n"
    "v_mfma_f32_16x16x32_bf16 v[212:215], v[92:95], a[108:111], v[212:215]\n"
    "ds_read_b128 a[76:79], v4 offset:3072\n"
    "v_mfma_f32_16x16x32_bf16 v[208:211], v[84:87], a[108:111], v[208:211]\n"
    "v_exp_f32 v25, v25\n"
    "v_mfma_f32_16x16x32_bf16 v[208:211], v[80:83], a[104:107], v[208:211]\n"
    "v_smfmac_f32_16x16x64_bf16 a[136:139], a[144:147], v[96:103], v10\n"
    "s_waitcnt lgkmcnt(4)\n"
    "v_mfma_f32_16x16x32_bf16 v[16:19], a[80:83], a[0:3], 4.0\n"
    "v_exp_f32 v26, v26\n"
    "v_mfma_f32_16x16x32_bf16 v[16:19], a[84:87], a[4:7], v[16:19]\n"
    "ds_read_b128 a[96:99], v6 offset:16384\n"
    "v_mfma_f32_16x16x32_bf16 v[16:19], a[88:91], a[8:11], v[16:19]\n"
    "v_exp_f32 v27, v27\n"
    "v_mfma_f32_16x16x32_bf16 v[16:19], a[92:95], a[12:15], v[16:19]\n"
    "v_cvt_pk_bf16_f32 v68, v24, v25\n"
    "v_cvt_pk_bf16_f32 v69, v26, v27\n"
    "v_mfma_f32_16x16x32_bf16 v[20:23], a[92:95], a[28:31], 4.0\n"
    "v_exp_f32 v28, v28\n"
    "v_mfma_f32_16x16x32_bf16 v[20:23], a[88:91], a[24:27], v[20:23]\n"
    "ds_read_b128 a[100:103], v6 offset:17408\n"
    "v_mfma_f32_16x16x32_bf16 v[20:23], a[84:87], a[20:23], v[20:23]\n"
    "global_load_lds_dwordx4 v8, s[62:63] offset:0 sc1\n"
    "v_mfma_f32_16x16x32_bf16 v[20:23], a[80:83], a[16:19], v[20:23]\n"
    "v_exp_f32 v29, v29\n"
    "v_mfma_f32_16x16x32_bf16 v[24:27], a[80:83], a[32:35], 4.0\n"
    "v_exp_f32 v30, v30\n"
    "v_mfma_f32_16x16x32_bf16 v[24:27], a[84:87], a[36:39], v[24:27]\n"
    "ds_read_b128 a[104:107], v6 offset:18432\n"
    "v_mfma_f32_16x16x32_bf16 v[24:27], a[88:91], a[40:43], v[24:27]\n"
    "v_exp_f32 v31, v31\n"
    "v_mfma_f32_16x16x32_bf16 v[24:27], a[92:95], a[44:47], v[24:27]\n"
    "v_cvt_pk_bf16_f32 v76, v28, v29\n"
    "v_cvt_pk_bf16_f32 v77, v30, v31\n"
    "v_mfma_f32_16x16x32_bf16 v[28:31], a[92:95], a[60:63], 4.0\n"
    "v_exp_f32 v16, v16\n"
    "v_mfma_f32_16x16x32_bf16 v[28:31], a[88:91], a[56:59], v[28:31]\n"
    "ds_read_b128 a[108:111], v6 offset:19456\n"
    "v_mfma_f32_16x16x32_bf16 v[28:31], a[84:87], a[52:55], v[28:31]\n"
    "v_exp_f32 v17, v17\n"
    "v_mfma_f32_16x16x32_bf16 v[28:31], a[80:83], a[48:51], v[28:31]\n"
    "v_mfma_f32_16x16x32_bf16 v[224:227], v[80:83], a[112:115], v[224:227]\n"
    "v_exp_f32 v18, v18\n"
    "v_mfma_f32_16x16x32_bf16 v[224:227], v[84:87], a[116:119], v[224:227]\n"
    "ds_read_b128 a[80:83], v4 offset:4096\n"
    "v_mfma_f32_16x16x32_bf16 v[228:231], v[92:95], a[116:119], v[228:231]\n"
    "v_exp_f32 v19, v19\n"
    "v_mfma_f32_16x16x32_bf16 v[228:231], v[88:91], a[112:115], v[228:231]\n"
    "v_cvt_pk_bf16_f32 v54, v16, v17\n"
    "v_cvt_pk_bf16_f32 v55, v18, v19\n"
    "v_mfma_f32_16x16x32_bf16 v[232:235], v[96:99], a[112:115], v[232:235]\n"
    "v_exp_f32 v20, v20\n"
    "v_mfma_f32_16x16x32_bf16 v[232:235], v[100:103], a[116:119], v[232:235]\n"
    "ds_read_b128 a[84:87], v4 offset:5120\n"
    "v_mfma_f32_16x16x32_bf16 v[236:239], v[108:111], a[116:119], v[236:239]\n"
    "global_load_lds_dwordx4 v8, s[62:63] offset:1024 sc1\n"
    "v_mfma_f32_16x16x32_bf16 v[236:239], v[104:107], a[112:115], v[236:239]\n"
    "v_exp_f32 v21, v21\n"
    "v_mfma_f32_16x16x32_bf16 v[252:255], v[104:107], a[120:123], v[252:255]\n"
    "v_exp_f32 v22, v22\n"
    "v_mfma_f32_16x16x32_bf16 v[252:255], v[108:111], a[124:127], v[252:255]\n"
    "ds_read_b128 a[88:91], v4 offset:6144\n"
    "v_mfma_f32_16x16x32_bf16 v[248:251], v[100:103], a[124:127], v[248:251]\n"
    "v_exp_f32 v23, v23\n"
    "v_mfma_f32_16x16x32_bf16 v[248:251], v[96:99], a[120:123], v[248:251]\n"
    "v_cvt_pk_bf16_f32 v62, v20, v21\n"
    "v_cvt_pk_bf16_f32 v63, v22, v23\n"
    "v_mfma_f32_16x16x32_bf16 v[244:247], v[88:91], a[120:123], v[244:247]\n"
    "v_exp_f32 v24, v24\n"
    "v_mfma_f32_16x16x32_bf16 v[244:247], v[92:95], a[124:127], v[244:247]\n"
    "ds_read_b128 a[92:95], v4 offset:7168\n"
    "v_mfma_f32_16x16x32_bf16 v[240:243], v[84:87], a[124:127], v[240:243]\n"
    "v_exp_f32 v25, v25\n"
    "v_mfma_f32_16x16x32_bf16 v[240:243], v[80:83], a[120:123], v[240:243]\n"
    "v_smfmac_f32_16x16x64_bf16 a[140:143], a[144:147], v[104:111], v10\n"
    "s_waitcnt lgkmcnt(4)\n"
    "global_load_dwordx4 v[116:119], v2, s[52:53]\n"
    "v_mfma_f32_16x16x32_bf16 v[16:19], a[64:67], a[0:3], 4.0\n"
    "v_exp_f32 v26, v26\n"
    "v_mfma_f32_16x16x32_bf16 v[16:19], a[68:71], a[4:7], v[16:19]\n"
    "ds_read_b128 a[112:115], v6 offset:20480\n"
    "v_mfma_f32_16x16x32_bf16 v[16:19], a[72:75], a[8:11], v[16:19]\n"
    "v_exp_f32 v27, v27\n"
    "v_mfma_f32_16x16x32_bf16 v[16:19], a[76:79], a[12:15], v[16:19]\n"
    "v_cvt_pk_bf16_f32 v70, v24, v25\n"
    "v_cvt_pk_bf16_f32 v71, v26, v27\n"
    "v_mfma_f32_16x16x32_bf16 v[20:23], a[76:79], a[28:31], 4.0\n"
    "v_exp_f32 v28, v28\n"
    "v_mfma_f32_16x16x32_bf16 v[20:23], a[72:75], a[24:27], v[20:23]\n"
    "ds_read_b128 a[116:119], v6 offset:21504\n"
    "v_mfma_f32_16x16x32_bf16 v[20:23], a[68:71], a[20:23], v[20:23]\n"
    "global_load_lds_dwordx4 v8, s[62:63] offset:2048 sc1\n"
    "v_mfma_f32_16x16x32_bf16 v[20:23], a[64:67], a[16:19], v[20:23]\n"
    "v_exp_f32 v29, v29\n"
    "v_mfma_f32_16x16x32_bf16 v[24:27], a[64:67], a[32:35], 4.0\n"
    "v_exp_f32 v30, v30\n"
    "v_mfma_f32_16x16x32_bf16 v[24:27], a[68:71], a[36:39], v[24:27]\n"
    "ds_read_b128 a[120:123], v6 offset:22528\n"
    "v_mfma_f32_16x16x32_bf16 v[24:27], a[72:75], a[40:43], v[24:27]\n"
    "v_exp_f32 v31, v31\n"
    "v_mfma_f32_16x16x32_bf16 v[24:27], a[76:79], a[44:47], v[24:27]\n"
    "v_cvt_pk_bf16_f32 v78, v28, v29\n"
    "v_cvt_pk_bf16_f32 v79, v30, v31\n"
    "v_mfma_f32_16x16x32_bf16 v[28:31], a[76:79], a[60:63], 4.0\n"
    "v_exp_f32 v16, v16\n"
    "v_mfma_f32_16x16x32_bf16 v[28:31], a[72:75], a[56:59], v[28:31]\n"
    "ds_read_b128 a[124:127], v6 offset:23552\n"
    "v_mfma_f32_16x16x32_bf16 v[28:31], a[68:71], a[52:55], v[28:31]\n"
    "v_exp_f32 v17, v17\n"
    "v_mfma_f32_16x16x32_bf16 v[28:31], a[64:67], a[48:51], v[28:31]\n"
    "v_mfma_f32_16x16x32_bf16 v[128:131], v[48:51], a[96:99], v[128:131]\n"
    "v_exp_f32 v18, v18\n"
    "v_mfma_f32_16x16x32_bf16 v[128:131], v[52:55], a[100:103], v[128:131]\n"
    "ds_read_b128 a[64:67], v4 offset:8192\n"
    "v_mfma_f32_16x16x32_bf16 v[132:135], v[60:63], a[100:103], v[132:135]\n"
    "v_exp_f32 v19, v19\n"
    "v_mfma_f32_16x16x32_bf16 v[132:135], v[56:59], a[96:99], v[132:135]\n"
    "v_cvt_pk_bf16_f32 v80, v16, v17\n"
    "v_cvt_pk_bf16_f32 v81, v18, v19\n"
    "v_mfma_f32_16x16x32_bf16 v[136:139], v[64:67], a[96:99], v[136:139]\n"
    "v_exp_f32 v20, v20\n"
    "v_mfma_f32_16x16x32_bf16 v[136:139], v[68:71], a[100:103], v[136:139]\n"
    "ds_read_b128 a[68:71], v4 offset:9216\n"
    "v_mfma_f32_16x16x32_bf16 v[140:143], v[76:79], a[100:103], v[140:143]\n"
    "global_load_lds_dwordx4 v8, s[62:63] offset:3072 sc1\n"
    "v_mfma_f32_16x16x32_bf16 v[140:143], v[72:75], a[96:99], v[140:143]\n"
    "v_exp_f32 v21, v21\n"
    "v_mfma_f32_16x16x32_bf16 v[156:159], v[72:75], a[104:107], v[156:159]\n"
    "v_exp_f32 v22, v22\n"
    "v_mfma_f32_16x16x32_bf16 v[156:159], v[76:79], a[108:111], v[156:159]\n"
    "ds_read_b128 a[72:75], v4 offset:10240\n"
    "v_mfma_f32_16x16x32_bf16 v[152:155], v[68:71], a[108:111], v[152:155]\n"
    "v_exp_f32 v23, v23\n"
    "v_mfma_f32_16x16x32_bf16 v[152:155], v[64:67], a[104:107], v[152:155]\n"
    "v_cvt_pk_bf16_f32 v88, v20, v21\n"
    "v_cvt_pk_bf16_f32 v89, v22, v23\n"
    "v_mfma_f32_16x16x32_bf16 v[148:151], v[56:59], a[104:107], v[148:151]\n"
    "s_add_u32 m0, s69, 81920\n"
    "v_exp_f32 v24, v24\n"
    "v_mfma_f32_16x16x32_bf16 v[148:151], v[60:63], a[108:111], v[148:151]\n"
    "ds_read_b128 a[76:79], v4 offset:11264\n"
    "v_mfma_f32_16x16x32_bf16 v[144:147], v[52:55], a[108:111], v[144:147]\n"
    "v_exp_f32 v25, v25\n"
    "v_mfma_f32_16x16x32_bf16 v[144:147], v[48:51], a[104:107], v[144:147]\n"
    "v_smfmac_f32_16x16x64_bf16 a[128:131], a[144:147], v[48:55], v10\n"
    "s_waitcnt lgkmcnt(4)\n"
    "v_mfma_f32_16x16x32_bf16 v[16:19], a[80:83], a[0:3], 4.0\n"
    "s_add_u32 s75, s74, 3\n"
    "v_exp_f32 v26, v26\n"
    "v_mfma_f32_16x16x32_bf16 v[16:19], a[84:87], a[4:7], v[16:19]\n"
    "ds_read_b128 a[96:99], v6 offset:24576\n"
    "v_mfma_f32_16x16x32_bf16 v[16:19], a[88:91], a[8:11], v[16:19]\n"
    "s_and_b32 s75, s75, 3\n"
    "v_exp_f32 v27, v27\n"
    "v_mfma_f32_16x16x32_bf16 v[16:19], a[92:95], a[12:15], v[16:19]\n"
    "v_cvt_pk_bf16_f32 v96, v24, v25\n"
    "v_cvt_pk_bf16_f32 v97, v26, v27\n"
    "v_mfma_f32_16x16x32_bf16 v[20:23], a[92:95], a[28:31], 4.0\n"
    "s_lshl_b32 s75, s75, 12\n"
    "v_exp_f32 v28, v28\n"
    "v_mfma_f32_16x16x32_bf16 v[20:23], a[88:91], a[24:27], v[20:23]\n"
    "L%=_w10:\n"
    "s_cbranch_execz L%=_wret\n"
    "ds_read_b128 a[100:103], v6 offset:25600\n"
    "v_mfma_f32_16x16x32_bf16 v[20:23], a[84:87], a[20:23], v[20:23]\n"
    "global_load_lds_dwordx4 v9, s[62:63] offset:0 sc1\n"
    "v_mfma_f32_16x16x32_bf16 v[20:23], a[80:83], a[16:19], v[20:23]\n"
    "v_exp_f32 v29, v29\n"
    "v_mfma_f32_16x16x32_bf16 v[24:27], a[80:83], a[32:35], 4.0\n"
    "s_add_u32 s48, s54, s75\n"
    "v_exp_f32 v30, v30\n"
    "v_mfma_f32_16x16x32_bf16 v[24:27], a[84:87], a[36:39], v[24:27]\n"
    "ds_read_b128 a[104:107], v6 offset:26624\n"
    "v_mfma_f32_16x16x32_bf16 v[24:27], a[88:91], a[40:43], v[24:27]\n"
    "s_addc_u32 s49, s55, 0\n"
    "v_exp_f32 v31, v31\n"
    "v_mfma_f32_16x16x32_bf16 v[24:27], a[92:95], a[44:47], v[24:27]\n"
    "v_cvt_pk_bf16_f32 v104, v28, v29\n"
    "v_cvt_pk_bf16_f32 v105, v30, v31\n"
    "v_mfma_f32_16x16x32_bf16 v[28:31], a[92:95], a[60:63], 4.0\n"
    "v_exp_f32 v16, v16\n"
    "v_mfma_f32_16x16x32_bf16 v[28:31], a[88:91], a[56:59], v[28:31]\n"
    "ds_read_b128 a[108:111], v6 offset:27648\n"
    "v_mfma_f32_16x16x32_bf16 v[28:31], a[84:87], a[52:55], v[28:31]\n"
    "v_exp_f32 v17, v17\n"
    "v_mfma_f32_16x16x32_bf16 v[28:31], a[80:83], a[48:51], v[28:31]\n"
    "v_mfma_f32_16x16x32_bf16 v[160:163], v[48:51], a[112:115], v[160:163]\n"
    "v_exp_f32 v18, v18\n"
    "v_mfma_f32_16x16x32_bf16 v[160:163], v[52:55], a[116:119], v[160:163]\n"
    "ds_read_b128 a[80:83], v4 offset:12288\n"
    "v_mfma_f32_16x16x32_bf16 v[164:167], v[60:63], a[116:119], v[164:167]\n"
    "v_exp_f32 v19, v19\n"
    "v_mfma_f32_16x16x32_bf16 v[164:167], v[56:59], a[112:115], v[164:167]\n"
    "v_cvt_pk_bf16_f32 v82, v16, v17\n"
    "v_cvt_pk_bf16_f32 v83, v18, v19\n"
    "v_mfma_f32_16x16x32_bf16 v[168:171], v[64:67], a[112:115], v[168:171]\n"
    "v_exp_f32 v20, v20\n"
    "v_mfma_f32_16x16x32_bf16 v[168:171], v[68:71], a[116:119], v[168:171]\n"
    "ds_read_b128 a[84:87], v4 offset:13312\n"
    "v_mfma_f32_16x16x32_bf16 v[172:175], v[76:79], a[116:119], v[172:175]\n"
    "global_load_lds_dwordx4 v9, s[62:63] offset:1024 sc1\n"
    "v_mfma_f32_16x16x32_bf16 v[172:175], v[72:75], a[112:115], v[172:175]\n"
    "v_exp_f32 v21, v21\n"
    "v_mfma_f32_16x16x32_bf16 v[188:191], v[72:75], a[120:123], v[188:191]\n"
    "v_exp_f32 v22, v22\n"
    "v_mfma_f32_16x16x32_bf16 v[188:191], v[76:79], a[124:127], v[188:191]\n"
    "ds_read_b128 a[88:91], v4 offset:14336\n"
    "v_mfma_f32_16x16x32_bf16 v[184:187], v[68:71], a[124:127], v[184:187]\n"
    "v_exp_f32 v23, v23\n"
    "v_mfma_f32_16x16x32_bf16 v[184:187], v[64:67], a[120:123], v[184:187]\n"
    "v_cvt_pk_bf16_f32 v90, v20, v21\n"
    "v_cvt_pk_bf16_f32 v91, v22, v23\n"
    "v_mfma_f32_16x16x32_bf16 v[180:183], v[56:59], a[120:123], v[180:183]\n"
    "v_exp_f32 v24, v24\n"
    "v_mfma_f32_16x16x32_bf16 v[180:183], v[60:63], a[124:127], v[180:183]\n"
    "ds_read_b128 a[92:95], v4 offset:15360\n"
    "v_mfma_f32_16x16x32_bf16 v[176:179], v[52:55], a[124:127], v[176:179]\n"
    "v_exp_f32 v25, v25\n"
    "v_mfma_f32_16x16x32_bf16 v[176:179], v[48:51], a[120:123], v[176:179]\n"
    "v_smfmac_f32_16x16x64_bf16 a[132:135], a[144:147], v[56:63], v10\n"
    "s_waitcnt lgkmcnt(4)\n"
    "v_mfma_f32_16x16x32_bf16 v[16:19], a[64:67], a[0:3], 4.0\n"
    "v_exp_f32 v26, v26\n"
    "v_mfma_f32_16x16x32_bf16 v[16:19], a[68:71], a[4:7], v[16:19]\n"
    "ds_read_b128 a[112:115], v6 offset:28672\n"
    "v_mfma_f32_16x16x32_bf16 v[16:19], a[72:75], a[8:11], v[16:19]\n"
    "v_exp_f32 v27, v27\n"
    "v_mfma_f32_16x16x32_bf16 v[16:19], a[76:79], a[12:15], v[16:19]\n"
    "v_cvt_pk_bf16_f32 v98, v24, v25\n"
    "v_cvt_pk_bf16_f32 v99, v26, v27\n"
    "v_mfma_f32_16x16x32_bf16 v[20:23], a[76:79], a[28:31], 4.0\n"
    "v_exp_f32 v28, v28\n"
    "v_mfma_f32_16x16x32_bf16 v[20:23], a[72:75], a[24:27], v[20:23]\n"
    "ds_read_b128 a[116:119], v6 offset:29696\n"
    "v_mfma_f32_16x16x32_bf16 v[20:23], a[68:71], a[20:23], v[20:23]\n"
    "global_load_lds_dwordx4 v9, s[62:63] offset:2048 sc1\n"
    "v_mfma_f32_16x16x32_bf16 v[20:23], a[64:67], a[16:19], v[20:23]\n"
    "v_exp_f32 v29, v29\n"
    "v_mfma_f32_16x16x32_bf16 v[24:27], a[64:67], a[32:35], 4.0\n"
    "v_exp_f32 v30, v30\n"
    "v_mfma_f32_16x16x32_bf16 v[24:27], a[68:71], a[36:39], v[24:27]\n"
    "ds_read_b128 a[120:123], v6 offset:30720\n"
    "v_mfma_f32_16x16x32_bf16 v[24:27], a[72:75], a[40:43], v[24:27]\n"
    "v_exp_f32 v31, v31\n"
    "v_mfma_f32_16x16x32_bf16 v[24:27], a[76:79], a[44:47], v[24:27]\n"
    "v_cvt_pk_bf16_f32 v106, v28, v29\n"
    "v_cvt_pk_bf16_f32 v107, v30, v31\n"
    "v_mfma_f32_16x16x32_bf16 v[28:31], a[76:79], a[60:63], 4.0\n"
    "v_exp_f32 v16, v16\n"
    "v_mfma_f32_16x16x32_bf16 v[28:31], a[72:75], a[56:59], v[28:31]\n"
    "ds_read_b128 a[124:127], v6 offset:31744\n"
    "v_mfma_f32_16x16x32_bf16 v[28:31], a[68:71], a[52:55], v[28:31]\n"
    "s_waitcnt vmcnt(8)\n"
    "v_exp_f32 v17, v17\n"
    "v_mfma_f32_16x16x32_bf16 v[28:31], a[64:67], a[48:51], v[28:31]\n"
    "s_barrier\n"
    "v_mfma_f32_16x16x32_bf16 v[192:195], v[48:51], a[96:99], v[192:195]\n"
    "v_exp_f32 v18, v18\n"
    "v_mfma_f32_16x16x32_bf16 v[192:195], v[52:55], a[100:103], v[192:195]\n"
    "ds_read_b128 a[64:67], v4 offset:32768\n"
    "v_mfma_f32_16x16x32_bf16 v[196:199], v[60:63], a[100:103], v[196:199]\n"
    "v_exp_f32 v19, v19\n"
    "v_mfma_f32_16x16x32_bf16 v[196:199], v[56:59], a[96:99], v[196:199]\n"
    "v_cvt_pk_bf16_f32 v84, v16, v17\n"
    "v_cvt_pk_bf16_f32 v85, v18, v19\n"
    "v_mfma_f32_16x16x32_bf16 v[200:203], v[64:67], a[96:99], v[200:203]\n"
    "v_exp_f32 v20, v20\n"
    "v_mfma_f32_16x16x32_bf16 v[200:203], v[68:71], a[100:103], v[200:203]\n"
    "ds_read_b128 a[68:71], v4 offset:33792\n"
    "v_mfma_f32_16x16x32_bf16 v[204:207], v[76:79], a[100:103], v[204:207]\n"
    "global_load_lds_dwordx4 v9, s[62:63] offset:3072 sc1\n"
    "v_mfma_f32_16x16x32_bf16 v[204:207], v[72:75], a[96:99], v[204:207]\n"
    "v_exp_f32 v21, v21\n"
    "v_mfma_f32_16x16x32_bf16 v[220:223], v[72:75], a[104:107], v[220:223]\n"
    "v_exp_f32 v22, v22\n"
    "v_mfma_f32_16x16x32_bf16 v[220:223], v[76:79], a[108:111], v[220:223]\n"
    "ds_read_b128 a[72:75], v4 offset:34816\n"
    "v_mfma_f32_16x16x32_bf16 v[216:219], v[68:71], a[108:111], v[216:219]\n"
    "s_add_u32 s62, s62, 0x8000\n"
    "v_exp_f32 v23, v23\n"
    "v_mfma_f32_16x16x32_bf16 v[216:219], v[64:67], a[104:107], v[216:219]\n"
    "v_cvt_pk_bf16_f32 v92, v20, v21\n"
    "v_cvt_pk_bf16_f32 v93, v22, v23\n"
    "v_mfma_f32_16x16x32_bf16 v[212:215], v[56:59], a[104:107], v[212:215]\n"
    "s_add_u32 m0, s69, 114688\n"
    "v_exp_f32 v24, v24\n"
    "v_mfma_f32_16x16x32_bf16 v[212:215], v[60:63], a[108:111], v[212:215]\n"
    "ds_read_b128 a[76:79], v4 offset:35840\n"
    "v_mfma_f32_16x16x32_bf16 v[208:211], v[52:55], a[108:111], v[208:211]\n"
    "v_exp_f32 v25, v25\n"
    "v_mfma_f32_16x16x32_bf16 v[208:211], v[48:51], a[104:107], v[208:211]\n"
    "global_load_lds_dwordx4 v9, s[62:63] offset:0 sc1\n"
    "v_smfmac_f32_16x16x64_bf16 a[136:139], a[144:147], v[64:71], v10\n"
    "s_waitcnt lgkmcnt(4)\n"
    "v_mfma_f32_16x16x32_bf16 v[16:19], a[80:83], a[0:3], 4.0\n"
    "s_lshl_b32 s61, %3, 2\n"
    "v_exp_f32 v26, v26\n"
    "v_mfma_f32_16x16x32_bf16 v[16:19], a[84:87], a[4:7], v[16:19]\n"
    "ds_read_b128 a[96:99], v6 offset:49152\n"
    "v_mfma_f32_16x16x32_bf16 v[16:19], a[88:91], a[8:11], v[16:19]\n"
    "s_add_u32 s61, s61, 131072\n"
    "v_exp_f32 v27, v27\n"
    "v_mfma_f32_16x16x32_bf16 v[16:19], a[92:95], a[12:15], v[16:19]\n"
    "v_cvt_pk_bf16_f32 v100, v24, v25\n"
    "v_cvt_pk_bf16_f32 v101, v26, v27\n"
    "v_mfma_f32_16x16x32_bf16 v[20:23], a[92:95], a[28:31], 4.0\n"
    "v_exp_f32 v28, v28\n"
    "v_mfma_f32_16x16x32_bf16 v[20:23], a[88:91], a[24:27], v[20:23]\n"
    "ds_read_b128 a[100:103], v6 offset:50176\n"
    "v_mfma_f32_16x16x32_bf16 v[20:23], a[84:87], a[20:23], v[20:23]\n"
    "global_load_lds_dwordx4 v9, s[62:63] offset:1024 sc1\n"
    "v_mfma_f32_16x16x32_bf16 v[20:23], a[80:83], a[16:19], v[20:23]\n"
    "v_exp_f32 v29, v29\n"
    "v_mfma_f32_16x16x32_bf16 v[24:27], a[80:83], a[32:35], 4.0\n"
    "v_exp_f32 v30, v30\n"
    "v_mfma_f32_16x16x32_bf16 v[24:27], a[84:87], a[36:39], v[24:27]\n"
    "ds_read_b128 a[104:107], v6 offset:51200\n"
    "v_mfma_f32_16x16x32_bf16 v[24:27], a[88:91], a[40:43], v[24:27]\n"
    "v_exp_f32 v31, v31\n"
    "v_mfma_f32_16x16x32_bf16 v[24:27], a[92:95], a[44:47], v[24:27]\n"
    "v_cvt_pk_bf16_f32 v108, v28, v29\n"
    "v_cvt_pk_bf16_f32 v109, v30, v31\n"
    "v_mfma_f32_16x16x32_bf16 v[28:31], a[92:95], a[60:63], 4.0\n"
    "v_exp_f32 v16, v16\n"
    "v_mfma_f32_16x16x32_bf16 v[28:31], a[88:91], a[56:59], v[28:31]\n"
    "ds_read_b128 a[108:111], v6 offset:52224\n"
    "v_mfma_f32_16x16x32_bf16 v[28:31], a[84:87], a[52:55], v[28:31]\n"
    "v_exp_f32 v17, v17\n"
    "v_mfma_f32_16x16x32_bf16 v[28:31], a[80:83], a[48:51], v[28:31]\n"
    "global_load_lds_dwordx4 v9, s[62:63] offset:2048 sc1\n"
    "v_mfma_f32_16x16x32_bf16 v[224:227], v[48:51], a[112:115], v[224:227]\n"
    "v_exp_f32 v18, v18\n"
    "v_mfma_f32_16x16x32_bf16 v[224:227], v[52:55], a[116:119], v[224:227]\n"
    "ds_read_b128 a[80:83], v4 offset:36864\n"
    "v_mfma_f32_16x16x32_bf16 v[228:231], v[60:63], a[116:119], v[228:231]\n"
    "v_exp_f32 v19, v19\n"
    "v_mfma_f32_16x16x32_bf16 v[228:231], v[56:59], a[112:115], v[228:231]\n"
    "v_cvt_pk_bf16_f32 v86, v16, v17\n"
    "v_cvt_pk_bf16_f32 v87, v18, v19\n"
    "v_mfma_f32_16x16x32_bf16 v[232:235], v[64:67], a[112:115], v[232:235]\n"
    "v_exp_f32 v20, v20\n"
    "v_mfma_f32_16x16x32_bf16 v[232:235], v[68:71], a[116:119], v[232:235]\n"
    "ds_read_b128 a[84:87], v4 offset:37888\n"
    "v_mfma_f32_16x16x32_bf16 v[236:239], v[76:79], a[116:119], v[236:239]\n"
    "global_load_lds_dwordx4 v9, s[62:63] offset:3072 sc1\n"
    "v_mfma_f32_16x16x32_bf16 v[236:239], v[72:75], a[112:115], v[236:239]\n"
    "v_exp_f32 v21, v21\n"
    "v_mfma_f32_16x16x32_bf16 v[252:255], v[72:75], a[120:123], v[252:255]\n"
    "v_exp_f32 v22, v22\n"
    "v_mfma_f32_16x16x32_bf16 v[252:255], v[76:79], a[124:127], v[252:255]\n"
    "ds_read_b128 a[88:91], v4 offset:38912\n"
    "v_mfma_f32_16x16x32_bf16 v[248:251], v[68:71], a[124:127], v[248:251]\n"
    "v_exp_f32 v23, v23\n"
    "v_mfma_f32_16x16x32_bf16 v[248:251], v[64:67], a[120:123], v[248:251]\n"
    "v_cvt_pk_bf16_f32 v94, v20, v21\n"
    "v_cvt_pk_bf16_f32 v95, v22, v23\n"
    "v_mfma_f32_16x16x32_bf16 v[244:247], v[56:59], a[120:123], v[244:247]\n"
    "v_exp_f32 v24, v24\n"
    "v_mfma_f32_16x16x32_bf16 v[244:247], v[60:63], a[124:127], v[244:247]\n"
    "ds_read_b128 a[92:95], v4 offset:39936\n"
    "v_mfma_f32_16x16x32_bf16 v[240:243], v[52:55], a[124:127], v[240:243]\n"
    "s_waitcnt vmcnt(10)\n"
    "v_exp_f32 v25, v25\n"
    "v_mfma_f32_16x16x32_bf16 v[240:243], v[48:51], a[120:123], v[240:243]\n"
    "v_smfmac_f32_16x16x64_bf16 a[140:143], a[144:147], v[72:79], v10\n"
    "s_waitcnt lgkmcnt(4)\n"
    "v_mfma_f32_16x16x32_bf16 v[16:19], a[64:67], a[0:3], 4.0\n"
    "v_exp_f32 v26, v26\n"
    "v_mfma_f32_16x16x32_bf16 v[16:19], a[68:71], a[4:7], v[16:19]\n"
    "ds_read_b128 a[112:115], v6 offset:53248\n"
    "v_and_b32 v112, 0x7fff7fff, v112\n"
    "v_mfma_f32_16x16x32_bf16 v[16:19], a[72:75], a[8:11], v[16:19]\n"
    "v_exp_f32 v27, v27\n"
    "v_mfma_f32_16x16x32_bf16 v[16:19], a[76:79], a[12:15], v[16:19]\n"
    "v_cvt_pk_bf16_f32 v102, v24, v25\n"
    "v_cvt_pk_bf16_f32 v103, v26, v27\n"
    "v_mfma_f32_16x16x32_bf16 v[20:23], a[76:79], a[28:31], 4.0\n"
    "v_exp_f32 v28, v28\n"
    "v_mfma_f32_16x16x32_bf16 v[20:23], a[72:75], a[24:27], v[20:23]\n"
    "ds_read_b128 a[116:119], v6 offset:54272\n"
    "v_and_b32 v113, 0x7fff7fff, v113\n"
    "v_mfma_f32_16x16x32_bf16 v[20:23], a[68:71], a[20:23], v[20:23]\n"
    "v_and_b32 v114, 0x7fff7fff, v114\n"
    "v_and_b32 v115, 0x7fff7fff, v115\n"
    "v_mfma_f32_16x16x32_bf16 v[20:23], a[64:67], a[16:19], v[20:23]\n"
    "v_exp_f32 v29, v29\n"
    "v_mfma_f32_16x16x32_bf16 v[24:27], a[64:67], a[32:35], 4.0\n"
    "v_exp_f32 v30, v30\n"
    "v_mfma_f32_16x16x32_bf16 v[24:27], a[68:71], a[36:39], v[24:27]\n"
    "ds_read_b128 a[120:123], v6 offset:55296\n"
    "v_and_b32 v116, 0x7fff7fff, v116\n"
    "v_mfma_f32_16x16x32_bf16 v[24:27], a[72:75], a[40:43], v[24:27]\n"
    "v_exp_f32 v31, v31\n"
    "v_mfma_f32_16x16x32_bf16 v[24:27], a[76:79], a[44:47], v[24:27]\n"
    "v_cvt_pk_bf16_f32 v110, v28, v29\n"
    "v_cvt_pk_bf16_f32 v111, v30, v31\n"
    "v_mfma_f32_16x16x32_bf16 v[28:31], a[76:79], a[60:63], 4.0\n"
    "v_exp_f32 v16, v16\n"
    "v_mfma_f32_16x16x32_bf16 v[28:31], a[72:75], a[56:59], v[28:31]\n"
    "ds_read_b128 a[124:127], v6 offset:56320\n"
    "v_and_b32 v117, 0x7fff7fff, v117\n"
    "v_mfma_f32_16x16x32_bf16 v[28:31], a[68:71], a[52:55], v[28:31]\n"
    "v_exp_f32 v17, v17\n"
    "v_mfma_f32_16x16x32_bf16 v[28:31], a[64:67], a[48:51], v[28:31]\n"
    "v_and_b32 v118, 0x7fff7fff, v118\n"
    "v_and_b32 v119, 0x7fff7fff, v119\n"
    "v_mfma_f32_16x16x32_bf16 v[128:131], v[80:83], a[96:99], v[128:131]\n"
    "v_exp_f32 v18, v18\n"
    "v_mfma_f32_16x16x32_bf16 v[128:131], v[84:87], a[100:103], v[128:131]\n"
    "ds_read_b128 a[64:67], v4 offset:40960\n"
    "v_lshlrev_b32 v120, 16, v112\n"
    "v_mfma_f32_16x16x32_bf16 v[132:135], v[92:95], a[100:103], v[132:135]\n"
    "v_exp_f32 v19, v19\n"
    "v_mfma_f32_16x16x32_bf16 v[132:135], v[88:91], a[96:99], v[132:135]\n"
    "v_cvt_pk_bf16_f32 v48, v16, v17\n"
    "v_cvt_pk_bf16_f32 v49, v18, v19\n"
    "v_mfma_f32_16x16x32_bf16 v[136:139], v[96:99], a[96:99], v[136:139]\n"
    "v_exp_f32 v20, v20\n"
    "v_mfma_f32_16x16x32_bf16 v[136:139], v[100:103], a[100:103], v[136:139]\n"
    "ds_read_b128 a[68:71], v4 offset:41984\n"
    "v_max_u32 v122, v112, v120\n"
    "v_mfma_f32_16x16x32_bf16 v[140:143], v[108:111], a[100:103], v[140:143]\n"
    "v_lshlrev_b32 v121, 16, v113\n"
    "v_max3_u32 v122, v122, v113, v121\n"
    "v_mfma_f32_16x16x32_bf16 v[140:143], v[104:107], a[96:99], v[140:143]\n"
    "v_exp_f32 v21, v21\n"
    "v_mfma_f32_16x16x32_bf16 v[156:159], v[104:107], a[104:107], v[156:159]\n"
    "v_exp_f32 v22, v22\n"
    "v_mfma_f32_16x16x32_bf16 v[156:159], v[108:111], a[108:111], v[156:159]\n"
    "ds_read_b128 a[72:75], v4 offset:43008\n"
    "v_lshlrev_b32 v120, 16, v114\n"
    "v_mfma_f32_16x16x32_bf16 v[152:155], v[100:103], a[108:111], v[152:155]\n"
    "v_exp_f32 v23, v23\n"
    "v_mfma_f32_16x16x32_bf16 v[152:155], v[96:99], a[104:107], v[152:155]\n"
    "v_cvt_pk_bf16_f32 v56, v20, v21\n"
    "v_cvt_pk_bf16_f32 v57, v22, v23\n"
    "v_mfma_f32_16x16x32_bf16 v[148:151], v[88:91], a[104:107], v[148:151]\n"
    "v_exp_f32 v24, v24\n"
    "v_mfma_f32_16x16x32_bf16 v[148:151], v[92:95], a[108:111], v[148:151]\n"
    "ds_read_b128 a[76:79], v4 offset:44032\n"
    "v_max3_u32 v122, v122, v114, v120\n"
    "v_mfma_f32_16x16x32_bf16 v[144:147], v[84:87], a[108:111], v[144:147]\n"
    "v_exp_f32 v25, v25\n"
    "v_mfma_f32_16x16x32_bf16 v[144:147], v[80:83], a[104:107], v[144:147]\n"
    "v_lshlrev_b32 v121, 16, v115\n"
    "v_max3_u32 v122, v122, v115, v121\n"
    "v_smfmac_f32_16x16x64_bf16 a[128:131], a[144:147], v[80:87], v10\n"
    "s_waitcnt lgkmcnt(4)\n"
    "v_lshlrev_b32 v120, 16, v116\n"
    "v_max3_u32 v122, v122, v116, v120\n"
    "v_mfma_f32_16x16x32_bf16 v[16:19], a[80:83], a[0:3], 4.0\n"
    "v_exp_f32 v26, v26\n"
    "v_mfma_f32_16x16x32_bf16 v[16:19], a[84:87], a[4:7], v[16:19]\n"
    "ds_read_b128 a[96:99], v6 offset:57344\n"
    "v_lshlrev_b32 v121, 16, v117\n"
    "v_mfma_f32_16x16x32_bf16 v[16:19], a[88:91], a[8:11], v[16:19]\n"
    "v_exp_f32 v27, v27\n"
    "v_mfma_f32_16x16x32_bf16 v[16:19], a[92:95], a[12:15], v[16:19]\n"
    "v_cvt_pk_bf16_f32 v64, v24, v25\n"
    "v_cvt_pk_bf16_f32 v65, v26, v27\n"
    "v_mfma_f32_16x16x32_bf16 v[20:23], a[92:95], a[28:31], 4.0\n"
    "v_exp_f32 v28, v28\n"
    "v_mfma_f32_16x16x32_bf16 v[20:23], a[88:91], a[24:27], v[20:23]\n"
    "ds_read_b128 a[100:103], v6 offset:58368\n"
    "v_max3_u32 v122, v122, v117, v121\n"
    "v_mfma_f32_16x16x32_bf16 v[20:23], a[84:87], a[20:23], v[20:23]\n"
    "v_lshlrev_b32 v120, 16, v118\n"
    "v_max3_u32 v122, v122, v118, v120\n"
    "v_mfma_f32_16x16x32_bf16 v[20:23], a[80:83], a[16:19], v[20:23]\n"
    "v_exp_f32 v29, v29\n"
    "v_mfma_f32_16x16x32_bf16 v[24:27], a[80:83], a[32:35], 4.0\n"
    "v_exp_f32 v30, v30\n"
    "v_mfma_f32_16x16x32_bf16 v[24:27], a[84:87], a[36:39], v[24:27]\n"
    "ds_read_b128 a[104:107], v6 offset:59392\n"
    "v_lshlrev_b32 v121, 16, v119\n"
    "v_mfma_f32_16x16x32_bf16 v[24:27], a[88:91], a[40:43], v[24:27]\n"
    "v_exp_f32 v31, v31\n"
    "v_mfma_f32_16x16x32_bf16 v[24:27], a[92:95], a[44:47], v[24:27]\n"
    "v_cvt_pk_bf16_f32 v72, v28, v29\n"
    "v_cvt_pk_bf16_f32 v73, v30, v31\n"
    "v_mfma_f32_16x16x32_bf16 v[28:31], a[92:95], a[60:63], 4.0\n"
    "v_exp_f32 v16, v16\n"
    "v_mfma_f32_16x16x32_bf16 v[28:31], a[88:91], a[56:59], v[28:31]\n"
    "ds_read_b128 a[108:111], v6 offset:60416\n"
    "v_max3_u32 v122, v122, v119, v121\n"
    "v_mfma_f32_16x16x32_bf16 v[28:31], a[84:87], a[52:55], v[28:31]\n"
    "v_exp_f32 v17, v17\n"
    "v_mfma_f32_16x16x32_bf16 v[28:31], a[80:83], a[48:51], v[28:31]\n"
    "v_cmp_le_u32 vcc, 0x49800000, v122\n"
    "v_mov_b32 v2, s61\n"
    "v_mfma_f32_16x16x32_bf16 v[160:163], v[80:83], a[112:115], v[160:163]\n"
    "v_exp_f32 v18, v18\n"
    "v_mfma_f32_16x16x32_bf16 v[160:163], v[84:87], a[116:119], v[160:163]\n"
    "ds_read_b128 a[80:83], v4 offset:45056\n"
    "v_mfma_f32_16x16x32_bf16 v[164:167], v[92:95], a[116:119], v[164:167]\n"
    "v_exp_f32 v19, v19\n"
    "v_mfma_f32_16x16x32_bf16 v[164:167], v[88:91], a[112:115], v[164:167]\n"
    "v_cvt_pk_bf16_f32 v50, v16, v17\n"
    "v_cvt_pk_bf16_f32 v51, v18, v19\n"
    "v_mfma_f32_16x16x32_bf16 v[168:171], v[96:99], a[112:115], v[168:171]\n"
    "v_exp_f32 v20, v20\n"
    "v_mfma_f32_16x16x32_bf16 v[168:171], v[100:103], a[116:119], v[168:171]\n"
    "ds_read_b128 a[84:87], v4 offset:46080\n"
    "v_mfma_f32_16x16x32_bf16 v[172:175], v[108:111], a[116:119], v[172:175]\n"
    "v_mfma_f32_16x16x32_bf16 v[172:175], v[104:107], a[112:115], v[172:175]\n"
    "v_exp_f32 v21, v21\n"
    "v_mfma_f32_16x16x32_bf16 v[188:191], v[104:107], a[120:123], v[188:191]\n"
    "v_exp_f32 v22, v22\n"
    "v_mfma_f32_16x16x32_bf16 v[188:191], v[108:111], a[124:127], v[188:191]\n"
    "ds_read_b128 a[88:91], v4 offset:47104\n"
    "v_mfma_f32_16x16x32_bf16 v[184:187], v[100:103], a[124:127], v[184:187]\n"
    "v_exp_f32 v23, v23\n"
    "v_mfma_f32_16x16x32_bf16 v[184:187], v[96:99], a[120:123], v[184:187]\n"
    "v_cvt_pk_bf16_f32 v58, v20, v21\n"
    "v_cvt_pk_bf16_f32 v59, v22, v23\n"
    "v_mfma_f32_16x16x32_bf16 v[180:183], v[88:91], a[120:123], v[180:183]\n"
    "v_exp_f32 v24, v24\n"
    "v_mfma_f32_16x16x32_bf16 v[180:183], v[92:95], a[124:127], v[180:183]\n"
    "ds_read_b128 a[92:95], v4 offset:48128\n"
    "v_mfma_f32_16x16x32_bf16 v[176:179], v[84:87], a[124:127], v[176:179]\n"
    "v_exp_f32 v25, v25\n"
    "v_mfma_f32_16x16x32_bf16 v[176:179], v[80:83], a[120:123], v[176:179]\n"
    "v_smfmac_f32_16x16x64_bf16 a[132:135], a[144:147], v[88:95], v10\n"
    "s_waitcnt lgkmcnt(4)\n"
    "v_mfma_f32_16x16x32_bf16 v[16:19], a[64:67], a[0:3], 4.0\n"
    "s_cmp_lg_u64 vcc, 0\n"
    "v_exp_f32 v26, v26\n"
    "v_mfma_f32_16x16x32_bf16 v[16:19], a[68:71], a[4:7], v[16:19]\n"
    "ds_read_b128 a[112:115], v6 offset:61440\n"
    "v_mfma_f32_16x16x32_bf16 v[16:19], a[72:75], a[8:11], v[16:19]\n"
    "s_cselect_b32 s75, 1, 0\n"
    "v_exp_f32 v27, v27\n"
    "v_mfma_f32_16x16x32_bf16 v[16:19], a[76:79], a[12:15], v[16:19]\n"
    "v_cvt_pk_bf16_f32 v66, v24, v25\n"
    "v_cvt_pk_bf16_f32 v67, v26, v27\n"
    "v_mfma_f32_16x16x32_bf16 v[20:23], a[76:79], a[28:31], 4.0\n"
    "s_or_b32 s71, s71, s75\n"
    "v_exp_f32 v28, v28\n"
    "v_mfma_f32_16x16x32_bf16 v[20:23], a[72:75], a[24:27], v[20:23]\n"
    "ds_read_b128 a[116:119], v6 offset:62464\n"
    "v_mov_b32 v3, s71\n"
    "v_mfma_f32_16x16x32_bf16 v[20:23], a[68:71], a[20:23], v[20:23]\n"
    "ds_write_b32 v2, v3\n"
    "v_mfma_f32_16x16x32_bf16 v[20:23], a[64:67], a[16:19], v[20:23]\n"
    "v_exp_f32 v29, v29\n"
    "v_mfma_f32_16x16x32_bf16 v[24:27], a[64:67], a[32:35], 4.0\n"
    "v_exp_f32 v30, v30\n"
    "v_mfma_f32_16x16x32_bf16 v[24:27], a[68:71], a[36:39], v[24:27]\n"
    "ds_read_b128 a[120:123], v6 offset:63488\n"
    "v_mfma_f32_16x16x32_bf16 v[24:27], a[72:75], a[40:43], v[24:27]\n"
    "v_exp_f32 v31, v31\n"
    "v_mfma_f32_16x16x32_bf16 v[24:27], a[76:79], a[44:47], v[24:27]\n"
    "v_cvt_pk_bf16_f32 v74, v28, v29\n"
    "v_cvt_pk_bf16_f32 v75, v30, v31\n"
    "v_mfma_f32_16x16x32_bf16 v[28:31], a[76:79], a[60:63], 4.0\n"
    "s_waitcnt lgkmcnt(1)\n"
    "v_exp_f32 v16, v16\n"
    "v_mfma_f32_16x16x32_bf16 v[28:31], a[72:75], a[56:59], v[28:31]\n"
    "ds_read_b128 a[124:127], v6 offset:64512\n"
    "v_mfma_f32_16x16x32_bf16 v[28:31], a[68:71], a[52:55], v[28:31]\n"
    "s_waitcnt vmcnt(0)\n"
    "v_exp_f32 v17, v17\n"
    "v_mfma_f32_16x16x32_bf16 v[28:31], a[64:67], a[48:51], v[28:31]\n"
    "s_barrier\n"
    "v_mfma_f32_16x16x32_bf16 v[192:195], v[80:83], a[96:99], v[192:195]\n"
    "v_exp_f32 v18, v18\n"
    "v_mfma_f32_16x16x32_bf16 v[192:195], v[84:87], a[100:103], v[192:195]\n"
    "ds_read_b128 a[160:163], v5 offset:0\n"
    "ds_read_b128 a[164:167], v5 offset:1024\n"
    "v_mfma_f32_16x16x32_bf16 v[196:199], v[92:95], a[100:103], v[196:199]\n"
    "v_exp_f32 v19, v19\n"
    "v_mfma_f32_16x16x32_bf16 v[196:199], v[88:91], a[96:99], v[196:199]\n"
    "v_cvt_pk_bf16_f32 v52, v16, v17\n"
    "v_cvt_pk_bf16_f32 v53, v18, v19\n"
    "v_mfma_f32_16x16x32_bf16 v[200:203], v[96:99], a[96:99], v[200:203]\n"
    "v_exp_f32 v20, v20\n"
    "v_mfma_f32_16x16x32_bf16 v[200:203], v[100:103], a[100:103], v[200:203]\n"
    "ds_read_b128 a[168:171], v5 offset:2048\n"
    "v_mfma_f32_16x16x32_bf16 v[204:207], v[108:111], a[100:103], v[204:207]\n"
    "ds_read_b128 a[172:175], v5 offset:3072\n"
    "ds_read_b128 a[188:191], v5 offset:7168\n"
    "v_mfma_f32_16x16x32_bf16 v[204:207], v[104:107], a[96:99], v[204:207]\n"
    "v_exp_f32 v21, v21\n"
    "v_mfma_f32_16x16x32_bf16 v[220:223], v[104:107], a[104:107], v[220:223]\n"
    "v_exp_f32 v22, v22\n"
    "v_mfma_f32_16x16x32_bf16 v[220:223], v[108:111], a[108:111], v[220:223]\n"
    "ds_read_b128 a[184:187], v5 offset:6144\n"
    "ds_read_b128 a[180:183], v5 offset:5120\n"
    "v_mfma_f32_16x16x32_bf16 v[216:219], v[100:103], a[108:111], v[216:219]\n"
    "v_exp_f32 v23, v23\n"
    "v_mfma_f32_16x16x32_bf16 v[216:219], v[96:99], a[104:107], v[216:219]\n"
    "v_cvt_pk_bf16_f32 v60, v20, v21\n"
    "v_cvt_pk_bf16_f32 v61, v22, v23\n"
    "v_mfma_f32_16x16x32_bf16 v[212:215], v[88:91], a[104:107], v[212:215]\n"
    "v_exp_f32 v24, v24\n"
    "v_mfma_f32_16x16x32_bf16 v[212:215], v[92:95], a[108:111], v[212:215]\n"
    "ds_read_b128 a[176:179], v5 offset:4096\n"
    "v_mfma_f32_16x16x32_bf16 v[208:211], v[84:87], a[108:111], v[208:211]\n"
    "v_exp_f32 v25, v25\n"
    "v_mfma_f32_16x16x32_bf16 v[208:211], v[80:83], a[104:107], v[208:211]\n"
    "s_waitcnt lgkmcnt(3)\n"
    "ds_read_b128 a[192:195], v5 offset:8192\n"
    "ds_read_b128 a[196:199], v5 offset:9216\n"
    "v_smfmac_f32_16x16x64_bf16 a[136:139], a[144:147], v[96:103], v10\n"
    "ds_read_b128 a[200:203], v5 offset:10240\n"
    "ds_read_b128 a[204:207], v5 offset:11264\n"
    "v_mfma_f32_16x16x32_bf16 v[16:19], a[80:83], a[0:3], 4.0\n"
    "v_exp_f32 v26, v26\n"
    "v_mfma_f32_16x16x32_bf16 v[16:19], a[84:87], a[4:7], v[16:19]\n"
    "ds_read_b128 a[220:223], v5 offset:15360\n"
    "ds_read_b128 a[216:219], v5 offset:14336\n"
    "v_mfma_f32_16x16x32_bf16 v[16:19], a[88:91], a[8:11], v[16:19]\n"
    "v_exp_f32 v27, v27\n"
    "v_mfma_f32_16x16x32_bf16 v[16:19], a[92:95], a[12:15], v[16:19]\n"
    "v_cvt_pk_bf16_f32 v68, v24, v25\n"
    "v_cvt_pk_bf16_f32 v69, v26, v27\n"
    "v_mfma_f32_16x16x32_bf16 v[20:23], a[92:95], a[28:31], 4.0\n"
    "v_exp_f32 v28, v28\n"
    "v_mfma_f32_16x16x32_bf16 v[20:23], a[88:91], a[24:27], v[20:23]\n"
    "ds_read_b128 a[212:215], v5 offset:13312\n"
    "v_mfma_f32_16x16x32_bf16 v[20:23], a[84:87], a[20:23], v[20:23]\n"
    "s_waitcnt lgkmcnt(5)\n"
    "ds_read_b128 a[208:211], v5 offset:12288\n"
    "ds_read_b128 a[224:227], v7 offset:16384\n"
    "v_mfma_f32_16x16x32_bf16 v[20:23], a[80:83], a[16:19], v[20:23]\n"
    "v_exp_f32 v29, v29\n"
    "v_mfma_f32_16x16x32_bf16 v[24:27], a[80:83], a[32:35], 4.0\n"
    "v_exp_f32 v30, v30\n"
    "v_mfma_f32_16x16x32_bf16 v[24:27], a[84:87], a[36:39], v[24:27]\n"
    "ds_read_b128 a[228:231], v7 offset:17408\n"
    "ds_read_b128 a[236:239], v7 offset:19456\n"
    "v_mfma_f32_16x16x32_bf16 v[24:27], a[88:91], a[40:43], v[24:27]\n"
    "v_exp_f32 v31, v31\n"
    "v_mfma_f32_16x16x32_bf16 v[24:27], a[92:95], a[44:47], v[24:27]\n"
    "v_cvt_pk_bf16_f32 v76, v28, v29\n"
    "v_cvt_pk_bf16_f32 v77, v30, v31\n"
    "v_mfma_f32_16x16x32_bf16 v[28:31], a[92:95], a[60:63], 4.0\n"
    "v_exp_f32 v16, v16\n"
    "v_mfma_f32_16x16x32_bf16 v[28:31], a[88:91], a[56:59], v[28:31]\n"
    "ds_read_b128 a[232:235], v7 offset:18432\n"
    "v_mfma_f32_16x16x32_bf16 v[28:31], a[84:87], a[52:55], v[28:31]\n"
    "v_exp_f32 v17, v17\n"
    "v_mfma_f32_16x16x32_bf16 v[28:31], a[80:83], a[48:51], v[28:31]\n"
    "s_waitcnt lgkmcnt(3)\n"
    "ds_read_b128 a[240:243], v7 offset:20480\n"
    "ds_read_b128 a[244:247], v7 offset:21504\n"
    "v_mfma_f32_16x16x32_bf16 v[224:227], v[80:83], a[112:115], v[224:227]\n"
    "v_exp_f32 v18, v18\n"
    "v_mfma_f32_16x16x32_bf16 v[224:227], v[84:87], a[116:119], v[224:227]\n"
    "ds_read_b128 a[252:255], v7 offset:23552\n"
    "ds_read_b128 a[248:251], v7 offset:22528\n"
    "v_mfma_f32_16x16x32_bf16 v[228:231], v[92:95], a[116:119], v[228:231]\n"
    "v_exp_f32 v19, v19\n"
    "v_mfma_f32_16x16x32_bf16 v[228:231], v[88:91], a[112:115], v[228:231]\n"
    "v_cvt_pk_bf16_f32 v54, v16, v17\n"
    "v_cvt_pk_bf16_f32 v55, v18, v19\n"
    "v_mfma_f32_16x16x32_bf16 v[232:235], v[96:99], a[112:115], v[232:235]\n"
    "v_exp_f32 v20, v20\n"
    "v_mfma_f32_16x16x32_bf16 v[232:235], v[100:103], a[116:119], v[232:235]\n"
    "ds_read_b128 a[148:151], v7 offset:24576\n"
    "v_mfma_f32_16x16x32_bf16 v[236:239], v[108:111], a[116:119], v[236:239]\n"
    "ds_read_b128 a[152:155], v7 offset:25600\n"
    "ds_read_b128 v[32:35], v7 offset:27648\n"
    "v_mfma_f32_16x16x32_bf16 v[236:239], v[104:107], a[112:115], v[236:239]\n"
    "v_exp_f32 v21, v21\n"
    "v_mfma_f32_16x16x32_bf16 v[252:255], v[104:107], a[120:123], v[252:255]\n"
    "v_exp_f32 v22, v22\n"
    "v_mfma_f32_16x16x32_bf16 v[252:255], v[108:111], a[124:127], v[252:255]\n"
    "s_waitcnt lgkmcnt(3)\n"
    "ds_read_b128 a[156:159], v7 offset:26624\n"
    "ds_read_b128 v[36:39], v7 offset:28672\n"
    "v_mfma_f32_16x16x32_bf16 v[248:251], v[100:103], a[124:127], v[248:251]\n"
    "v_exp_f32 v23, v23\n"
    "v_mfma_f32_16x16x32_bf16 v[248:251], v[96:99], a[120:123], v[248:251]\n"
    "v_cvt_pk_bf16_f32 v62, v20, v21\n"
    "v_cvt_pk_bf16_f32 v63, v22, v23\n"
    "v_mfma_f32_16x16x32_bf16 v[244:247], v[88:91], a[120:123], v[244:247]\n"
    "v_exp_f32 v24, v24\n"
    "v_mfma_f32_16x16x32_bf16 v[244:247], v[92:95], a[124:127], v[244:247]\n"
    "ds_read_b128 v[40:43], v7 offset:29696\n"
    "v_mfma_f32_16x16x32_bf16 v[240:243], v[84:87], a[124:127], v[240:243]\n"
    "v_exp_f32 v25, v25\n"
    "v_mfma_f32_16x16x32_bf16 v[240:243], v[80:83], a[120:123], v[240:243]\n"
    "ds_read_b128 v[124:127], v7 offset:31744\n"
    "ds_read_b128 v[44:47], v7 offset:30720\n"
    "v_smfmac_f32_16x16x64_bf16 a[140:143], a[144:147], v[104:111], v10\n"
    "ds_read_b128 a[64:67], v7 offset:49152\n"
    "ds_read_b128 a[68:71], v7 offset:50176\n"
    "v_mfma_f32_16x16x32_bf16 v[16:19], a[160:163], a[0:3], 4.0\n"
    "v_exp_f32 v26, v26\n"
    "v_mfma_f32_16x16x32_bf16 v[16:19], a[164:167], a[4:7], v[16:19]\n"
    "s_waitcnt lgkmcnt(5)\n"
    "ds_read_b128 a[76:79], v7 offset:52224\n"
    "ds_read_b128 a[72:75], v7 offset:51200\n"
    "v_mfma_f32_16x16x32_bf16 v[16:19], a[168:171], a[8:11], v[16:19]\n"
    "v_exp_f32 v27, v27\n"
    "v_mfma_f32_16x16x32_bf16 v[16:19], a[172:175], a[12:15], v[16:19]\n"
    "v_cvt_pk_bf16_f32 v70, v24, v25\n"
    "v_cvt_pk_bf16_f32 v71, v26, v27\n"
    "v_mfma_f32_16x16x32_bf16 v[20:23], a[188:191], a[12:15], 4.0\n"
    "v_exp_f32 v28, v28\n"
    "v_mfma_f32_16x16x32_bf16 v[20:23], a[184:187], a[8:11], v[20:23]\n"
    "ds_read_b128 a[96:99], v7 offset:53248\n"
    "ds_read_b128 a[100:103], v7 offset:54272\n"
    "v_mfma_f32_16x16x32_bf16 v[20:23], a[180:183], a[4:7], v[20:23]\n"
    "ds_read_b128 a[108:111], v7 offset:56320\n"
    "ds_read_b128 a[104:107], v7 offset:55296\n"
    "v_mfma_f32_16x16x32_bf16 v[20:23], a[176:179], a[0:3], v[20:23]\n"
    "v_exp_f32 v29, v29\n"
    "v_mfma_f32_16x16x32_bf16 v[24:27], a[192:195], a[0:3], 4.0\n"
    "v_exp_f32 v30, v30\n"
    "v_mfma_f32_16x16x32_bf16 v[24:27], a[196:199], a[4:7], v[24:27]\n"
    "s_waitcnt lgkmcnt(4)\n"
    "ds_read_b128 a[80:83], v7 offset:57344\n"
    "ds_read_b128 a[84:87], v7 offset:58368\n"
    "v_mfma_f32_16x16x32_bf16 v[24:27], a[200:203], a[8:11], v[24:27]\n"
    "v_exp_f32 v31, v31\n"
    "v_mfma_f32_16x16x32_bf16 v[24:27], a[204:207], a[12:15], v[24:27]\n"
    "v_cvt_pk_bf16_f32 v78, v28, v29\n"
    "v_cvt_pk_bf16_f32 v79, v30, v31\n"
    "v_mfma_f32_16x16x32_bf16 v[28:31], a[220:223], a[12:15], 4.0\n"
    "v_exp_f32 v16, v16\n"
    "v_mfma_f32_16x16x32_bf16 v[28:31], a[216:219], a[8:11], v[28:31]\n"
    "v_exp_f32 v17, v17\n"
    "v_mfma_f32_16x16x32_bf16 v[28:31], a[212:215], a[4:7], v[28:31]\n"
    "v_exp_f32 v18, v18\n"
    "v_mfma_f32_16x16x32_bf16 v[28:31], a[208:211], a[0:3], v[28:31]\n"
    "ds_read_b128 a[92:95], v7 offset:60416\n"
    "v_cvt_pk_bf16_f32 v80, v16, v17\n"
    "v_mfma_f32_16x16x32_bf16 v[128:131], v[48:51], a[224:227], v[128:131]\n"
    "v_exp_f32 v19, v19\n"
    "v_mfma_f32_16x16x32_bf16 v[128:131], v[52:55], a[228:231], v[128:131]\n"
    "v_exp_f32 v20, v20\n"
    "v_mfma_f32_16x16x32_bf16 v[144:147], v[52:55], a[236:239], v[144:147]\n"
    "v_exp_f32 v21, v21\n"
    "v_mfma_f32_16x16x32_bf16 v[144:147], v[48:51], a[232:235], v[144:147]\n"
    "v_exp_f32 v22, v22\n"
    "v_mfma_f32_16x16x32_bf16 v[160:163], v[48:51], a[240:243], v[160:163]\n"
    "v_cvt_pk_bf16_f32 v81, v18, v19\n"
    "v_cvt_pk_bf16_f32 v82, v20, v21\n"
    "v_mfma_f32_16x16x32_bf16 v[160:163], v[52:55], a[244:247], v[160:163]\n"
    "v_exp_f32 v23, v23\n"
    "v_mfma_f32_16x16x32_bf16 v[176:179], v[52:55], a[252:255], v[176:179]\n"
    "v_exp_f32 v24, v24\n"
    "v_mfma_f32_16x16x32_bf16 v[176:179], v[48:51], a[248:251], v[176:179]\n"
    "v_exp_f32 v25, v25\n"
    "v_mfma_f32_16x16x32_bf16 v[192:195], v[48:51], a[148:151], v[192:195]\n"
    "v_exp_f32 v26, v26\n"
    "v_mfma_f32_16x16x32_bf16 v[192:195], v[52:55], a[152:155], v[192:195]\n"
    "v_cvt_pk_bf16_f32 v83, v22, v23\n"
    "v_cvt_pk_bf16_f32 v84, v24, v25\n"
    "v_mfma_f32_16x16x32_bf16 v[208:211], v[52:55], v[32:35], v[208:211]\n"
    "v_exp_f32 v27, v27\n"
    "v_mfma_f32_16x16x32_bf16 v[208:211], v[48:51], a[156:159], v[208:211]\n"
    "v_exp_f32 v28, v28\n"
    "v_mfma_f32_16x16x32_bf16 v[224:227], v[48:51], v[36:39], v[224:227]\n"
    "L%=_w11:\n"
    "s_cbranch_execz L%=_wret\n"
    "v_exp_f32 v29, v29\n"
    "v_mfma_f32_16x16x32_bf16 v[224:227], v[52:55], v[40:43], v[224:227]\n"
    "v_exp_f32 v30, v30\n"
    "v_mfma_f32_16x16x32_bf16 v[240:243], v[52:55], v[124:127], v[240:243]\n"
    "v_exp_f32 v31, v31\n"
    "v_mfma_f32_16x16x32_bf16 v[240:243], v[48:51], v[44:47], v[240:243]\n"
    "v_cvt_pk_bf16_f32 v85, v26, v27\n"
    "v_cvt_pk_bf16_f32 v86, v28, v29\n"
    "v_cvt_pk_bf16_f32 v87, v30, v31\n"
    "v_smfmac_f32_16x16x64_bf16 a[128:131], a[144:147], v[48:55], v10\n"
    "ds_read_b128 a[88:91], v7 offset:59392\n"
    "v_mov_b32 v12, 0x3d000000\n"
    "v_smfmac_f32_16x16x64_bf16 a[128:131], a[144:147], v[80:87], v10\n"
    "ds_read_b128 a[112:115], v7 offset:61440\n"
    "v_mov_b32 v3, 131072\n"
    "v_mfma_f32_16x16x32_bf16 v[128:131], v[80:83], a[64:67], v[128:131]\n"
    "ds_read_b128 a[116:119], v7 offset:62464\n"
    "v_mfma_f32_16x16x32_bf16 v[128:131], v[84:87], a[68:71], v[128:131]\n"
    "ds_read_b128 a[124:127], v7 offset:64512\n"
    "v_mfma_f32_16x16x32_bf16 v[144:147], v[84:87], a[76:79], v[144:147]\n"
    "s_waitcnt lgkmcnt(4)\n"
    "ds_read_b128 a[120:123], v7 offset:63488\n"
    "v_mfma_f32_16x16x32_bf16 v[144:147], v[80:83], a[72:75], v[144:147]\n"
    "v_mfma_f32_16x16x32_bf16 v[160:163], v[80:83], a[96:99], v[160:163]\n"
    "s_nop 0\n"
    "v_mfma_f32_16x16x4_f32 v[112:115], a128, v12, 0\n"
    "v_mfma_f32_16x16x32_bf16 v[160:163], v[84:87], a[100:103], v[160:163]\n"
    "v_mfma_f32_16x16x32_bf16 v[176:179], v[84:87], a[108:111], v[176:179]\n"
    "v_mfma_f32_16x16x32_bf16 v[176:179], v[80:83], a[104:107], v[176:179]\n"
    "v_mfma_f32_16x16x32_bf16 v[192:195], v[80:83], a[80:83], v[192:195]\n"
    "v_mfma_f32_16x16x32_bf16 v[192:195], v[84:87], a[84:87], v[192:195]\n"
    "v_mfma_f32_16x16x32_bf16 v[208:211], v[84:87], a[92:95], v[208:211]\n"
    "s_waitcnt lgkmcnt(0)\n"
    "s_nop 2\n"
    "v_max_u32 v8, v112, v113\n"
    "v_min_u32 v9, v112, v113\n"
    "v_mfma_f32_16x16x32_bf16 v[208:211], v[80:83], a[88:91], v[208:211]\n"
    "v_max3_u32 v8, v8, v114, v115\n"
    "v_min3_u32 v9, v9, v114, v115\n"
    "v_mfma_f32_16x16x32_bf16 v[224:227], v[80:83], a[112:115], v[224:227]\n"
    "v_rcp_f32 v112, v112\n"
    "v_mfma_f32_16x16x32_bf16 v[224:227], v[84:87], a[116:119], v[224:227]\n"
    "v_rcp_f32 v113, v113\n"
    "v_mfma_f32_16x16x32_bf16 v[240:243], v[84:87], a[124:127], v[240:243]\n"
    "v_rcp_f32 v114, v114\n"
    "v_mfma_f32_16x16x32_bf16 v[240:243], v[80:83], a[120:123], v[240:243]\n"
    "v_rcp_f32 v115, v115\n"
    "v_mfma_f32_16x16x32_bf16 v[16:19], a[160:163], a[16:19], 4.0\n"
    "ds_read_b128 v[120:123], v3\n"
    "v_mfma_f32_16x16x32_bf16 v[16:19], a[164:167], a[20:23], v[16:19]\n"
    "v_mul_f32 v128, v128, v112\n"
    "v_mul_f32 v144, v144, v112\n"
    "v_mfma_f32_16x16x32_bf16 v[16:19], a[168:171], a[24:27], v[16:19]\n"
    "v_mul_f32 v160, v160, v112\n"
    "v_mul_f32 v176, v176, v112\n"
    "v_mfma_f32_16x16x32_bf16 v[16:19], a[172:175], a[28:31], v[16:19]\n"
    "v_mul_f32 v192, v192, v112\n"
    "v_mul_f32 v208, v208, v112\n"
    "v_mfma_f32_16x16x32_bf16 v[20:23], a[188:191], a[28:31], 4.0\n"
    "v_mul_f32 v224, v224, v112\n"
    "v_mul_f32 v240, v240, v112\n"
    "v_mfma_f32_16x16x32_bf16 v[20:23], a[184:187], a[24:27], v[20:23]\n"
    "v_cvt_pk_bf16_f32 v48, v128, v144\n"
    "v_cvt_pk_bf16_f32 v49, v160, v176\n"
    "v_mfma_f32_16x16x32_bf16 v[20:23], a[180:183], a[20:23], v[20:23]\n"
    "v_cvt_pk_bf16_f32 v50, v192, v208\n"
    "v_cvt_pk_bf16_f32 v51, v224, v240\n"
    "v_mfma_f32_16x16x32_bf16 v[20:23], a[176:179], a[16:19], v[20:23]\n"
    "global_store_dwordx4 v6, v[48:51], s[42:43] offset:0 sc1\n"
    "v_mul_f32 v129, v129, v113\n"
    "v_mfma_f32_16x16x32_bf16 v[24:27], a[192:195], a[16:19], 4.0\n"
    "v_exp_f32 v16, v16\n"
    "v_mfma_f32_16x16x32_bf16 v[24:27], a[196:199], a[20:23], v[24:27]\n"
    "v_exp_f32 v17, v17\n"
    "v_mfma_f32_16x16x32_bf16 v[24:27], a[200:203], a[24:27], v[24:27]\n"
    "v_cvt_pk_bf16_f32 v88, v16, v17\n"
    "v_mul_f32 v145, v145, v113\n"
    "v_mfma_f32_16x16x32_bf16 v[24:27], a[204:207], a[28:31], v[24:27]\n"
    "v_exp_f32 v18, v18\n"
    "v_mfma_f32_16x16x32_bf16 v[28:31], a[220:223], a[28:31], 4.0\n"
    "v_exp_f32 v19, v19\n"
    "v_mfma_f32_16x16x32_bf16 v[28:31], a[216:219], a[24:27], v[28:31]\n"
    "v_cvt_pk_bf16_f32 v89, v18, v19\n"
    "v_mul_f32 v161, v161, v113\n"
    "v_mfma_f32_16x16x32_bf16 v[28:31], a[212:215], a[20:23], v[28:31]\n"
    "v_exp_f32 v20, v20\n"
    "v_mfma_f32_16x16x32_bf16 v[28:31], a[208:211], a[16:19], v[28:31]\n"
    "v_exp_f32 v21, v21\n"
    "v_mfma_f32_16x16x32_bf16 v[132:135], v[56:59], a[224:227], v[132:135]\n"
    "v_cvt_pk_bf16_f32 v90, v20, v21\n"
    "v_mul_f32 v177, v177, v113\n"
    "v_mfma_f32_16x16x32_bf16 v[132:135], v[60:63], a[228:231], v[132:135]\n"
    "v_exp_f32 v22, v22\n"
    "v_mfma_f32_16x16x32_bf16 v[148:151], v[60:63], a[236:239], v[148:151]\n"
    "v_exp_f32 v23, v23\n"
    "v_mfma_f32_16x16x32_bf16 v[148:151], v[56:59], a[232:235], v[148:151]\n"
    "v_cvt_pk_bf16_f32 v91, v22, v23\n"
    "v_mul_f32 v193, v193, v113\n"
    "v_mfma_f32_16x16x32_bf16 v[164:167], v[56:59], a[240:243], v[164:167]\n"
    "v_exp_f32 v24, v24\n"
    "v_mfma_f32_16x16x32_bf16 v[164:167], v[60:63], a[244:247], v[164:167]\n"
    "v_exp_f32 v25, v25\n"
    "v_mfma_f32_16x16x32_bf16 v[180:183], v[60:63], a[252:255], v[180:183]\n"
    "v_cvt_pk_bf16_f32 v92, v24, v25\n"
    "v_mul_f32 v209, v209, v113\n"
    "v_mfma_f32_16x16x32_bf16 v[180:183], v[56:59], a[248:251], v[180:183]\n"
    "v_exp_f32 v26, v26\n"
    "v_mfma_f32_16x16x32_bf16 v[196:199], v[56:59], a[148:151], v[196:199]\n"
    "v_exp_f32 v27, v27\n"
    "v_mfma_f32_16x16x32_bf16 v[196:199], v[60:63], a[152:155], v[196:199]\n"
    "v_cvt_pk_bf16_f32 v93, v26, v27\n"
    "v_mul_f32 v225, v225, v113\n"
    "v_mfma_f32_16x16x32_bf16 v[212:215], v[60:63], v[32:35], v[212:215]\n"
    "v_exp_f32 v28, v28\n"
    "v_mfma_f32_16x16x32_bf16 v[212:215], v[56:59], a[156:159], v[212:215]\n"
    "v_exp_f32 v29, v29\n"
    "v_mfma_f32_16x16x32_bf16 v[228:231], v[56:59], v[36:39], v[228:231]\n"
    "v_cvt_pk_bf16_f32 v94, v28, v29\n"
    "v_mul_f32 v241, v241, v113\n"
    "v_mfma_f32_16x16x32_bf16 v[228:231], v[60:63], v[40:43], v[228:231]\n"
    "v_exp_f32 v30, v30\n"
    "v_mfma_f32_16x16x32_bf16 v[244:247], v[60:63], v[124:127], v[244:247]\n"
    "v_exp_f32 v31, v31\n"
    "v_mfma_f32_16x16x32_bf16 v[244:247], v[56:59], v[44:47], v[244:247]\n"
    "v_cvt_pk_bf16_f32 v95, v30, v31\n"
    "v_cvt_pk_bf16_f32 v52, v129, v145\n"
    "v_smfmac_f32_16x16x64_bf16 a[132:135], a[144:147], v[56:63], v10\n"
    "v_cvt_pk_bf16_f32 v53, v161, v177\n"
    "v_cvt_pk_bf16_f32 v54, v193, v209\n"
    "v_smfmac_f32_16x16x64_bf16 a[132:135], a[144:147], v[88:95], v10\n"
    "v_cvt_pk_bf16_f32 v55, v225, v241\n"
    "v_mul_f32 v130, v130, v114\n"
    "v_mfma_f32_16x16x32_bf16 v[132:135], v[88:91], a[64:67], v[132:135]\n"
    "global_store_dwordx4 v6, v[52:55], s[42:43] offset:1024 sc1\n"
    "v_mul_f32 v146, v146, v114\n"
    "v_mfma_f32_16x16x32_bf16 v[132:135], v[92:95], a[68:71], v[132:135]\n"
    "v_mul_f32 v162, v162, v114\n"
    "v_mul_f32 v178, v178, v114\n"
    "v_mfma_f32_16x16x32_bf16 v[148:151], v[92:95], a[76:79], v[148:151]\n"
    "v_mul_f32 v194, v194, v114\n"
    "v_mul_f32 v210, v210, v114\n"
    "v_mfma_f32_16x16x32_bf16 v[148:151], v[88:91], a[72:75], v[148:151]\n"
    "v_mul_f32 v226, v226, v114\n"
    "v_mul_f32 v242, v242, v114\n"
    "v_mfma_f32_16x16x32_bf16 v[164:167], v[88:91], a[96:99], v[164:167]\n"
    "v_cvt_pk_bf16_f32 v80, v130, v146\n"
    "v_cvt_pk_bf16_f32 v81, v162, v178\n"
    "v_mfma_f32_16x16x4_f32 v[116:119], a132, v12, 0\n"
    "v_cvt_pk_bf16_f32 v82, v194, v210\n"
    "v_cvt_pk_bf16_f32 v83, v226, v242\n"
    "v_mfma_f32_16x16x32_bf16 v[164:167], v[92:95], a[100:103], v[164:167]\n"
    "global_store_dwordx4 v6, v[80:83], s[42:43] offset:2048 sc1\n"
    "v_mul_f32 v131, v131, v115\n"
    "v_mfma_f32_16x16x32_bf16 v[180:183], v[92:95], a[108:111], v[180:183]\n"
    "v_mul_f32 v147, v147, v115\n"
    "v_mul_f32 v163, v163, v115\n"
    "v_mfma_f32_16x16x32_bf16 v[180:183], v[88:91], a[104:107], v[180:183]\n"
    "v_mul_f32 v179, v179, v115\n"
    "v_mul_f32 v195, v195, v115\n"
    "v_mfma_f32_16x16x32_bf16 v[196:199], v[88:91], a[80:83], v[196:199]\n"
    "v_mul_f32 v211, v211, v115\n"
    "v_mul_f32 v227, v227, v115\n"
    "v_mfma_f32_16x16x32_bf16 v[196:199], v[92:95], a[84:87], v[196:199]\n"
    "v_mul_f32 v243, v243, v115\n"
    "v_cvt_pk_bf16_f32 v84, v131, v147\n"
    "v_mfma_f32_16x16x32_bf16 v[212:215], v[92:95], a[92:95], v[212:215]\n"
    "v_max3_u32 v8, v8, v116, v117\n"
    "v_min3_u32 v9, v9, v116, v117\n"
    "v_mfma_f32_16x16x32_bf16 v[212:215], v[88:91], a[88:91], v[212:215]\n"
    "v_max3_u32 v8, v8, v118, v119\n"
    "v_min3_u32 v9, v9, v118, v119\n"
    "v_mfma_f32_16x16x32_bf16 v[228:231], v[88:91], a[112:115], v[228:231]\n"
    "v_rcp_f32 v116, v116\n"
    "v_mfma_f32_16x16x32_bf16 v[228:231], v[92:95], a[116:119], v[228:231]\n"
    "v_rcp_f32 v117, v117\n"
    "v_mfma_f32_16x16x32_bf16 v[244:247], v[92:95], a[124:127], v[244:247]\n"
    "v_rcp_f32 v118, v118\n"
    "v_mfma_f32_16x16x32_bf16 v[244:247], v[88:91], a[120:123], v[244:247]\n"
    "v_rcp_f32 v119, v119\n"
    "v_cvt_pk_bf16_f32 v85, v163, v179\n"
    "v_cvt_pk_bf16_f32 v86, v195, v211\n"
    "v_cvt_pk_bf16_f32 v87, v227, v243\n"
    "global_store_dwordx4 v6, v[84:87], s[42:43] offset:3072 sc1\n"
    "v_mfma_f32_16x16x32_bf16 v[16:19], a[160:163], a[32:35], 4.0\n"
    "v_mfma_f32_16x16x32_bf16 v[16:19], a[164:167], a[36:39], v[16:19]\n"
    "v_mul_f32 v132, v132, v116\n"
    "v_mul_f32 v148, v148, v116\n"
    "v_mfma_f32_16x16x32_bf16 v[16:19], a[168:171], a[40:43], v[16:19]\n"
    "v_mul_f32 v164, v164, v116\n"
    "v_mul_f32 v180, v180, v116\n"
    "v_mfma_f32_16x16x32_bf16 v[16:19], a[172:175], a[44:47], v[16:19]\n"
    "v_mul_f32 v196, v196, v116\n"
    "v_mul_f32 v212, v212, v116\n"
    "v_mfma_f32_16x16x32_bf16 v[20:23], a[188:191], a[44:47], 4.0\n"
    "v_mul_f32 v228, v228, v116\n"
    "v_mul_f32 v244, v244, v116\n"
    "v_mfma_f32_16x16x32_bf16 v[20:23], a[184:187], a[40:43], v[20:23]\n"
    "v_cvt_pk_bf16_f32 v56, v132, v148\n"
    "v_cvt_pk_bf16_f32 v57, v164, v180\n"
    "v_mfma_f32_16x16x32_bf16 v[20:23], a[180:183], a[36:39], v[20:23]\n"
    "v_cvt_pk_bf16_f32 v58, v196, v212\n"
    "v_cvt_pk_bf16_f32 v59, v228, v244\n"
    "v_mfma_f32_16x16x32_bf16 v[20:23], a[176:179], a[32:35], v[20:23]\n"
    "global_store_dwordx4 v6, v[56:59], s[44:45] offset:0 sc1\n"
    "v_mul_f32 v133, v133, v117\n"
    "v_mfma_f32_16x16x32_bf16 v[24:27], a[192:195], a[32:35], 4.0\n"
    "v_exp_f32 v16, v16\n"
    "v_mfma_f32_16x16x32_bf16 v[24:27], a[196:199], a[36:39], v[24:27]\n"
    "v_exp_f32 v17, v17\n"
    "v_mfma_f32_16x16x32_bf16 v[24:27], a[200:203], a[40:43], v[24:27]\n"
    "v_cvt_pk_bf16_f32 v96, v16, v17\n"
    "v_mul_f32 v149, v149, v117\n"
    "v_mfma_f32_16x16x32_bf16 v[24:27], a[204:207], a[44:47], v[24:27]\n"
    "v_exp_f32 v18, v18\n"
    "v_mfma_f32_16x16x32_bf16 v[28:31], a[220:223], a[44:47], 4.0\n"
    "v_exp_f32 v19, v19\n"
    "v_mfma_f32_16x16x32_bf16 v[28:31], a[216:219], a[40:43], v[28:31]\n"
    "v_cvt_pk_bf16_f32 v97, v18, v19\n"
    "v_mul_f32 v165, v165, v117\n"
    "v_mfma_f32_16x16x32_bf16 v[28:31], a[212:215], a[36:39], v[28:31]\n"
    "v_exp_f32 v20, v20\n"
    "v_mfma_f32_16x16x32_bf16 v[28:31], a[208:211], a[32:35], v[28:31]\n"
    "v_exp_f32 v21, v21\n"
    "v_mfma_f32_16x16x32_bf16 v[136:139], v[64:67], a[224:227], v[136:139]\n"
    "v_cvt_pk_bf16_f32 v98, v20, v21\n"
    "v_mul_f32 v181, v181, v117\n"
    "v_mfma_f32_16x16x32_bf16 v[136:139], v[68:71], a[228:231], v[136:139]\n"
    "v_exp_f32 v22, v22\n"
    "v_mfma_f32_16x16x32_bf16 v[152:155], v[68:71], a[236:239], v[152:155]\n"
    "v_exp_f32 v23, v23\n"
    "v_mfma_f32_16x16x32_bf16 v[152:155], v[64:67], a[232:235], v[152:155]\n"
    "v_cvt_pk_bf16_f32 v99, v22, v23\n"
    "v_mul_f32 v197, v197, v117\n"
    "v_mfma_f32_16x16x32_bf16 v[168:171], v[64:67], a[240:243], v[168:171]\n"
    "v_exp_f32 v24, v24\n"
    "v_mfma_f32_16x16x32_bf16 v[168:171], v[68:71], a[244:247], v[168:171]\n"
    "v_exp_f32 v25, v25\n"
    "v_mfma_f32_16x16x32_bf16 v[184:187], v[68:71], a[252:255], v[184:187]\n"
    "v_cvt_pk_bf16_f32 v100, v24, v25\n"
    "v_mul_f32 v213, v213, v117\n"
    "v_mfma_f32_16x16x32_bf16 v[184:187], v[64:67], a[248:251], v[184:187]\n"
    "v_exp_f32 v26, v26\n"
    "v_mfma_f32_16x16x32_bf16 v[200:203], v[64:67], a[148:151], v[200:203]\n"
    "v_exp_f32 v27, v27\n"
    "v_mfma_f32_16x16x32_bf16 v[200:203], v[68:71], a[152:155], v[200:203]\n"
    "v_cvt_pk_bf16_f32 v101, v26, v27\n"
    "v_mul_f32 v229, v229, v117\n"
    "v_mfma_f32_16x16x32_bf16 v[216:219], v[68:71], v[32:35], v[216:219]\n"
    "v_exp_f32 v28, v28\n"
    "v_mfma_f32_16x16x32_bf16 v[216:219], v[64:67], a[156:159], v[216:219]\n"
    "v_exp_f32 v29, v29\n"
    "v_mfma_f32_16x16x32_bf16 v[232:235], v[64:67], v[36:39], v[232:235]\n"
    "v_cvt_pk_bf16_f32 v102, v28, v29\n"
    "v_mul_f32 v245, v245, v117\n"
    "v_mfma_f32_16x16x32_bf16 v[232:235], v[68:71], v[40:43], v[232:235]\n"
    "v_exp_f32 v30, v30\n"
    "v_mfma_f32_16x16x32_bf16 v[248:251], v[68:71], v[124:127], v[248:251]\n"
    "v_exp_f32 v31, v31\n"
    "v_mfma_f32_16x16x32_bf16 v[248:251], v[64:67], v[44:47], v[248:251]\n"
    "v_cvt_pk_bf16_f32 v103, v30, v31\n"
    "v_cvt_pk_bf16_f32 v60, v133, v149\n"
    "v_smfmac_f32_16x16x64_bf16 a[136:139], a[144:147], v[64:71], v10\n"
    "v_cvt_pk_bf16_f32 v61, v165, v181\n"
    "v_cvt_pk_bf16_f32 v62, v197, v213\n"
    "v_smfmac_f32_16x16x64_bf16 a[136:139], a[144:147], v[96:103], v10\n"
    "v_cvt_pk_bf16_f32 v63, v229, v245\n"
    "v_mul_f32 v134, v134, v118\n"
    "v_mfma_f32_16x16x32_bf16 v[136:139], v[96:99], a[64:67], v[136:139]\n"
    "global_store_dwordx4 v6, v[60:63], s[44:45] offset:1024 sc1\n"
    "v_mul_f32 v150, v150, v118\n"
    "v_mfma_f32_16x16x32_bf16 v[136:139], v[100:103], a[68:71], v[136:139]\n"
    "v_mul_f32 v166, v166, v118\n"
    "v_mul_f32 v182, v182, v118\n"
    "v_mfma_f32_16x16x32_bf16 v[152:155], v[100:103], a[76:79], v[152:155]\n"
    "v_mul_f32 v198, v198, v118\n"
    "v_mul_f32 v214, v214, v118\n"
    "v_mfma_f32_16x16x32_bf16 v[152:155], v[96:99], a[72:75], v[152:155]\n"
    "v_mul_f32 v230, v230, v118\n"
    "v_mul_f32 v246, v246, v118\n"
    "v_mfma_f32_16x16x32_bf16 v[168:171], v[96:99], a[96:99], v[168:171]\n"
    "v_cvt_pk_bf16_f32 v88, v134, v150\n"
    "v_cvt_pk_bf16_f32 v89, v166, v182\n"
    "v_mfma_f32_16x16x4_f32 v[112:115], a136, v12, 0\n"
    "v_cvt_pk_bf16_f32 v90, v198, v214\n"
    "v_cvt_pk_bf16_f32 v91, v230, v246\n"
    "v_mfma_f32_16x16x32_bf16 v[168:171], v[100:103], a[100:103], v[168:171]\n"
    "global_store_dwordx4 v6, v[88:91], s[44:45] offset:2048 sc1\n"
    "v_mul_f32 v135, v135, v119\n"
    "v_mfma_f32_16x16x32_bf16 v[184:187], v[100:103], a[108:111], v[184:187]\n"
    "v_mul_f32 v151, v151, v119\n"
    "v_mul_f32 v167, v167, v119\n"
    "v_mfma_f32_16x16x32_bf16 v[184:187], v[96:99], a[104:107], v[184:187]\n"
    "v_mul_f32 v183, v183, v119\n"
    "v_mul_f32 v199, v199, v119\n"
    "v_mfma_f32_16x16x32_bf16 v[200:203], v[96:99], a[80:83], v[200:203]\n"
    "v_mul_f32 v215, v215, v119\n"
    "v_mul_f32 v231, v231, v119\n"
    "v_mfma_f32_16x16x32_bf16 v[200:203], v[100:103], a[84:87], v[200:203]\n"
    "v_mul_f32 v247, v247, v119\n"
    "v_cvt_pk_bf16_f32 v92, v135, v151\n"
    "v_mfma_f32_16x16x32_bf16 v[216:219], v[100:103], a[92:95], v[216:219]\n"
    "v_max3_u32 v8, v8, v112, v113\n"
    "v_min3_u32 v9, v9, v112, v113\n"
    "v_mfma_f32_16x16x32_bf16 v[216:219], v[96:99], a[88:91], v[216:219]\n"
    "v_max3_u32 v8, v8, v114, v115\n"
    "v_min3_u32 v9, v9, v114, v115\n"
    "v_mfma_f32_16x16x32_bf16 v[232:235], v[96:99], a[112:115], v[232:235]\n"
    "v_rcp_f32 v112, v112\n"
    "v_mfma_f32_16x16x32_bf16 v[232:235], v[100:103], a[116:119], v[232:235]\n"
    "v_rcp_f32 v113, v113\n"
    "v_mfma_f32_16x16x32_bf16 v[248:251], v[100:103], a[124:127], v[248:251]\n"
    "v_rcp_f32 v114, v114\n"
    "v_mfma_f32_16x16x32_bf16 v[248:251], v[96:99], a[120:123], v[248:251]\n"
    "v_rcp_f32 v115, v115\n"
    "v_cvt_pk_bf16_f32 v93, v167, v183\n"
    "v_cvt_pk_bf16_f32 v94, v199, v215\n"
    "v_cvt_pk_bf16_f32 v95, v231, v247\n"
    "global_store_dwordx4 v6, v[92:95], s[44:45] offset:3072 sc1\n"
    "v_mfma_f32_16x16x32_bf16 v[16:19], a[160:163], a[48:51], 4.0\n"
    "v_mfma_f32_16x16x32_bf16 v[16:19], a[164:167], a[52:55], v[16:19]\n"
    "v_mul_f32 v136, v136, v112\n"
    "v_mul_f32 v152, v152, v112\n"
    "v_mfma_f32_16x16x32_bf16 v[16:19], a[168:171], a[56:59], v[16:19]\n"
    "v_mul_f32 v168, v168, v112\n"
    "v_mul_f32 v184, v184, v112\n"
    "v_mfma_f32_16x16x32_bf16 v[16:19], a[172:175], a[60:63], v[16:19]\n"
    "v_mul_f32 v200, v200, v112\n"
    "v_mul_f32 v216, v216, v112\n"
    "v_mfma_f32_16x16x32_bf16 v[20:23], a[188:191], a[60:63], 4.0\n"
    "v_mul_f32 v232, v232, v112\n"
    "v_mul_f32 v248, v248, v112\n"
    "v_mfma_f32_16x16x32_bf16 v[20:23], a[184:187], a[56:59], v[20:23]\n"
    "v_cvt_pk_bf16_f32 v64, v136, v152\n"
    "v_cvt_pk_bf16_f32 v65, v168, v184\n"
    "v_mfma_f32_16x16x32_bf16 v[20:23], a[180:183], a[52:55], v[20:23]\n"
    "v_cvt_pk_bf16_f32 v66, v200, v216\n"
    "v_cvt_pk_bf16_f32 v67, v232, v248\n"
    "v_mfma_f32_16x16x32_bf16 v[20:23], a[176:179], a[48:51], v[20:23]\n"
    "global_store_dwordx4 v6, v[64:67], s[46:47] offset:0 sc1\n"
    "v_mul_f32 v137, v137, v113\n"
    "v_mfma_f32_16x16x32_bf16 v[24:27], a[192:195], a[48:51], 4.0\n"
    "v_exp_f32 v16, v16\n"
    "v_mfma_f32_16x16x32_bf16 v[24:27], a[196:199], a[52:55], v[24:27]\n"
    "v_exp_f32 v17, v17\n"
    "v_mfma_f32_16x16x32_bf16 v[24:27], a[200:203], a[56:59], v[24:27]\n"
    "v_cvt_pk_bf16_f32 v104, v16, v17\n"
    "v_mul_f32 v153, v153, v113\n"
    "v_mfma_f32_16x16x32_bf16 v[24:27], a[204:207], a[60:63], v[24:27]\n"
    "v_exp_f32 v18, v18\n"
    "v_mfma_f32_16x16x32_bf16 v[28:31], a[220:223], a[60:63], 4.0\n"
    "v_exp_f32 v19, v19\n"
    "v_mfma_f32_16x16x32_bf16 v[28:31], a[216:219], a[56:59], v[28:31]\n"
    "v_cvt_pk_bf16_f32 v105, v18, v19\n"
    "v_mul_f32 v169, v169, v113\n"
    "v_mfma_f32_16x16x32_bf16 v[28:31], a[212:215], a[52:55], v[28:31]\n"
    "v_exp_f32 v20, v20\n"
    "v_mfma_f32_16x16x32_bf16 v[28:31], a[208:211], a[48:51], v[28:31]\n"
    "v_exp_f32 v21, v21\n"
    "v_mfma_f32_16x16x32_bf16 v[140:143], v[72:75], a[224:227], v[140:143]\n"
    "v_cvt_pk_bf16_f32 v106, v20, v21\n"
    "v_mul_f32 v185, v185, v113\n"
    "v_mfma_f32_16x16x32_bf16 v[140:143], v[76:79], a[228:231], v[140:143]\n"
    "v_exp_f32 v22, v22\n"
    "v_mfma_f32_16x16x32_bf16 v[156:159], v[76:79], a[236:239], v[156:159]\n"
    "v_exp_f32 v23, v23\n"
    "v_mfma_f32_16x16x32_bf16 v[156:159], v[72:75], a[232:235], v[156:159]\n"
    "v_cvt_pk_bf16_f32 v107, v22, v23\n"
    "v_mul_f32 v201, v201, v113\n"
    "v_mfma_f32_16x16x32_bf16 v[172:175], v[72:75], a[240:243], v[172:175]\n"
    "v_exp_f32 v24, v24\n"
    "v_mfma_f32_16x16x32_bf16 v[172:175], v[76:79], a[244:247], v[172:175]\n"
    "v_exp_f32 v25, v25\n"
    "v_mfma_f32_16x16x32_bf16 v[188:191], v[76:79], a[252:255], v[188:191]\n"
    "v_cvt_pk_bf16_f32 v108, v24, v25\n"
    "v_mul_f32 v217, v217, v113\n"
    "v_mfma_f32_16x16x32_bf16 v[188:191], v[72:75], a[248:251], v[188:191]\n"
    "v_exp_f32 v26, v26\n"
    "v_mfma_f32_16x16x32_bf16 v[204:207], v[72:75], a[148:151], v[204:207]\n"
    "v_exp_f32 v27, v27\n"
    "v_mfma_f32_16x16x32_bf16 v[204:207], v[76:79], a[152:155], v[204:207]\n"
    "v_cvt_pk_bf16_f32 v109, v26, v27\n"
    "v_mul_f32 v233, v233, v113\n"
    "v_mfma_f32_16x16x32_bf16 v[220:223], v[76:79], v[32:35], v[220:223]\n"
    "v_exp_f32 v28, v28\n"
    "v_mfma_f32_16x16x32_bf16 v[220:223], v[72:75], a[156:159], v[220:223]\n"
    "v_exp_f32 v29, v29\n"
    "v_mfma_f32_16x16x32_bf16 v[236:239], v[72:75], v[36:39], v[236:239]\n"
    "v_cvt_pk_bf16_f32 v110, v28, v29\n"
    "v_mul_f32 v249, v249, v113\n"
    "v_mfma_f32_16x16x32_bf16 v[236:239], v[76:79], v[40:43], v[236:239]\n"
    "v_exp_f32 v30, v30\n"
    "v_mfma_f32_16x16x32_bf16 v[252:255], v[76:79], v[124:127], v[252:255]\n"
    "v_exp_f32 v31, v31\n"
    "v_mfma_f32_16x16x32_bf16 v[252:255], v[72:75], v[44:47], v[252:255]\n"
    "v_cvt_pk_bf16_f32 v111, v30, v31\n"
    "v_cvt_pk_bf16_f32 v68, v137, v153\n"
    "v_smfmac_f32_16x16x64_bf16 a[140:143], a[144:147], v[72:79], v10\n"
    "v_cvt_pk_bf16_f32 v69, v169, v185\n"
    "v_cvt_pk_bf16_f32 v70, v201, v217\n"
    "v_smfmac_f32_16x16x64_bf16 a[140:143], a[144:147], v[104:111], v10\n"
    "v_cvt_pk_bf16_f32 v71, v233, v249\n"
    "v_mul_f32 v138, v138, v114\n"
    "v_mfma_f32_16x16x32_bf16 v[140:143], v[104:107], a[64:67], v[140:143]\n"
    "global_store_dwordx4 v6, v[68:71], s[46:47] offset:1024 sc1\n"
    "v_mul_f32 v154, v154, v114\n"
    "v_mfma_f32_16x16x32_bf16 v[140:143], v[108:111], a[68:71], v[140:143]\n"
    "v_mul_f32 v170, v170, v114\n"
    "v_mul_f32 v186, v186, v114\n"
    "v_mfma_f32_16x16x32_bf16 v[156:159], v[108:111], a[76:79], v[156:159]\n"
    "v_mul_f32 v202, v202, v114\n"
    "v_mul_f32 v218, v218, v114\n"
    "v_mfma_f32_16x16x32_bf16 v[156:159], v[104:107], a[72:75], v[156:159]\n"
    "v_mul_f32 v234, v234, v114\n"
    "v_mul_f32 v250, v250, v114\n"
    "v_mfma_f32_16x16x32_bf16 v[172:175], v[104:107], a[96:99], v[172:175]\n"
    "v_cvt_pk_bf16_f32 v96, v138, v154\n"
    "v_cvt_pk_bf16_f32 v97, v170, v186\n"
    "v_mfma_f32_16x16x4_f32 v[116:119], a140, v12, 0\n"
    "v_cvt_pk_bf16_f32 v98, v202, v218\n"
    "v_cvt_pk_bf16_f32 v99, v234, v250\n"
    "v_mfma_f32_16x16x32_bf16 v[172:175], v[108:111], a[100:103], v[172:175]\n"
    "global_store_dwordx4 v6, v[96:99], s[46:47] offset:2048 sc1\n"
    "v_mul_f32 v139, v139, v115\n"
    "v_mfma_f32_16x16x32_bf16 v[188:191], v[108:111], a[108:111], v[188:191]\n"
    "v_mul_f32 v155, v155, v115\n"
    "v_mul_f32 v171, v171, v115\n"
    "v_mfma_f32_16x16x32_bf16 v[188:191], v[104:107], a[104:107], v[188:191]\n"
    "v_mul_f32 v187, v187, v115\n"
    "v_mul_f32 v203, v203, v115\n"
    "v_mfma_f32_16x16x32_bf16 v[204:207], v[104:107], a[80:83], v[204:207]\n"
    "v_mul_f32 v219, v219, v115\n"
    "v_mul_f32 v235, v235, v115\n"
    "v_mfma_f32_16x16x32_bf16 v[204:207], v[108:111], a[84:87], v[204:207]\n"
    "v_mul_f32 v251, v251, v115\n"
    "v_cvt_pk_bf16_f32 v100, v139, v155\n"
    "v_mfma_f32_16x16x32_bf16 v[220:223], v[108:111], a[92:95], v[220:223]\n"
    "v_max3_u32 v8, v8, v116, v117\n"
    "v_min3_u32 v9, v9, v116, v117\n"
    "v_mfma_f32_16x16x32_bf16 v[220:223], v[104:107], a[88:91], v[220:223]\n"
    "v_max3_u32 v8, v8, v118, v119\n"
    "v_min3_u32 v9, v9, v118, v119\n"
    "v_mfma_f32_16x16x32_bf16 v[236:239], v[104:107], a[112:115], v[236:239]\n"
    "v_rcp_f32 v116, v116\n"
    "v_mfma_f32_16x16x32_bf16 v[236:239], v[108:111], a[116:119], v[236:239]\n"
    "v_rcp_f32 v117, v117\n"
    "v_mfma_f32_16x16x32_bf16 v[252:255], v[108:111], a[124:127], v[252:255]\n"
    "v_rcp_f32 v118, v118\n"
    "v_mfma_f32_16x16x32_bf16 v[252:255], v[104:107], a[120:123], v[252:255]\n"
    "v_rcp_f32 v119, v119\n"
    "v_cvt_pk_bf16_f32 v101, v171, v187\n"
    "v_cvt_pk_bf16_f32 v102, v203, v219\n"
    "v_cvt_pk_bf16_f32 v103, v235, v251\n"
    "global_store_dwordx4 v6, v[100:103], s[46:47] offset:3072 sc1\n"
    "v_pk_mul_f32 v[140:141], v[140:141], v[116:117]\n"
    "v_pk_mul_f32 v[156:157], v[156:157], v[116:117]\n"
    "v_pk_mul_f32 v[172:173], v[172:173], v[116:117]\n"
    "v_pk_mul_f32 v[188:189], v[188:189], v[116:117]\n"
    "v_pk_mul_f32 v[204:205], v[204:205], v[116:117]\n"
    "v_pk_mul_f32 v[220:221], v[220:221], v[116:117]\n"
    "v_pk_mul_f32 v[236:237], v[236:237], v[116:117]\n"
    "v_pk_mul_f32 v[252:253], v[252:253], v[116:117]\n"
    "v_cvt_pk_bf16_f32 v72, v140, v156\n"
    "v_cvt_pk_bf16_f32 v73, v172, v188\n"
    "v_cvt_pk_bf16_f32 v74, v204, v220\n"
    "v_cvt_pk_bf16_f32 v75, v236, v252\n"
    "global_store_dwordx4 v6, v[72:75], s[48:49] offset:0 sc1\n"
    "v_cvt_pk_bf16_f32 v76, v141, v157\n"
    "v_cvt_pk_bf16_f32 v77, v173, v189\n"
    "v_cvt_pk_bf16_f32 v78, v205, v221\n"
    "v_cvt_pk_bf16_f32 v79, v237, v253\n"
    "global_store_dwordx4 v6, v[76:79], s[48:49] offset:1024 sc1\n"
    "v_pk_mul_f32 v[142:143], v[142:143], v[118:119]\n"
    "v_pk_mul_f32 v[158:159], v[158:159], v[118:119]\n"
    "v_pk_mul_f32 v[174:175], v[174:175], v[118:119]\n"
    "v_pk_mul_f32 v[190:191], v[190:191], v[118:119]\n"
    "v_pk_mul_f32 v[206:207], v[206:207], v[118:119]\n"
    "v_pk_mul_f32 v[222:223], v[222:223], v[118:119]\n"
    "v_pk_mul_f32 v[238:239], v[238:239], v[118:119]\n"
    "v_pk_mul_f32 v[254:255], v[254:255], v[118:119]\n"
    "v_cvt_pk_bf16_f32 v104, v142, v158\n"
    "v_cvt_pk_bf16_f32 v105, v174, v190\n"
    "v_cvt_pk_bf16_f32 v106, v206, v222\n"
    "v_cvt_pk_bf16_f32 v107, v238, v254\n"
    "global_store_dwordx4 v6, v[104:107], s[48:49] offset:2048 sc1\n"
    "v_cvt_pk_bf16_f32 v108, v143, v159\n"
    "v_cvt_pk_bf16_f32 v109, v175, v191\n"
    "v_cvt_pk_bf16_f32 v110, v207, v223\n"
    "v_cvt_pk_bf16_f32 v111, v239, v255\n"
    "global_store_dwordx4 v6, v[108:111], s[48:49] offset:3072 sc1\n"
    "s_mov_b32 s61, 0x2b800000\n"
    "s_mov_b32 s75, 0x63800000\n"
    "v_cmp_lt_u32 vcc, s75, v8\n"
    "v_cmp_gt_u32 s[78:79], s61, v9\n"
    "s_waitcnt lgkmcnt(0)\n"
    "v_or3_b32 v4, v120, v121, v122\n"
    "v_or_b32 v4, v4, v123\n"
    "s_or_b64 s[78:79], s[78:79], vcc\n"
    "v_readfirstlane_b32 s61, v4\n"
    "s_or_b32 s78, s78, s79\n"
    "s_or_b32 %0, s78, s61\n"
    "s_and_b32 s61, s71, 4\n"
    "s_or_b32 s61, s61, %3\n"
    "s_cbranch_scc1 L%=_noe\n"
    "v_mov_b32 v4, 0x8000\n"
    "v_mov_b32 v5, s72\n"
    "s_mov_b64 exec, 1\n"
    "global_store_dword v4, v5, s[76:77] sc1\n"
    "s_mov_b64 exec, -1\n"
    "L%=_noe:\n"
    "s_cmp_lg_u32 %0, 0\n"
    "s_cbranch_scc1 L%=_fin\n"
    "s_endpgm\n"
    "L%=_fin:\n"
    : "=&s"(status) : "s"(ka), "s"(bid), "s"(wid)
    : "v0","v1","v2","v3","v4","v5","v6","v7","v8","v9","v10","v11","v12","v13","v14","v15","v16","v17","v18","v19","v20","v21","v22","v23","v24","v25","v26","v27","v28","v29","v30","v31","v32","v33","v34","v35","v36","v37","v38","v39","v40","v41","v42","v43","v44","v45","v46","v47","v48","v49","v50","v51","v52","v53","v54","v55","v56","v57","v58","v59","v60","v61","v62","v63","v64","v65","v66","v67","v68","v69","v70","v71","v72","v73","v74","v75","v76","v77","v78","v79","v80","v81","v82","v83","v84","v85","v86","v87","v88","v89","v90","v91","v92","v93","v94","v95","v96","v97","v98","v99","v100","v101","v102","v103","v104","v105","v106","v107","v108","v109","v110","v111","v112","v113","v114","v115","v116","v117","v118","v119","v120","v121","v122","v123","v124","v125","v126","v127","v128","v129","v130","v131","v132","v133","v134","v135","v136","v137","v138","v139","v140","v141","v142","v143","v144","v145","v146","v147","v148","v149","v150","v151","v152","v153","v154","v155","v156","v157","v158","v159","v160","v161","v162","v163","v164","v165","v166","v167","v168","v169","v170","v171","v172","v173","v174","v175","v176","v177","v178","v179","v180","v181","v182","v183","v184","v185","v186","v187","v188","v189","v190","v191","v192","v193","v194","v195","v196","v197","v198","v199","v200","v201","v202","v203","v204","v205","v206","v207","v208","v209","v210","v211","v212","v213","v214","v215","v216","v217","v218","v219","v220","v221","v222","v223","v224","v225","v226","v227","v228","v229","v230","v231","v232","v233","v234","v235","v236","v237","v238","v239","v240","v241","v242","v243","v244","v245","v246","v247","v248","v249","v250","v251","v252","v253","v254","v255","a0","a1","a2","a3","a4","a5","a6","a7","a8","a9","a10","a11","a12","a13","a14","a15","a16","a17","a18","a19","a20","a21","a22","a23","a24","a25","a26","a27","a28","a29","a30","a31","a32","a33","a34","a35","a36","a37","a38","a39","a40","a41","a42","a43","a44","a45","a46","a47","a48","a49","a50","a51","a52","a53","a54","a55","a56","a57","a58","a59","a60","a61","a62","a63","a64","a65","a66","a67","a68","a69","a70","a71","a72","a73","a74","a75","a76","a77","a78","a79","a80","a81","a82","a83","a84","a85","a86","a87","a88","a89","a90","a91","a92","a93","a94","a95","a96","a97","a98","a99","a100","a101","a102","a103","a104","a105","a106","a107","a108","a109","a110","a111","a112","a113","a114","a115","a116","a117","a118","a119","a120","a121","a122","a123","a124","a125","a126","a127","a128","a129","a130","a131","a132","a133","a134","a135","a136","a137","a138","a139","a140","a141","a142","a143","a144","a145","a146","a147","a148","a149","a150","a151","a152","a153","a154","a155","a156","a157","a158","a159","a160","a161","a162","a163","a164","a165","a166","a167","a168","a169","a170","a171","a172","a173","a174","a175","a176","a177","a178","a179","a180","a181","a182","a183","a184","a185","a186","a187","a188","a189","a190","a191","a192","a193","a194","a195","a196","a197","a198","a199","a200","a201","a202","a203","a204","a205","a206","a207","a208","a209","a210","a211","a212","a213","a214","a215","a216","a217","a218","a219","a220","a221","a222","a223","a224","a225","a226","a227","a228","a229","a230","a231","a232","a233","a234","a235","a236","a237","a238","a239","a240","a241","a242","a243","a244","a245","a246","a247","a248","a249","a250","a251","a252","a253","a254","a255","s34","s35","s36","s37","s38","s39","s40","s41","s42","s43","s44","s45","s46","s47","s48","s49","s50","s51","s52","s53","s54","s55","s56","s57","s58","s59","s60","s61","s62","s63","s64","s65","s66","s67","s68","s69","s70","s71","s72","s73","s74","s75","s76","s77","s78","s79","s80","s81","s82","s83","s84","s85","vcc","scc","memory");
  if (status != 0) {
    asm volatile("s_waitcnt vmcnt(0)" : "+s"(ka) :: "memory");
    exact_rows(ka->q, ka->qk, ka->qv, ka->ks, ka->kz, ka->vs, ka->vz, ka->out, bid & 7, (bid & 7) * 8192 + (bid >> 3) * 256 + wid * 64);
  }
}

extern "C" void kernel_launch(void* const* d_in, const int* in_sizes, int n_in, void* d_out, int out_size, void* d_ws, size_t ws_size, hipStream_t stream) {
  (void)hipFuncSetAttribute((const void*)kv_quant_attn_kernel, hipFuncAttributeMaxDynamicSharedMemorySize, 163840);
  hipLaunchKernelGGL(kv_quant_attn_kernel, dim3(256), dim3(256), 163840, stream, (const u16*)d_in[0], (const int*)d_in[1], (const int*)d_in[2], (const u16*)d_in[3], (const u16*)d_in[4],
                     (const u16*)d_in[5], (const u16*)d_in[6], (u16*)d_out, d_ws);
}
```
